```python
import torch

def _softmax(x, axis=-1):
    return torch.softmax(x, dim=axis)

def _gelu(x):
    return torch.nn.functional.gelu(x, approximate='tanh')

def _silu(x):
    return torch.nn.functional.silu(x)

def _sigmoid(x):
    return torch.sigmoid(x)

def _layernorm(x, axis=-1, eps=1e-05):
    m = x.mean(dim=axis, keepdim=True)
    v = x.var(dim=axis, keepdim=True, unbiased=False)
    return (x - m) / torch.sqrt(v + eps)

def _scatter_add(tgt, idx, src, axis=0):
    return tgt.clone().index_add_(axis, idx, src)

def _segment_sum(src, seg, n):
    out = torch.zeros((n,) + src.shape[1:], dtype=src.dtype)
    return out.index_add_(0, seg, src)

def _pad(x, pw):
    flat = tuple((v for pair in reversed(pw) for v in pair))
    return torch.nn.functional.pad(x, flat)
kernel_name = 'adaLN_cond__mha_block_cached__moe_arch_mod__prompt'

def setup_inputs():

    _w = lambda a: (lambda t: t.to(torch.bfloat16) if t.is_floating_point() else t)(torch.as_tensor(a))

    def _A_setup():
        B, Sq, D = (1, 2048, 512)
        H, Sk, Dh = (32, 2048, 128)
        return {'x': _w((lambda *s: torch.randn(*s, dtype=torch.bfloat16) * 0.1)(B, Sq, D)), 'k_cache': _w((lambda *s: torch.randn(*s, dtype=torch.bfloat16) * 0.8)(B, H, Sk, Dh)), 'v_cache': _w((lambda *s: torch.randn(*s, dtype=torch.bfloat16) * 1.6)(B, H, Sk, Dh)), 'wq': _w((lambda *s: torch.randn(*s, dtype=torch.bfloat16) * 0.8)(D, H * Dh) * D ** (-0.5)), 'wo': _w((lambda *s: torch.randn(*s, dtype=torch.bfloat16) * 1.6)(H * Dh, D) * (H * Dh) ** (-0.5))}
    _A_in = _A_setup()

    def _B_setup():
        B, S, D, F, C = (1, 2048, 512, 1792, 8)
        return {'x': _w((lambda *s: torch.randn(*s, dtype=torch.bfloat16) * 0.1)(B, S, D)), 'w_r': _w((lambda *s: torch.randn(*s, dtype=torch.bfloat16) * 3.2)(D)), 'w_gate': _w((lambda *s: torch.randn(*s, dtype=torch.bfloat16) * 3.2)(D, F)), 'w_up': _w((lambda *s: torch.randn(*s, dtype=torch.bfloat16) * 3.2)(D, F)), 'w_down': _w((lambda *s: torch.randn(*s, dtype=torch.bfloat16) * 3.2)(F, D))}
    _B_in = _B_setup()
    return {'A_x': _A_in['x'], 'A_k_cache': _A_in['k_cache'], 'A_v_cache': _A_in['v_cache'], 'A_wq': _A_in['wq'], 'A_wo': _A_in['wo'], 'B_x': _B_in['x'], 'B_w_r': _B_in['w_r'], 'B_w_gate': _B_in['w_gate'], 'B_w_up': _B_in['w_up'], 'B_w_down': _B_in['w_down'], 'c': _w((lambda *s: torch.randn(*s, dtype=torch.bfloat16) * 0.2)(1, 512) * 8.0), 'w_mod': _w((lambda *s: torch.randn(*s, dtype=torch.bfloat16) * 0.2)(512, 6 * 512) * 512 ** (-0.5) * 8.0)}

def reference(A_x, A_k_cache, A_v_cache, A_wq, A_wo, B_x, B_w_r, B_w_gate, B_w_up, B_w_down, c, w_mod):

    def _A_ref(x, k_cache, v_cache, wq, wo):
        B, Sq, D = x.shape
        H, Dh = (32, 128)
        q = (lambda x, *d: x.permute(*d))((x @ wq).reshape(B, Sq, H, Dh), 0, 2, 1, 3)
        scores = torch.einsum('bhqd,bhkd->bhqk', q, k_cache) * Dh ** (-0.5)
        p = _softmax(scores, axis=-1)
        o = torch.einsum('bhqk,bhkd->bhqd', p, v_cache)
        return (lambda x, *d: x.permute(*d))(o, 0, 2, 1, 3).reshape(B, Sq, H * Dh) @ wo

    def _B_ref(x, w_r, w_gate, w_up, w_down):
        B, S, D = x.shape
        C = 8
        r = x @ w_r
        idx = torch.argsort(-r, axis=-1)[:, :C]
        idx_e = (lambda x, n, axis: torch.repeat_interleave(x, n, dim=axis))(idx.reshape(B, C, 1), D, axis=2)
        x_sel = (lambda x, i, axis: torch.take_along_dim(x, i, dim=axis))(x, idx_e, axis=1)
        g = _silu(x_sel @ w_gate)
        u = x_sel @ w_up
        y = g * u @ w_down
        r_sel = _sigmoid((lambda x, i, axis: torch.take_along_dim(x, i, dim=axis))(r, idx, axis=1))
        y = y * r_sel.reshape(B, C, 1)
        b_off = (torch.arange(B) * S).reshape(B, 1)
        flat_idx = (idx + b_off).reshape(B * C)
        zero = (x * 0.0).reshape(B * S, D)
        upd = _scatter_add(zero, flat_idx, y.reshape(B * C, D), axis=0)
        return x + upd.reshape(B, S, D)
    x = A_x
    mod = (_silu(c) @ w_mod).reshape(c.shape[0], 6, 1, -1)
    sa, ca, ga, sb, cb, gb = (mod[:, 0].clone(), mod[:, 1].clone(), mod[:, 2].clone(), mod[:, 3].clone(), mod[:, 4].clone(), mod[:, 5].clone())
    h = _layernorm(x, axis=-1) * (1 + ca) + sa
    x = x + ga * _A_ref(h, A_k_cache, A_v_cache, A_wq, A_wo)
    h = _layernorm(x, axis=-1) * (1 + cb) + sb
    x = x + gb * _B_ref(h, B_w_r, B_w_gate, B_w_up, B_w_down)
    return x
```

```cpp
#include <hip/hip_runtime.h>
#include <hip/hip_bf16.h>
#include <cstdio>
#include <cstdint>

typedef unsigned short u16;
typedef __attribute__((ext_vector_type(8))) short bf16x8;
typedef __attribute__((ext_vector_type(4))) short s16x4;
typedef __attribute__((ext_vector_type(16))) float f32x16;
typedef __attribute__((ext_vector_type(4))) float f32x4;
typedef __attribute__((ext_vector_type(2))) float f32x2;
typedef __attribute__((ext_vector_type(4))) unsigned u32x4;
typedef __attribute__((ext_vector_type(2))) __bf16 bf16x2;
#define DI __device__ __forceinline__

constexpr int S = 2048, D = 512, H = 32, DH = 128, HD = H * DH, F = 1792, C = 8;

DI float bf2f(u16 v) { return __uint_as_float(((unsigned)v) << 16); }
DI unsigned cvtpk(float lo, float hi) { f32x2 v = {lo, hi}; bf16x2 b = __builtin_convertvector(v, bf16x2); return __builtin_bit_cast(unsigned, b); }
DI u16 f2bf(float f) { return (u16)(cvtpk(f, 0.f) & 0xffffu); }
DI float rbf(float f) { return bf2f(f2bf(f)); }

__global__ __launch_bounds__(256) void k_mod(const u16* __restrict__ c, const u16* __restrict__ wmod, float* __restrict__ mod) {
  __shared__ float sc[512];
  __shared__ float red[4][64];
  const int tid = threadIdx.x;
  for (int i = tid; i < 512; i += 256) { float x = bf2f(c[i]); sc[i] = rbf(x / (1.f + __expf(-x))); }
  __syncthreads();
  const int col = blockIdx.x * 64 + (tid & 63), kq = tid >> 6;
  float acc = 0.f;
  for (int k = kq * 128; k < kq * 128 + 128; ++k) acc += sc[k] * bf2f(wmod[(size_t)k * 3072 + col]);
  red[kq][tid & 63] = acc;
  __syncthreads();
  if (tid < 64) mod[col] = rbf(red[0][tid] + red[1][tid] + red[2][tid] + red[3][tid]);
}

DI float wave_sum(float v) {
  for (int o = 32; o > 0; o >>= 1) v += __shfl_xor(v, o);
  return v;
}

DI void ln_row(const float (&x)[8], float (&y)[8]) {
  float s = 0.f;
  for (int j = 0; j < 8; ++j) s += x[j];
  s = wave_sum(s);
  const float mean = s * (1.f / 512.f);
  float q = 0.f;
  for (int j = 0; j < 8; ++j) { float d = x[j] - mean; q += d * d; }
  q = wave_sum(q);
  const float m = rbf(mean), v = rbf(q * (1.f / 512.f));
  const float sd = rbf(sqrtf(rbf(v + 1e-5f)));
  for (int j = 0; j < 8; ++j) y[j] = rbf(rbf(x[j] - m) / sd);
}

__global__ __launch_bounds__(256) void k_ln1(const u16* __restrict__ x, const float* __restrict__ mod, u16* __restrict__ h) {
  const int row = blockIdx.x * 4 + (threadIdx.x >> 6), lane = threadIdx.x & 63;
  bf16x8 xv = *(const bf16x8*)(x + (size_t)row * D + lane * 8);
  float xf[8], y[8];
  for (int j = 0; j < 8; ++j) xf[j] = bf2f((u16)xv[j]);
  ln_row(xf, y);
  bf16x8 o;
  for (int j = 0; j < 8; ++j) {
    const int k = lane * 8 + j;
    const float sa = mod[k], ca = mod[512 + k];
    o[j] = (short)f2bf(rbf(y[j] * rbf(1.f + ca)) + sa);
  }
  *(bf16x8*)(h + (size_t)row * D + lane * 8) = o;
}

__global__ __launch_bounds__(256) void k_gemm(const u16* __restrict__ A, const u16* __restrict__ B, u16* __restrict__ Cc, int M, int N, int K) {
  __shared__ __attribute__((aligned(16))) u16 As[64][40];
  __shared__ __attribute__((aligned(16))) u16 Bs[64][40];
  const int tid = threadIdx.x, lane = tid & 63, wid = tid >> 6, wr = wid >> 1, wc = wid & 1;
  const int m0 = blockIdx.y * 64, n0 = blockIdx.x * 64;
  f32x16 acc = {};
  for (int k0 = 0; k0 < K; k0 += 32) {
    { const int r = tid >> 2, c8 = (tid & 3) * 8;
      *(bf16x8*)&As[r][c8] = *(const bf16x8*)(A + (size_t)(m0 + r) * K + k0 + c8); }
    { const int kk = tid >> 3, c8 = (tid & 7) * 8;
      bf16x8 b = *(const bf16x8*)(B + (size_t)(k0 + kk) * N + n0 + c8);
      for (int j = 0; j < 8; ++j) Bs[c8 + j][kk] = (u16)b[j]; }
    __syncthreads();
    for (int ks = 0; ks < 2; ++ks) {
      bf16x8 a = *(const bf16x8*)&As[wr * 32 + (lane & 31)][ks * 16 + (lane >> 5) * 8];
      bf16x8 b = *(const bf16x8*)&Bs[wc * 32 + (lane & 31)][ks * 16 + (lane >> 5) * 8];
      acc = __builtin_amdgcn_mfma_f32_32x32x16_bf16(a, b, acc, 0, 0, 0);
    }
    __syncthreads();
  }
  for (int r = 0; r < 16; ++r) {
    const int row = (r & 3) + 8 * (r >> 2) + 4 * (lane >> 5);
    Cc[(size_t)(m0 + wr * 32 + row) * N + n0 + wc * 32 + (lane & 31)] = f2bf(acc[r]);
  }
}

constexpr int NW = 8, QBLK = 32, KVBLK = 64;
constexpr float SCALE = 0.088388347648318440f;
constexpr float THR = 8.f;
constexpr int LDQ = HD, LDK = DH, LDO = HD;
constexpr size_t SHM_V = KVBLK * DH * 2, SHM_K = KVBLK * DH * 2, SHM_ATTN = 2 * SHM_V + 2 * SHM_K + NW * 64 * 4;
#define KSWZ(row, colB) ((row) * 256 + ((colB) ^ (((row) & 7) << 4)))
#define SBAR() __builtin_amdgcn_sched_barrier(0)
DI int crow(int r, int hi) { return (r & 3) + 8 * (r >> 2) + 4 * hi; }
DI unsigned cvtpk_a(float lo, float hi) { unsigned r; asm volatile("v_cvt_pk_bf16_f32 %0, %1, %2" : "=v"(r) : "v"(lo), "v"(hi)); return r; }
DI void partialSM(f32x16& p0, f32x16& p1, float& m_reg, float& mn, float& alpha) {
  constexpr float Cc = SCALE * 1.4426950408889634f;
  float pmax = p0[0]; for (int r = 1; r < 16; ++r) pmax = fmaxf(pmax, p0[r]); for (int r = 0; r < 16; ++r) pmax = fmaxf(pmax, p1[r]);
  { auto rr = __builtin_amdgcn_permlane32_swap(__float_as_uint(pmax), __float_as_uint(pmax), false, false);
    pmax = fmaxf(__uint_as_float(rr[0]), __uint_as_float(rr[1])); }
  if (__builtin_expect(__all(pmax - m_reg <= THR / SCALE), 1)) { mn = m_reg; alpha = 1.f; }
  else { mn = fmaxf(m_reg, pmax); alpha = __builtin_amdgcn_exp2f((m_reg - mn) * Cc); m_reg = mn; }
  float mnC = -mn * Cc;
  for (int r = 0; r < 16; ++r) p0[r] = fmaf(p0[r], Cc, mnC); for (int r = 0; r < 16; ++r) p1[r] = fmaf(p1[r], Cc, mnC);
  for (int r = 0; r < 16; ++r) p0[r] = __builtin_amdgcn_exp2f(p0[r]);
}
DI void finishSM(f32x16& p0, f32x16& p1, float alpha, float& l_reg, bf16x8& pa0, bf16x8& pa1, bf16x8& pa2, bf16x8& pa3) {
  for (int r = 0; r < 16; ++r) p1[r] = __builtin_amdgcn_exp2f(p1[r]);
  float ps = 0; for (int r = 0; r < 16; ++r) ps += p0[r]; for (int r = 0; r < 16; ++r) ps += p1[r];
  { auto rr = __builtin_amdgcn_permlane32_swap(__float_as_uint(ps), __float_as_uint(ps), false, false);
    ps = __uint_as_float(rr[0]) + __uint_as_float(rr[1]); }
  l_reg = l_reg * alpha + ps;
#define PK4(P, BASE, OUT) do { unsigned a0 = cvtpk_a(P[BASE + 0], P[BASE + 1]), a1 = cvtpk_a(P[BASE + 2], P[BASE + 3]);   \
    unsigned b0 = cvtpk_a(P[BASE + 4], P[BASE + 5]), b1 = cvtpk_a(P[BASE + 6], P[BASE + 7]);                              \
    auto r0 = __builtin_amdgcn_permlane32_swap(a0, b0, false, false); auto r1 = __builtin_amdgcn_permlane32_swap(a1, b1, false, false); \
    u32x4 w = {r0[0], r1[0], r0[1], r1[1]}; OUT = *reinterpret_cast<bf16x8*>(&w); } while (0)
  PK4(p0, 0, pa0); PK4(p0, 8, pa1); PK4(p1, 0, pa2); PK4(p1, 8, pa3);
#undef PK4
}
DI void qkt(f32x16& p0, f32x16& p1, const u16* Ks, const bf16x8* qr, int r32, int hi) {
  p0 = f32x16{}; p1 = f32x16{};
  for (int d0 = 0; d0 < 8; ++d0) { int cb = (d0 * 16 + hi * 8) * 2;
    bf16x8 b0 = *reinterpret_cast<const bf16x8*>((const char*)Ks + KSWZ(r32, cb));
    bf16x8 b1 = *reinterpret_cast<const bf16x8*>((const char*)Ks + KSWZ(32 + r32, cb));
    p0 = __builtin_amdgcn_mfma_f32_32x32x16_bf16(b0, qr[d0], p0, 0, 0, 0);
    p1 = __builtin_amdgcn_mfma_f32_32x32x16_bf16(b1, qr[d0], p1, 0, 0, 0); }
}
DI int v_st(int k, int c) { const int kk = (k & ~0xC) | ((k & 4) << 1) | ((k & 8) >> 1); return ((kk >> 3) * 4 + (c >> 5)) * 512 + ((kk & 7) * 32 + (c & 31)) * 2; }
DI int v_rd_base(int lane) { return ((lane & 3) << 3) | (((lane >> 2) & 3) << 6) | (((lane >> 4) & 1) << 5) | (((lane >> 5) & 1) << 8); }
constexpr int v_rd_off(int d0, int ks, int half) { return d0 * 512 + ks * 4096 + half * 2048; }
template <int OFF> DI s16x4 tr_read(int vb) {
  s16x4 r; asm volatile("ds_read_b64_tr_b16 %0, %1 offset:%2" : "=&v"(r) : "v"(vb), "i"(OFF) : "memory"); return r;
}
template <int D0> DI void pv_one(f32x16& od, int vb, bf16x8 pa0, bf16x8 pa1, bf16x8 pa2, bf16x8 pa3) {
  const s16x4 l0 = tr_read<v_rd_off(D0, 0, 0)>(vb), h0 = tr_read<v_rd_off(D0, 0, 1)>(vb), l1 = tr_read<v_rd_off(D0, 1, 0)>(vb), h1 = tr_read<v_rd_off(D0, 1, 1)>(vb);
  const s16x4 l2 = tr_read<v_rd_off(D0, 2, 0)>(vb), h2 = tr_read<v_rd_off(D0, 2, 1)>(vb), l3 = tr_read<v_rd_off(D0, 3, 0)>(vb), h3 = tr_read<v_rd_off(D0, 3, 1)>(vb);
  asm volatile("s_waitcnt lgkmcnt(0)" ::: "memory"); SBAR();
#define PK(L, H) (bf16x8){L[0], L[1], L[2], L[3], H[0], H[1], H[2], H[3]}
  od = __builtin_amdgcn_mfma_f32_32x32x16_bf16(pa0, PK(l0, h0), od, 0, 0, 0);
  od = __builtin_amdgcn_mfma_f32_32x32x16_bf16(pa1, PK(l1, h1), od, 0, 0, 0);
  od = __builtin_amdgcn_mfma_f32_32x32x16_bf16(pa2, PK(l2, h2), od, 0, 0, 0);
  od = __builtin_amdgcn_mfma_f32_32x32x16_bf16(pa3, PK(l3, h3), od, 0, 0, 0);
#undef PK
}
DI void pv_d0(f32x16* o, int vb, bf16x8 pa0, bf16x8 pa1, bf16x8 pa2, bf16x8 pa3) {
  pv_one<0>(o[0], vb, pa0, pa1, pa2, pa3); pv_one<1>(o[1], vb, pa0, pa1, pa2, pa3); pv_one<2>(o[2], vb, pa0, pa1, pa2, pa3); pv_one<3>(o[3], vb, pa0, pa1, pa2, pa3);
}
DI void attn_dense_body(const u16* __restrict__ Qb, const u16* __restrict__ Kh, const u16* __restrict__ Vh, u16* __restrict__ Ob, int seq, char* lds) {
  constexpr int SDEPTH = 2;
  const int tid = threadIdx.x, wid = tid >> 6, lane = tid & 63, r32 = lane & 31, hi = lane >> 5;
  u16* V_lds = (u16*)lds; u16* K_lds = (u16*)(lds + 2 * SHM_V);
  float* ws = (float*)(lds + 2 * SHM_V + 2 * SHM_K) + wid * 64; float* li_l = ws; float* al_l = ws + 32;
  float m_reg = -1e30f, l_reg = 0; f32x16 o[4] = {}; bf16x8 qr[8];
  const u16* Qw = Qb + (long)(wid * QBLK + r32) * LDQ + hi * 8;
#pragma unroll
  for (int d0 = 0; d0 < 8; ++d0) qr[d0] = *(const bf16x8*)(Qw + d0 * 16);
  const int sr = tid >> 4, sc = (tid & 15) * 8, vst0 = v_st(sr, sc), vst1 = v_st(32 + sr, sc);
  const int vb0 = (int)(uintptr_t)V_lds + v_rd_base(lane);
  struct { bf16x8 vs0, vs1, ks0, ks1; } sr_[SDEPTH];
#define LD8(p) (*(const bf16x8*)(p))
#define SLOAD(i, k0) do { sr_[i].vs0 = LD8(&Vh[(long)((k0) + sr) * LDK + sc]); sr_[i].vs1 = LD8(&Vh[(long)((k0) + 32 + sr) * LDK + sc]); \
    sr_[i].ks0 = LD8(&Kh[(long)((k0) + sr) * LDK + sc]); sr_[i].ks1 = LD8(&Kh[(long)((k0) + 32 + sr) * LDK + sc]); } while (0)
#define SWRITE(b, i) do { *(bf16x8*)((char*)V_lds + (b) * SHM_V + vst0) = sr_[i].vs0;          \
    *(bf16x8*)((char*)V_lds + (b) * SHM_V + vst1) = sr_[i].vs1; int kc = sc * 2;               \
    *(bf16x8*)((char*)K_lds + (b) * SHM_K + KSWZ(sr, kc)) = sr_[i].ks0;                       \
    *(bf16x8*)((char*)K_lds + (b) * SHM_K + KSWZ(32 + sr, kc)) = sr_[i].ks1; } while (0)
#define SWAIT() asm volatile("s_waitcnt vmcnt(4)" ::: "memory")
#define RESC(a) do { if (__any((a) < 1.f)) { if (hi == 0) al_l[r32] = (a); asm volatile("s_waitcnt lgkmcnt(0)" ::: "memory"); \
    for (int d = 0; d < 4; ++d) for (int r = 0; r < 16; ++r) o[d][r] *= al_l[crow(r, hi)]; } } while (0)
  f32x16 pA0, pA1, pB0, pB1; float mnA, mnB, alA, alB; bf16x8 pa0, pa1, pa2, pa3; const int NT = seq / KVBLK;
  constexpr int SE = 0, SO = SDEPTH - 1;
  SLOAD(SE, 0); asm volatile("s_waitcnt vmcnt(0)" ::: "memory"); SWRITE(0, SE); __syncthreads();
  qkt(pA0, pA1, K_lds, qr, r32, hi); partialSM(pA0, pA1, m_reg, mnA, alA);
  SLOAD(SO, KVBLK); if (2 < NT) SLOAD(SE, 2 * KVBLK);
  SWAIT(); SWRITE(1, SO); __syncthreads();
  for (int j = 1; j + 1 < NT; j += 2) {
    SBAR(); qkt(pB0, pB1, (u16*)((char*)K_lds + SHM_K), qr, r32, hi);
    finishSM(pA0, pA1, alA, l_reg, pa0, pa1, pa2, pa3); SBAR();
    SLOAD(SO, (j + SDEPTH) * KVBLK); SBAR();
    pv_d0(o, vb0, pa0, pa1, pa2, pa3); partialSM(pB0, pB1, m_reg, mnB, alB);
    __syncthreads(); SWAIT(); SWRITE(0, SE);
    RESC(alB); __syncthreads();
    SBAR(); qkt(pA0, pA1, K_lds, qr, r32, hi);
    finishSM(pB0, pB1, alB, l_reg, pa0, pa1, pa2, pa3); SBAR();
    if (j + 3 < NT) SLOAD(SE, (j + 1 + SDEPTH) * KVBLK); SBAR();
    pv_d0(o, vb0 + (int)SHM_V, pa0, pa1, pa2, pa3); partialSM(pA0, pA1, m_reg, mnA, alA);
    __syncthreads(); SWAIT(); SWRITE(1, SO);
    RESC(alA); __syncthreads();
  }
  SBAR(); qkt(pB0, pB1, (u16*)((char*)K_lds + SHM_K), qr, r32, hi);
  finishSM(pA0, pA1, alA, l_reg, pa0, pa1, pa2, pa3); SBAR();
  pv_d0(o, vb0, pa0, pa1, pa2, pa3); partialSM(pB0, pB1, m_reg, mnB, alB);
  __syncthreads(); RESC(alB);
  finishSM(pB0, pB1, alB, l_reg, pa0, pa1, pa2, pa3); SBAR();
  pv_d0(o, vb0 + (int)SHM_V, pa0, pa1, pa2, pa3);
  if (hi == 0) li_l[r32] = l_reg; asm volatile("s_waitcnt lgkmcnt(0)" ::: "memory");
  float rli[16];
#pragma unroll
  for (int r = 0; r < 16; ++r) rli[r] = __builtin_amdgcn_rcpf(li_l[crow(r, hi)]);
  u16* Ow = Ob + (long)(wid * QBLK) * LDO;
#pragma unroll
  for (int r = 0; r < 16; ++r) { int orow = crow(r, hi);
    for (int d0 = 0; d0 < 4; ++d0) Ow[(long)orow * LDO + d0 * 32 + r32] = f2bf(o[d0][r] * rli[r]); }
}
__global__ __launch_bounds__(64 * NW, 1)
void k_attn(const u16* __restrict__ Q, const u16* __restrict__ K, const u16* __restrict__ V, u16* __restrict__ O) {
  extern __shared__ __attribute__((aligned(16))) char shm[];
  const int b = blockIdx.x;
  const int h = (b & 7) * 4 + ((b >> 3) & 3), qb = b >> 5;
  const long i0 = (long)qb * 256;
  attn_dense_body(Q + i0 * HD + h * DH, K + (long)h * S * DH, V + (long)h * S * DH, O + i0 * HD + h * DH, S, shm);
}

__global__ __launch_bounds__(256) void k_ln2(const u16* __restrict__ x, const u16* __restrict__ ao, const float* __restrict__ mod, const u16* __restrict__ wr,
                                             u16* __restrict__ x1o, u16* __restrict__ h2o, float* __restrict__ rr, u16* __restrict__ out) {
  const int row = blockIdx.x * 4 + (threadIdx.x >> 6), lane = threadIdx.x & 63;
  bf16x8 xv = *(const bf16x8*)(x + (size_t)row * D + lane * 8);
  bf16x8 av = *(const bf16x8*)(ao + (size_t)row * D + lane * 8);
  bf16x8 wv = *(const bf16x8*)(wr + lane * 8);
  float x1[8], y[8];
  for (int j = 0; j < 8; ++j) { const int k = lane * 8 + j; x1[j] = rbf(bf2f((u16)xv[j]) + rbf(mod[1024 + k] * bf2f((u16)av[j]))); }
  ln_row(x1, y);
  bf16x8 o1, o2, o3; float racc = 0.f;
  for (int j = 0; j < 8; ++j) {
    const int k = lane * 8 + j;
    const float sb = mod[1536 + k], cb = mod[2048 + k], gb = mod[2560 + k];
    const float h2 = rbf(rbf(y[j] * rbf(1.f + cb)) + sb);
    racc += h2 * bf2f((u16)wv[j]);
    o1[j] = (short)f2bf(x1[j]); o2[j] = (short)f2bf(h2);
    o3[j] = (short)f2bf(x1[j] + rbf(gb * h2));
  }
  racc = wave_sum(racc);
  *(bf16x8*)(x1o + (size_t)row * D + lane * 8) = o1;
  *(bf16x8*)(h2o + (size_t)row * D + lane * 8) = o2;
  *(bf16x8*)(out + (size_t)row * D + lane * 8) = o3;
  if (lane == 0) rr[row] = rbf(racc);
}

__global__ __launch_bounds__(256) void k_moe1(const float* __restrict__ rr, const u16* __restrict__ h2, const u16* __restrict__ wg, const u16* __restrict__ wu,
                                              float* __restrict__ act, int* __restrict__ idxo) {
  __shared__ float sr[2048];
  __shared__ int sidx[8];
  __shared__ float sx[8][512];
  __shared__ float redv[4]; __shared__ int redi[4];
  __shared__ float pg[4][8][64], pu[4][8][64];
  const int tid = threadIdx.x, lane = tid & 63, wid = tid >> 6;
  for (int i = tid; i < 2048; i += 256) sr[i] = rr[i];
  __syncthreads();
  for (int c = 0; c < 8; ++c) {
    float bv = -INFINITY; int bi = 0x7fffffff;
    for (int i = tid; i < 2048; i += 256) { float v = sr[i]; if (v > bv || (v == bv && i < bi)) { bv = v; bi = i; } }
    for (int o = 32; o > 0; o >>= 1) { float ov = __shfl_xor(bv, o); int oi = __shfl_xor(bi, o); if (ov > bv || (ov == bv && oi < bi)) { bv = ov; bi = oi; } }
    if (lane == 0) { redv[wid] = bv; redi[wid] = bi; }
    __syncthreads();
    if (tid == 0) { float v = redv[0]; int ii = redi[0]; for (int w = 1; w < 4; ++w) if (redv[w] > v || (redv[w] == v && redi[w] < ii)) { v = redv[w]; ii = redi[w]; }
      if (ii == 0x7fffffff) ii = c;
      sidx[c] = ii; sr[ii] = -INFINITY; }
    __syncthreads();
  }
  if (blockIdx.x == 0 && tid < 8) idxo[tid] = sidx[tid];
  for (int i = tid; i < 8 * 512; i += 256) sx[i >> 9][i & 511] = bf2f(h2[(size_t)sidx[i >> 9] * D + (i & 511)]);
  __syncthreads();
  const int col = blockIdx.x * 64 + lane;
  float g[8] = {}, u[8] = {};
  for (int k = wid * 128; k < wid * 128 + 128; ++k) {
    const float wgv = bf2f(wg[(size_t)k * F + col]), wuv = bf2f(wu[(size_t)k * F + col]);
    for (int c = 0; c < 8; ++c) { g[c] += sx[c][k] * wgv; u[c] += sx[c][k] * wuv; }
  }
  for (int c = 0; c < 8; ++c) { pg[wid][c][lane] = g[c]; pu[wid][c][lane] = u[c]; }
  __syncthreads();
  for (int i = tid; i < 8 * 64; i += 256) {
    const int c = i >> 6, l = i & 63;
    const float gg = rbf(pg[0][c][l] + pg[1][c][l] + pg[2][c][l] + pg[3][c][l]);
    const float uu = rbf(pu[0][c][l] + pu[1][c][l] + pu[2][c][l] + pu[3][c][l]);
    const float sg = rbf(gg / (1.f + __expf(-gg)));
    act[c * F + blockIdx.x * 64 + l] = rbf(sg * uu);
  }
}
__global__ __launch_bounds__(256) void k_moe2(const float* __restrict__ act, const int* __restrict__ idx, const float* __restrict__ rr, const u16* __restrict__ wd,
                                              const u16* __restrict__ x1, const u16* __restrict__ h2, const float* __restrict__ mod, u16* __restrict__ out) {
  __shared__ float sa[8][F];
  __shared__ float py[4][8][64];
  const int tid = threadIdx.x, lane = tid & 63, wid = tid >> 6;
  for (int i = tid; i < 8 * F; i += 256) sa[i / F][i % F] = act[i];
  __syncthreads();
  const int col = blockIdx.x * 64 + lane;
  float y[8] = {};
  for (int k = wid * 448; k < wid * 448 + 448; ++k) {
    const float w = bf2f(wd[(size_t)k * D + col]);
    for (int c = 0; c < 8; ++c) y[c] += sa[c][k] * w;
  }
  for (int c = 0; c < 8; ++c) py[wid][c][lane] = y[c];
  __syncthreads();
  for (int i = tid; i < 8 * 64; i += 256) {
    const int c = i >> 6, l = i & 63, n = blockIdx.x * 64 + l, row = idx[c];
    const float yy = rbf(py[0][c][l] + py[1][c][l] + py[2][c][l] + py[3][c][l]);
    const float r = rr[row];
    const float sg = rbf(1.f / (1.f + __expf(-r)));
    const float ys = rbf(yy * sg);
    const float ob = rbf(bf2f(h2[(size_t)row * D + n]) + ys);
    out[(size_t)row * D + n] = f2bf(bf2f(x1[(size_t)row * D + n]) + rbf(mod[2560 + n] * ob));
  }
}

extern "C" void kernel_launch(void* const* d_in, const int* in_sizes, int n_in, void* d_out, int out_size, void* d_ws, size_t ws_size, hipStream_t stream) {
  const u16* x = (const u16*)d_in[0]; const u16* kc = (const u16*)d_in[1]; const u16* vc = (const u16*)d_in[2];
  const u16* wq = (const u16*)d_in[3]; const u16* wo = (const u16*)d_in[4];
  const u16* wr = (const u16*)d_in[6]; const u16* wg = (const u16*)d_in[7]; const u16* wu = (const u16*)d_in[8]; const u16* wd = (const u16*)d_in[9];
  const u16* c = (const u16*)d_in[10]; const u16* wmod = (const u16*)d_in[11];
  u16* out = (u16*)d_out;
  char* ws = (char*)d_ws;
  float* mod = (float*)(ws);
  float* rr = (float*)(ws + 16384);
  int* idx = (int*)(ws + 32768);
  float* act = (float*)(ws + 65536);
  u16* h = (u16*)(ws + (1 << 20));
  u16* x1 = (u16*)(ws + (3 << 20));
  u16* h2 = (u16*)(ws + (5 << 20));
  u16* ao = (u16*)(ws + (7 << 20));
  u16* q = (u16*)(ws + (16 << 20));
  u16* o = (u16*)(ws + (32 << 20));
  hipFuncSetAttribute((const void*)k_attn, hipFuncAttributeMaxDynamicSharedMemorySize, (int)SHM_ATTN);
  k_mod<<<48, 256, 0, stream>>>(c, wmod, mod);
  k_ln1<<<512, 256, 0, stream>>>(x, mod, h);
  k_gemm<<<dim3(HD / 64, S / 64), 256, 0, stream>>>(h, wq, q, S, HD, D);
  k_attn<<<256, 512, SHM_ATTN, stream>>>(q, kc, vc, o);
  k_gemm<<<dim3(D / 64, S / 64), 256, 0, stream>>>(o, wo, ao, S, D, HD);
  k_ln2<<<512, 256, 0, stream>>>(x, ao, mod, wr, x1, h2, rr, out);
  k_moe1<<<F / 64, 256, 0, stream>>>(rr, h2, wg, wu, act, idx);
  k_moe2<<<D / 64, 256, 0, stream>>>(act, idx, rr, wd, x1, h2, mod, out);
}
```

```cpp
#include <hip/hip_runtime.h>
#include <hip/hip_bf16.h>
#include <cstdint>

typedef unsigned short u16;
typedef __attribute__((ext_vector_type(8))) short bf16x8;
typedef __attribute__((ext_vector_type(16))) float f32x16;
typedef __attribute__((ext_vector_type(4))) float f32x4;
typedef __attribute__((ext_vector_type(2))) float f32x2;
typedef __attribute__((ext_vector_type(4))) unsigned u32x4;
typedef __attribute__((ext_vector_type(2))) unsigned u32x2;
typedef __attribute__((ext_vector_type(2))) __bf16 bf16x2;
#define DI __device__ __forceinline__

#define TLR(i)
#define TLRS
#define TL_BEGIN
#define TL_END(k)
#define TL_ARG
#define TLP(i)
#define TL_PASS
constexpr int S = 2048, D = 512, H = 32, DH = 128, HD = H * DH, F = 1792;

DI float bf2f(u16 v) { return __uint_as_float(((unsigned)v) << 16); }
DI float bflo(unsigned w) { return __uint_as_float(w << 16); }
DI float bfhi(unsigned w) { return __uint_as_float(w & 0xffff0000u); }
DI unsigned cvtpk(float lo, float hi) { f32x2 v = {lo, hi}; bf16x2 b = __builtin_convertvector(v, bf16x2); return __builtin_bit_cast(unsigned, b); }
DI u16 f2bf(float f) { return (u16)(cvtpk(f, 0.f) & 0xffffu); }
DI float rbf(float f) { return __uint_as_float(cvtpk(0.f, f)); }

DI float fdiv(float a, float b, float r) { const float q = a * r; return fmaf(fmaf(-q, b, a), r, q); }

typedef __attribute__((address_space(1))) unsigned gu32;
typedef __attribute__((ext_vector_type(4))) short s16x4;
#define RLX_AGENT __ATOMIC_RELAXED, __HIP_MEMORY_SCOPE_AGENT
constexpr unsigned SPIN_CAP = 1u << 16;
DI void unpack8(const u32x4& w, float (&f)[8]) {
#pragma unroll
  for (int d = 0; d < 4; ++d) { f[2 * d] = bflo(w[d]); f[2 * d + 1] = bfhi(w[d]); }
}
DI u32x4 pack8(const float (&f)[8]) { u32x4 w = {cvtpk(f[0], f[1]), cvtpk(f[2], f[3]), cvtpk(f[4], f[5]), cvtpk(f[6], f[7])}; return w; }
DI float red8(float v) {
  v += __int_as_float(__builtin_amdgcn_update_dpp(0, __float_as_int(v), 0x128, 0xf, 0xf, false));
  { auto r = __builtin_amdgcn_permlane16_swap(__float_as_uint(v), __float_as_uint(v), false, false); v = __uint_as_float(r[0]) + __uint_as_float(r[1]); }
  { auto r = __builtin_amdgcn_permlane32_swap(__float_as_uint(v), __float_as_uint(v), false, false); v = __uint_as_float(r[0]) + __uint_as_float(r[1]); }
  return v;
}
DI unsigned umax(unsigned a, unsigned b) { return a > b ? a : b; }
DI unsigned umin(unsigned a, unsigned b) { return a < b ? a : b; }
DI void bar_lds() { asm volatile("s_waitcnt lgkmcnt(0)\n\ts_barrier" ::: "memory"); }
DI unsigned wave_max_u32(unsigned v) {
  v = umax(v, (unsigned)__builtin_amdgcn_update_dpp(0, (int)v, 0x111, 0xf, 0xf, false));
  v = umax(v, (unsigned)__builtin_amdgcn_update_dpp(0, (int)v, 0x112, 0xf, 0xf, false));
  v = umax(v, (unsigned)__builtin_amdgcn_update_dpp(0, (int)v, 0x114, 0xf, 0xf, false));
  v = umax(v, (unsigned)__builtin_amdgcn_update_dpp(0, (int)v, 0x118, 0xf, 0xf, false));
  v = umax(v, (unsigned)__builtin_amdgcn_update_dpp(0, (int)v, 0x142, 0xa, 0xf, false));
  v = umax(v, (unsigned)__builtin_amdgcn_update_dpp(0, (int)v, 0x143, 0xc, 0xf, false));
  return (unsigned)__builtin_amdgcn_readlane((int)v, 63);
}
DI float key2r(unsigned key) { const unsigned o = key >> 16; const unsigned b = (o & 0x8000u) ? (o & 0x7fffu) : (~o & 0xffffu); return __uint_as_float(b << 16); }
DI __amdgpu_buffer_rsrc_t mkrsrc(const void* p, unsigned bytes) { return __builtin_amdgcn_make_buffer_rsrc((void*)p, (short)0, (int)bytes, 0x00020000); }
DI u32x4 ld16_sc1(__amdgpu_buffer_rsrc_t r, unsigned off) { return __builtin_bit_cast(u32x4, __builtin_amdgcn_raw_buffer_load_b128(r, (int)off, 0, 16)); }
DI void st16_sc1(__amdgpu_buffer_rsrc_t r, unsigned off, u32x4 v) { __builtin_amdgcn_raw_buffer_store_b128(v, r, (int)off, 0, 16); }
DI void drain() { asm volatile("s_waitcnt vmcnt(0)" ::: "memory"); }
template <int RB> DI void tr_frags2(unsigned addr, bf16x8& b0, bf16x8& b1) {
  s16x4 r0, r1, r2, r3;
  asm volatile("ds_read_b64_tr_b16 %0, %4\n\tds_read_b64_tr_b16 %1, %4 offset:%5\n\tds_read_b64_tr_b16 %2, %4 offset:%6\n\tds_read_b64_tr_b16 %3, %4 offset:%7\n\ts_waitcnt lgkmcnt(0)"
               : "=&v"(r0), "=&v"(r1), "=&v"(r2), "=&v"(r3) : "v"(addr), "i"(4 * RB), "i"(32 * RB), "i"(36 * RB) : "memory");
  b0 = __builtin_shufflevector(r0, r1, 0, 1, 2, 3, 4, 5, 6, 7);
  b1 = __builtin_shufflevector(r2, r3, 0, 1, 2, 3, 4, 5, 6, 7);
}

DI void tr_frags2b(unsigned a0, unsigned a1, bf16x8& b0, bf16x8& b1) {
  s16x4 r0, r1, r2, r3;
  asm volatile("ds_read_b64_tr_b16 %0, %4\n\tds_read_b64_tr_b16 %1, %4 offset:128\n\tds_read_b64_tr_b16 %2, %5\n\tds_read_b64_tr_b16 %3, %5 offset:128\n\ts_waitcnt lgkmcnt(0)"
               : "=&v"(r0), "=&v"(r1), "=&v"(r2), "=&v"(r3) : "v"(a0), "v"(a1) : "memory");
  b0 = __builtin_shufflevector(r0, r1, 0, 1, 2, 3, 4, 5, 6, 7);
  b1 = __builtin_shufflevector(r2, r3, 0, 1, 2, 3, 4, 5, 6, 7);
}
typedef __attribute__((address_space(1))) unsigned long long gu64;
DI float dpp_add(float v, const int ctrl_) {
  switch (ctrl_) {
    case 0: return v + __int_as_float(__builtin_amdgcn_update_dpp(0, __float_as_int(v), 0xb1, 0xf, 0xf, false));
    case 1: return v + __int_as_float(__builtin_amdgcn_update_dpp(0, __float_as_int(v), 0x4e, 0xf, 0xf, false));
    case 2: return v + __int_as_float(__builtin_amdgcn_update_dpp(0, __float_as_int(v), 0x124, 0xf, 0xf, false));
    default: return v + __int_as_float(__builtin_amdgcn_update_dpp(0, __float_as_int(v), 0x128, 0xf, 0xf, false));
  }
}
DI float x16_add(float v) { auto r = __builtin_amdgcn_permlane16_swap(__float_as_uint(v), __float_as_uint(v), false, false); return __uint_as_float(r[0]) + __uint_as_float(r[1]); }
DI float x32_add(float v) { auto r = __builtin_amdgcn_permlane32_swap(__float_as_uint(v), __float_as_uint(v), false, false); return __uint_as_float(r[0]) + __uint_as_float(r[1]); }
DI float red16(float v) { v = dpp_add(v, 0); v = dpp_add(v, 1); v = dpp_add(v, 2); return dpp_add(v, 3); }
DI float red32(float v) { v = dpp_add(v, 0); v = dpp_add(v, 1); v = dpp_add(v, 2); v = dpp_add(v, 3); return x16_add(v); }

#define NSEL 2
constexpr int KT_W = 131072 + 4096, KT_LDS = 163840;
__global__ __launch_bounds__(256, 1) void k_main(char* ws, const u16* __restrict__ wq, const u16* __restrict__ K, const u16* __restrict__ V, const u16* __restrict__ wo,
                                                 const u16* __restrict__ wg, const u16* __restrict__ wu, const u16* __restrict__ wd,
                                                 const u16* __restrict__ x, const u16* __restrict__ wr, const u16* __restrict__ cvec, const u16* __restrict__ wmod, u16* out TL_ARG) {
  extern __shared__ __attribute__((aligned(16))) char lds[];
  unsigned tid = threadIdx.x;
  unsigned tag;
  TLR(0);
  {
    const int b = blockIdx.x, lane = tid & 63, wid = tid >> 6;
    asm volatile("s_getpc_b64 s[36:37]\n\tL_PC1:\n\ts_and_b32 s37, s37, 0xffff\n\ts_and_b32 s36, s36, -16\n\ts_mov_b32 s38, L_CODE_END-L_PC1\n\ts_mov_b32 s39, 0x20000\n\t"
                 "buffer_load_dwordx4 a[252:255], %0, s[36:39], 0 offen"
                 :: "v"(((b >> 3) * 256 + tid) * 16) : "s36", "s37", "s38", "s39", "a252", "a253", "a254", "a255", "scc", "memory");
    gu32* fl = (gu32*)(ws + 16384);
    const unsigned ep = __hip_atomic_load(fl, RLX_AGENT);
    const int row = (b >> 5) * 256 + ((b & 7) * 4 + ((b >> 3) & 3)) * 8 + (tid >> 5), u = tid & 31;
    const u32x4 xa = *(const u32x4*)(x + (size_t)row * D + 16 * u), xb = *(const u32x4*)(x + (size_t)row * D + 16 * u + 8);
    const int n0 = (b < 192 ? (b & 7) * 24 + (b >> 3) : 0) * 16, kq = tid >> 1;
    u32x4 w[4];
#pragma unroll
    for (int i = 0; i < 4; ++i) w[i] = *(const u32x4*)(wmod + (size_t)(4 * kq + i) * 3072 + n0 + (tid & 1) * 8);
    const u32x2 cw = *(const u32x2*)(cvec + 4 * kq);
    __builtin_amdgcn_sched_barrier(0);
    const __amdgpu_buffer_rsrc_t rW = mkrsrc(ws, 3u << 20);
    TLR(1);
    {
      float xf[16];
#pragma unroll
      for (int d = 0; d < 4; ++d) { xf[2 * d] = bflo(xa[d]); xf[2 * d + 1] = bfhi(xa[d]); xf[8 + 2 * d] = bflo(xb[d]); xf[9 + 2 * d] = bfhi(xb[d]); }
      float s = 0.f;
#pragma unroll
      for (int e = 0; e < 16; ++e) s += xf[e];
      const float mean = red32(s) * (1.f / 512.f);
      float qq = 0.f;
#pragma unroll
      for (int e = 0; e < 16; ++e) { const float d = xf[e] - mean; qq += d * d; }
      qq = red32(qq);
      const float m = rbf(mean), var = rbf(qq * (1.f / 512.f));
      const float sd = rbf(sqrtf(rbf(var + 1e-5f)));
      const float rs = 1.f / sd;
      unsigned y[8];
#pragma unroll
      for (int d = 0; d < 8; ++d) y[d] = cvtpk(fdiv(rbf(xf[2 * d] - m), sd, rs), fdiv(rbf(xf[2 * d + 1] - m), sd, rs));
      const bool odd = u & 1;
      unsigned r[4];
#pragma unroll
      for (int d = 0; d < 4; ++d) r[d] = (unsigned)__builtin_amdgcn_update_dpp(0, (int)(odd ? y[d] : y[4 + d]), 0xb1, 0xf, 0xf, false);
      const unsigned base = (1u << 20) + (unsigned)(((row >> 4) * 16 + (u >> 1)) * 64 + (odd ? 32 : 0) + (row & 15)) * 16u;
      u32x4 o0, o1;
      if (!odd) { o0 = u32x4{y[0], y[1], r[0], r[1]}; o1 = u32x4{y[2], y[3], r[2], r[3]}; }
      else      { o0 = u32x4{r[0], r[1], y[4], y[5]}; o1 = u32x4{r[2], r[3], y[6], y[7]}; }
      st16_sc1(rW, base, o0); st16_sc1(rW, base + 256u, o1);
    }
    TLR(2);
    float* red = (float*)(lds + 131072);
    {
      float acc[8] = {};
#pragma unroll
      for (int i = 0; i < 4; ++i) {
        const float xx = (i & 1) ? bfhi(cw[i >> 1]) : bflo(cw[i >> 1]);
        const float sc = rbf(xx * __builtin_amdgcn_rcpf(1.f + __expf(-xx)));
#pragma unroll
        for (int d = 0; d < 4; ++d) { acc[2 * d] += sc * bflo(w[i][d]); acc[2 * d + 1] += sc * bfhi(w[i][d]); }
      }
#pragma unroll
      for (int e = 0; e < 8; ++e) { float v = dpp_add(acc[e], 1); v = dpp_add(v, 2); v = dpp_add(v, 3); v = x16_add(v); acc[e] = x32_add(v); }
      if (lane < 2) {
#pragma unroll
        for (int e = 0; e < 8; ++e) red[wid * 16 + lane * 8 + e] = acc[e];
      }
    }
    TLR(3);
    if (wid == 3) {
      if (lane < 8) __hip_atomic_store(fl + 4096 + b * 8 + lane, 0u, RLX_AGENT);
      if (lane >= 8 && lane < 36) __hip_atomic_store(fl + 8192 + b * 28 + lane - 8, 0xffffffffu, RLX_AGENT);
      if (lane == 36) __hip_atomic_store(fl + 1024 + b, 0u, RLX_AGENT);
      if (lane == 37 && b < 8) __hip_atomic_store(fl + 512 + b * 32, 0u, RLX_AGENT);
      if (lane >= 40 && lane < 48 && b < 8) __hip_atomic_store(fl + 1536 + b * 32 + lane - 40, 0u, RLX_AGENT);
    }
    bar_lds();
    if (wid == 0 && b < 192) {
      const int i = lane & 15, g = lane >> 4, n = n0 + i;
      const float m = rbf(red[i] + red[16 + i] + red[32 + i] + red[48 + i]);
      const int slot = n0 >> 9;
      const unsigned mb = __float_as_uint((slot == 1 || slot == 4) ? rbf(1.f + m) : m) >> 16;
      const unsigned nb = (unsigned)__builtin_amdgcn_update_dpp(0, (int)mb, 0xb1, 0xf, 0xf, false);
      if (g == 0 && !(i & 1)) __hip_atomic_store((gu32*)(ws + 2 * n), mb | (nb << 16), RLX_AGENT);
      const int k = n & 511, step = k >> 5, a = (k >> 4) & 1;
      if (slot == 0 && g == 0) __hip_atomic_store((gu32*)(ws + 8192 + ((step * 4 + (i >> 2)) * 16 + 8 + 4 * a + (i & 3)) * 4), __float_as_uint(m), RLX_AGENT);
      if (slot == 1) {
        const unsigned long long v = g == (i >> 2) ? (unsigned long long)mb << (16 * (i & 3)) : 0ull;
        __hip_atomic_store((gu64*)(ws + 131072 + step * 1024 + lane * 16 + 8 * a), v, RLX_AGENT);
      }
    }
    tag = __builtin_amdgcn_readfirstlane(ep) + 1u;
    TLR(4);
    TLR(5);
    TLRS;
  }
  asm volatile(
      "L_CODE_BEGIN:\n\t"
      ".p2align 3\n\t"
      "v_and_b32_e32 v241, 63, %0\n\t"
      "v_lshrrev_b32_e32 v218, 6, %0\n\t"
      "v_mov_b32_e32 v251, %0\n\t"
      "s_and_b32 s68, %6, 7\n\t"
      "s_lshr_b32 s69, %6, 3\n\t"
      "v_readfirstlane_b32 s56, v218\n\t"
      "s_and_b32 s74, s69, 3\n\t"
      "s_lshl_b32 s68, s68, 2\n\t"
      "s_add_u32 s57, s68, s74\n\t"
      "s_lshr_b32 s58, s69, 2\n\t"
      "s_mov_b64 s[52:53], %1\n\t"
      "s_nop 0\n\t"
      "s_add_u32 s52, s52, 8192\n\t"
      "s_addc_u32 s53, s53, 0\n\t"
      "s_nop 0\n\t"
      "s_and_b32 s53, s53, 0xffff\n\t"
      "s_mov_b32 s54, 4096\n\t"
      "s_mov_b32 s55, 0x20000\n\t"
      "s_mov_b64 s[80:81], %1\n\t"
      "s_nop 0\n\t"
      "s_add_u32 s80, s80, 131072\n\t"
      "s_addc_u32 s81, s81, 0\n\t"
      "s_nop 0\n\t"
      "s_and_b32 s81, s81, 0xffff\n\t"
      "s_mov_b32 s82, 16384\n\t"
      "s_mov_b32 s83, 0x20000\n\t"
      "s_lshl_b32 s68, s57, 8\n\t"
      "s_nop 0\n\t"
      "s_sub_u32 s69, 0x400000, s68\n\t"
      "s_mov_b64 s[40:41], %2\n\t"
      "s_add_u32 s40, s40, s68\n\t"
      "s_addc_u32 s41, s41, 0\n\t"
      "s_nop 0\n\t"
      "s_and_b32 s41, s41, 0xffff\n\t"
      "s_mov_b32 s42, s69\n\t"
      "s_nop 0\n\t"
      "s_mov_b32 s43, 0x20000\n\t"
      "s_lshl_b32 s68, s58, 2\n\t"
      "s_add_u32 s70, s68, s56\n\t"
      "s_lshl_b32 s68, s70, 16\n\t"
      "s_mov_b64 s[44:45], %1\n\t"
      "s_add_u32 s44, s44, 1048576\n\t"
      "s_addc_u32 s45, s45, 0\n\t"
      "s_add_u32 s44, s44, s68\n\t"
      "s_addc_u32 s45, s45, 0\n\t"
      "s_nop 0\n\t"
      "s_and_b32 s45, s45, 0xffff\n\t"
      "s_mov_b32 s46, 65536\n\t"
      "s_mov_b32 s47, 0x20000\n\t"
      "s_lshl_b32 s59, s56, 12\n\t"
      "s_nop 0\n\t"
      "s_add_u32 s60, s59, 0x10000\n\t"
      "s_mov_b32 s72, 0\n\t"
      "s_nop 0\n\t"
      "s_mov_b32 s64, 0xffff0000\n\t"
      "v_lshlrev_b32_e64 v246, 4, v241\n\t"
      "v_add_u32_e32 v247, 0x4000, v246\n\t"
      "v_add_u32_e32 v248, 0x8000, v246\n\t"
      "v_add_u32_e32 v249, 0xc000, v246\n\t"
      "v_bfe_u32 v218, v241, 1, 3\n\t"
      "v_lshrrev_b32_e32 v219, 4, v241\n\t"
      "v_and_b32_e32 v220, 1, v241\n\t"
      "v_lshlrev_b32_e64 v221, 5, v219\n\t"
      "v_lshl_add_u32 v221, v220, 4, v221\n\t"
      "v_lshl_add_u32 v244, v218, 13, v221\n\t"
      "s_lshl_b32 s68, s56, 17\n\t"
      "v_add_u32_e32 v244, s68, v244\n\t"
      "v_add_u32_e32 v245, 0x80, v244\n\t"
      "v_lshrrev_b32_e32 v218, 5, v241\n\t"
      "v_lshlrev_b32_e32 v218, 11, v218\n\t"
      "v_bfe_u32 v220, v241, 2, 3\n\t"
      "v_lshl_add_u32 v218, v220, 5, v218\n\t"
      "v_and_b32_e64 v220, 1, v241\n\t"
      "v_lshl_add_u32 v242, v220, 4, v218\n\t"
      "v_bfe_u32 v220, v241, 1, 1\n\t"
      "v_lshl_add_u32 v242, v220, 8, v242\n\t"
      "v_add_u32_e32 v243, 0x10000, v242\n\t"
      "v_lshrrev_b32_e32 v218, 4, v241\n\t"
      "v_lshlrev_b32_e32 v250, 6, v218\n\t"
      "v_add_u32_e32 v250, 0x20000, v250\n\t"
      "s_mov_b64 s[48:49], %1\n\t"
      "s_nop 0\n\t"
      "s_add_u32 s48, s48, 16640\n\t"
      "s_addc_u32 s49, s49, 0\n\t"
      "s_nop 0\n\t"
      "s_and_b32 s49, s49, 0xffff\n\t"
      "s_mov_b32 s50, 1024\n\t"
      "s_mov_b32 s51, 0x20000\n\t"
      "v_mov_b32_e32 v218, 0\n\t"
      "v_mov_b32_e32 v219, %10\n\t"
      "s_lshl_b32 s68, %6, 2\n\t"
      "s_waitcnt vmcnt(0)\n\t"
      "s_barrier\n\t"
      "s_cmp_lg_u32 s56, 1\n\t"
      "s_cbranch_scc1 L_NOFLAG\n\t"
      "s_mov_b64 exec, 1\n\t"
      "buffer_store_dword v219, v218, s[48:51], s68 offen sc1\n\t"
      "s_mov_b64 exec, -1\n\t"
      "L_NOFLAG:\n\t"
      "s_nop 1\n\t"
      "s_add_u32 m0, s59, 0\n\t"
      "s_mov_b32 s73, 0\n\t"
      "buffer_load_dwordx4 v244, s[40:43], s73 offen lds\n\t"
      "s_add_u32 m0, s59, 1024\n\t"
      "s_nop 0\n\t"
      "s_nop 0\n\t"
      "buffer_load_dwordx4 v245, s[40:43], s73 offen lds\n\t"
      "s_add_u32 m0, s59, 2048\n\t"
      "s_mov_b32 s73, 65536\n\t"
      "buffer_load_dwordx4 v244, s[40:43], s73 offen lds\n\t"
      "s_add_u32 m0, s59, 3072\n\t"
      "s_nop 0\n\t"
      "s_nop 0\n\t"
      "buffer_load_dwordx4 v245, s[40:43], s73 offen lds\n\t"
      "s_add_u32 m0, s59, 65536\n\t"
      "s_mov_b32 s73, 524288\n\t"
      "buffer_load_dwordx4 v244, s[40:43], s73 offen lds\n\t"
      "s_add_u32 m0, s59, 66560\n\t"
      "s_nop 0\n\t"
      "s_nop 0\n\t"
      "buffer_load_dwordx4 v245, s[40:43], s73 offen lds\n\t"
      "s_add_u32 m0, s59, 67584\n\t"
      "s_mov_b32 s73, 589824\n\t"
      "buffer_load_dwordx4 v244, s[40:43], s73 offen lds\n\t"
      "s_add_u32 m0, s59, 68608\n\t"
      "s_nop 0\n\t"
      "s_nop 0\n\t"
      "buffer_load_dwordx4 v245, s[40:43], s73 offen lds\n\t"
      "s_lshl_b32 s68, s57, 19\n\t"
      "s_mov_b64 s[76:77], %3\n\t"
      "s_add_u32 s76, s76, s68\n\t"
      "s_addc_u32 s77, s77, 0\n\t"
      "s_and_b32 s77, s77, 0xffff\n\t"
      "s_mov_b32 s78, 524288\n\t"
      "s_mov_b32 s79, 0x20000\n\t"
      "s_mov_b64 s[36:37], %4\n\t"
      "s_add_u32 s36, s36, s68\n\t"
      "s_addc_u32 s37, s37, 0\n\t"
      "s_nop 0\n\t"
      "s_sub_u32 s36, s36, 1024\n\t"
      "s_subb_u32 s37, s37, 0\n\t"
      "s_nop 0\n\t"
      "s_and_b32 s37, s37, 0xffff\n\t"
      "s_mov_b32 s38, 525312\n\t"
      "s_mov_b32 s39, 0x20000\n\t"
      "s_mov_b32 s65, 0x4e800000\n\t"
      "s_mov_b32 s66, 0x3e0293ee\n\t"
      "s_mov_b32 s63, 0\n\t"
      "s_mov_b32 s61, 0\n\t"
      "s_mov_b32 s62, 0\n\t"
      "s_nop 0\n\t"
      "v_bfe_u32 v218, v241, 1, 3\n\t"
      "v_lshrrev_b32_e32 v219, 4, v241\n\t"
      "v_and_b32_e32 v220, 1, v241\n\t"
      "v_lshlrev_b32_e64 v221, 5, v219\n\t"
      "v_lshl_add_u32 v221, v220, 4, v221\n\t"
      "v_lshl_add_u32 v239, v218, 8, v221\n\t"
      "v_add_u32_e64 v239, s59, v239\n\t"
      "v_add_u32_e32 v240, 0x80, v239\n\t"
      "v_add_u32_e32 v239, 0x400, v239\n\t"
      "v_and_b32_e32 v220, 15, v241\n\t"
      "v_add_u32_e32 v221, 0, v219\n\t"
      "v_xor_b32_e32 v221, v220, v221\n\t"
      "v_lshlrev_b32_e32 v221, 4, v221\n\t"
      "v_lshl_add_u32 v235, v219, 8, v221\n\t"
      "v_add_u32_e32 v235, s59, v235\n\t"
      "v_add_u32_e32 v221, 4, v219\n\t"
      "v_xor_b32_e32 v221, v220, v221\n\t"
      "v_lshlrev_b32_e32 v221, 4, v221\n\t"
      "v_lshl_add_u32 v236, v219, 8, v221\n\t"
      "v_add_u32_e32 v236, s59, v236\n\t"
      "v_add_u32_e32 v221, 8, v219\n\t"
      "v_xor_b32_e32 v221, v220, v221\n\t"
      "v_lshlrev_b32_e32 v221, 4, v221\n\t"
      "v_lshl_add_u32 v237, v219, 8, v221\n\t"
      "v_add_u32_e32 v237, s59, v237\n\t"
      "v_add_u32_e32 v221, 12, v219\n\t"
      "v_xor_b32_e32 v221, v220, v221\n\t"
      "v_lshlrev_b32_e32 v221, 4, v221\n\t"
      "v_lshl_add_u32 v238, v219, 8, v221\n\t"
      "v_add_u32_e32 v238, s59, v238\n\t"
      "v_add_u32_e32 v221, 0, v219\n\t"
      "v_xor_b32_e32 v221, v220, v221\n\t"
      "v_lshlrev_b32_e32 v221, 4, v221\n\t"
      "v_lshl_add_u32 v230, v220, 8, v221\n\t"
      "v_add_u32_e32 v221, 4, v219\n\t"
      "v_xor_b32_e32 v221, v220, v221\n\t"
      "v_lshlrev_b32_e64 v221, 4, v221\n\t"
      "v_lshl_add_u32 v231, v220, 8, v221\n\t"
      "v_add_u32_e32 v221, 8, v219\n\t"
      "v_xor_b32_e32 v221, v220, v221\n\t"
      "v_lshlrev_b32_e64 v221, 4, v221\n\t"
      "v_lshl_add_u32 v232, v220, 8, v221\n\t"
      "v_add_u32_e32 v221, 12, v219\n\t"
      "v_xor_b32_e32 v221, v220, v221\n\t"
      "v_lshlrev_b32_e64 v221, 4, v221\n\t"
      "v_lshl_add_u32 v233, v220, 8, v221\n\t"
      "v_lshrrev_b32_e32 v218, 5, v241\n\t"
      "v_lshlrev_b32_e32 v218, 11, v218\n\t"
      "v_bfe_u32 v220, v241, 2, 3\n\t"
      "v_lshl_add_u32 v218, v220, 5, v218\n\t"
      "v_and_b32_e64 v220, 3, v241\n\t"
      "v_lshl_add_u32 v234, v220, 3, v218\n\t"
      "v_add_u32_e32 v234, 0x10000, v234\n\t"
      "s_add_u32 m0, s59, 16384\n\t"
      "s_mov_b32 s73, 1048576\n\t"
      "buffer_load_dwordx4 v244, s[40:43], s73 offen lds\n\t"
      "s_add_u32 m0, s59, 17408\n\t"
      "s_nop 0\n\t"
      "s_nop 0\n\t"
      "buffer_load_dwordx4 v245, s[40:43], s73 offen lds\n\t"
      "s_add_u32 m0, s59, 18432\n\t"
      "s_mov_b32 s73, 1114112\n\t"
      "buffer_load_dwordx4 v244, s[40:43], s73 offen lds\n\t"
      "s_add_u32 m0, s59, 19456\n\t"
      "s_nop 0\n\t"
      "s_nop 0\n\t"
      "buffer_load_dwordx4 v245, s[40:43], s73 offen lds\n\t"
      "s_add_u32 m0, s59, 81920\n\t"
      "s_mov_b32 s73, 1572864\n\t"
      "buffer_load_dwordx4 v244, s[40:43], s73 offen lds\n\t"
      "s_add_u32 m0, s59, 82944\n\t"
      "s_nop 0\n\t"
      "s_nop 0\n\t"
      "buffer_load_dwordx4 v245, s[40:43], s73 offen lds\n\t"
      "s_add_u32 m0, s59, 83968\n\t"
      "s_mov_b32 s73, 1638400\n\t"
      "buffer_load_dwordx4 v244, s[40:43], s73 offen lds\n\t"
      "s_add_u32 m0, s59, 84992\n\t"
      "s_nop 0\n\t"
      "s_nop 0\n\t"
      "buffer_load_dwordx4 v245, s[40:43], s73 offen lds\n\t"
      "s_add_u32 m0, s59, 32768\n\t"
      "s_mov_b32 s73, 2097152\n\t"
      "buffer_load_dwordx4 v244, s[40:43], s73 offen lds\n\t"
      "s_add_u32 m0, s59, 33792\n\t"
      "s_nop 0\n\t"
      "s_nop 0\n\t"
      "buffer_load_dwordx4 v245, s[40:43], s73 offen lds\n\t"
      "s_add_u32 m0, s59, 34816\n\t"
      "s_mov_b32 s73, 2162688\n\t"
      "buffer_load_dwordx4 v244, s[40:43], s73 offen lds\n\t"
      "s_add_u32 m0, s59, 35840\n\t"
      "s_nop 0\n\t"
      "s_nop 0\n\t"
      "buffer_load_dwordx4 v245, s[40:43], s73 offen lds\n\t"
      "s_add_u32 m0, s59, 98304\n\t"
      "s_mov_b32 s73, 2621440\n\t"
      "buffer_load_dwordx4 v244, s[40:43], s73 offen lds\n\t"
      "s_add_u32 m0, s59, 99328\n\t"
      "s_nop 0\n\t"
      "s_nop 0\n\t"
      "buffer_load_dwordx4 v245, s[40:43], s73 offen lds\n\t"
      "s_add_u32 m0, s59, 100352\n\t"
      "s_mov_b32 s73, 2686976\n\t"
      "buffer_load_dwordx4 v244, s[40:43], s73 offen lds\n\t"
      "s_add_u32 m0, s59, 101376\n\t"
      "s_nop 0\n\t"
      "s_nop 0\n\t"
      "buffer_load_dwordx4 v245, s[40:43], s73 offen lds\n\t"
      "v_add_u32_e32 v252, 0x21000, v246\n\t"
      "v_mov_b32_e32 v162, 0\n\t"
      "v_mov_b32_e32 v163, 0\n\t"
      "v_mov_b32_e32 v178, 0\n\t"
      "v_mov_b32_e32 v179, 0\n\t"
      "v_accvgpr_write_b32 a0, 0\n\t"
      "v_accvgpr_write_b32 a1, 0\n\t"
      "v_accvgpr_write_b32 a2, 0\n\t"
      "v_accvgpr_write_b32 a3, 0\n\t"
      "v_accvgpr_write_b32 a4, 0\n\t"
      "v_accvgpr_write_b32 a5, 0\n\t"
      "v_accvgpr_write_b32 a6, 0\n\t"
      "v_accvgpr_write_b32 a7, 0\n\t"
      "v_accvgpr_write_b32 a8, 0\n\t"
      "v_accvgpr_write_b32 a9, 0\n\t"
      "v_accvgpr_write_b32 a10, 0\n\t"
      "v_accvgpr_write_b32 a11, 0\n\t"
      "v_accvgpr_write_b32 a12, 0\n\t"
      "v_accvgpr_write_b32 a13, 0\n\t"
      "v_accvgpr_write_b32 a14, 0\n\t"
      "v_accvgpr_write_b32 a15, 0\n\t"
      "v_accvgpr_write_b32 a16, 0\n\t"
      "v_accvgpr_write_b32 a17, 0\n\t"
      "v_accvgpr_write_b32 a18, 0\n\t"
      "v_accvgpr_write_b32 a19, 0\n\t"
      "v_accvgpr_write_b32 a20, 0\n\t"
      "v_accvgpr_write_b32 a21, 0\n\t"
      "v_accvgpr_write_b32 a22, 0\n\t"
      "v_accvgpr_write_b32 a23, 0\n\t"
      "v_accvgpr_write_b32 a24, 0\n\t"
      "v_accvgpr_write_b32 a25, 0\n\t"
      "v_accvgpr_write_b32 a26, 0\n\t"
      "v_accvgpr_write_b32 a27, 0\n\t"
      "v_accvgpr_write_b32 a28, 0\n\t"
      "v_accvgpr_write_b32 a29, 0\n\t"
      "v_accvgpr_write_b32 a30, 0\n\t"
      "v_accvgpr_write_b32 a31, 0\n\t"
      "v_accvgpr_write_b32 a32, 0\n\t"
      "v_accvgpr_write_b32 a33, 0\n\t"
      "v_accvgpr_write_b32 a34, 0\n\t"
      "v_accvgpr_write_b32 a35, 0\n\t"
      "v_accvgpr_write_b32 a36, 0\n\t"
      "v_accvgpr_write_b32 a37, 0\n\t"
      "v_accvgpr_write_b32 a38, 0\n\t"
      "v_accvgpr_write_b32 a39, 0\n\t"
      "v_accvgpr_write_b32 a40, 0\n\t"
      "v_accvgpr_write_b32 a41, 0\n\t"
      "v_accvgpr_write_b32 a42, 0\n\t"
      "v_accvgpr_write_b32 a43, 0\n\t"
      "v_accvgpr_write_b32 a44, 0\n\t"
      "v_accvgpr_write_b32 a45, 0\n\t"
      "v_accvgpr_write_b32 a46, 0\n\t"
      "v_accvgpr_write_b32 a47, 0\n\t"
      "v_accvgpr_write_b32 a48, 0\n\t"
      "v_accvgpr_write_b32 a49, 0\n\t"
      "v_accvgpr_write_b32 a50, 0\n\t"
      "v_accvgpr_write_b32 a51, 0\n\t"
      "v_accvgpr_write_b32 a52, 0\n\t"
      "v_accvgpr_write_b32 a53, 0\n\t"
      "v_accvgpr_write_b32 a54, 0\n\t"
      "v_accvgpr_write_b32 a55, 0\n\t"
      "v_accvgpr_write_b32 a56, 0\n\t"
      "v_accvgpr_write_b32 a57, 0\n\t"
      "v_accvgpr_write_b32 a58, 0\n\t"
      "v_accvgpr_write_b32 a59, 0\n\t"
      "v_accvgpr_write_b32 a60, 0\n\t"
      "v_accvgpr_write_b32 a61, 0\n\t"
      "v_accvgpr_write_b32 a62, 0\n\t"
      "v_accvgpr_write_b32 a63, 0\n\t"
      "v_accvgpr_write_b32 a64, 0\n\t"
      "v_accvgpr_write_b32 a65, 0\n\t"
      "v_accvgpr_write_b32 a66, 0\n\t"
      "v_accvgpr_write_b32 a67, 0\n\t"
      "v_accvgpr_write_b32 a68, 0\n\t"
      "v_accvgpr_write_b32 a69, 0\n\t"
      "v_accvgpr_write_b32 a70, 0\n\t"
      "v_accvgpr_write_b32 a71, 0\n\t"
      "v_accvgpr_write_b32 a72, 0\n\t"
      "v_accvgpr_write_b32 a73, 0\n\t"
      "v_accvgpr_write_b32 a74, 0\n\t"
      "v_accvgpr_write_b32 a75, 0\n\t"
      "v_accvgpr_write_b32 a76, 0\n\t"
      "v_accvgpr_write_b32 a77, 0\n\t"
      "v_accvgpr_write_b32 a78, 0\n\t"
      "v_accvgpr_write_b32 a79, 0\n\t"
      "v_accvgpr_write_b32 a80, 0\n\t"
      "v_accvgpr_write_b32 a81, 0\n\t"
      "v_accvgpr_write_b32 a82, 0\n\t"
      "v_accvgpr_write_b32 a83, 0\n\t"
      "v_accvgpr_write_b32 a84, 0\n\t"
      "v_accvgpr_write_b32 a85, 0\n\t"
      "v_accvgpr_write_b32 a86, 0\n\t"
      "v_accvgpr_write_b32 a87, 0\n\t"
      "v_accvgpr_write_b32 a88, 0\n\t"
      "v_accvgpr_write_b32 a89, 0\n\t"
      "v_accvgpr_write_b32 a90, 0\n\t"
      "v_accvgpr_write_b32 a91, 0\n\t"
      "v_accvgpr_write_b32 a92, 0\n\t"
      "v_accvgpr_write_b32 a93, 0\n\t"
      "v_accvgpr_write_b32 a94, 0\n\t"
      "v_accvgpr_write_b32 a95, 0\n\t"
      "v_accvgpr_write_b32 a96, 0\n\t"
      "v_accvgpr_write_b32 a97, 0\n\t"
      "v_accvgpr_write_b32 a98, 0\n\t"
      "v_accvgpr_write_b32 a99, 0\n\t"
      "v_accvgpr_write_b32 a100, 0\n\t"
      "v_accvgpr_write_b32 a101, 0\n\t"
      "v_accvgpr_write_b32 a102, 0\n\t"
      "v_accvgpr_write_b32 a103, 0\n\t"
      "v_accvgpr_write_b32 a104, 0\n\t"
      "v_accvgpr_write_b32 a105, 0\n\t"
      "v_accvgpr_write_b32 a106, 0\n\t"
      "v_accvgpr_write_b32 a107, 0\n\t"
      "v_accvgpr_write_b32 a108, 0\n\t"
      "v_accvgpr_write_b32 a109, 0\n\t"
      "v_accvgpr_write_b32 a110, 0\n\t"
      "v_accvgpr_write_b32 a111, 0\n\t"
      "v_accvgpr_write_b32 a112, 0\n\t"
      "v_accvgpr_write_b32 a113, 0\n\t"
      "v_accvgpr_write_b32 a114, 0\n\t"
      "v_accvgpr_write_b32 a115, 0\n\t"
      "v_accvgpr_write_b32 a116, 0\n\t"
      "v_accvgpr_write_b32 a117, 0\n\t"
      "v_accvgpr_write_b32 a118, 0\n\t"
      "v_accvgpr_write_b32 a119, 0\n\t"
      "v_accvgpr_write_b32 a120, 0\n\t"
      "v_accvgpr_write_b32 a121, 0\n\t"
      "v_accvgpr_write_b32 a122, 0\n\t"
      "v_accvgpr_write_b32 a123, 0\n\t"
      "v_accvgpr_write_b32 a124, 0\n\t"
      "v_accvgpr_write_b32 a125, 0\n\t"
      "v_accvgpr_write_b32 a126, 0\n\t"
      "v_accvgpr_write_b32 a127, 0\n\t"
      "s_cmp_lg_u32 s56, 0\n\t"
      "s_cbranch_scc1 L_POLLEDS\n\t"
      "s_mov_b32 s75, 0\n\t"
      "L_POLLS:\n\t"
      "s_nop 0\n\t"
      "buffer_load_dwordx4 v[222:225], v246, s[48:51], 0 offen sc1\n\t"
      "s_add_u32 s75, s75, 1\n\t"
      "s_waitcnt vmcnt(0)\n\t"
      "v_xor_b32_e32 v222, %10, v222\n\t"
      "v_xor_b32_e32 v223, %10, v223\n\t"
      "v_or_b32_e32 v222, v222, v223\n\t"
      "v_xor_b32_e32 v224, %10, v224\n\t"
      "v_or_b32_e32 v222, v222, v224\n\t"
      "v_xor_b32_e32 v225, %10, v225\n\t"
      "v_or_b32_e32 v222, v222, v225\n\t"
      "v_cmp_ne_u32_e32 vcc, 0, v222\n\t"
      "s_cmp_ge_u32 s75, 0x10000\n\t"
      "s_cbranch_scc1 L_POLLEDS\n\t"
      "s_cbranch_vccnz L_POLLS\n\t"
      "L_POLLEDS:\n\t"
      "s_barrier\n\t"
      "s_lshl_b32 s68, s56, 10\n\t"
      "buffer_load_dwordx4 v[0:3], v246, s[52:55], s68 offen sc1\n\t"
      "buffer_load_dwordx4 v[4:7], v246, s[80:83], s59 offen offset:0 sc1\n\t"
      "buffer_load_dwordx4 v[8:11], v246, s[80:83], s59 offen offset:1024 sc1\n\t"
      "buffer_load_dwordx4 v[12:15], v246, s[80:83], s59 offen offset:2048 sc1\n\t"
      "buffer_load_dwordx4 v[16:19], v246, s[80:83], s59 offen offset:3072 sc1\n\t"
      "buffer_load_dwordx4 v[64:67], v246, s[44:47], s72 offen offset:0 sc1\n\t"
      "buffer_load_dwordx4 v[72:75], v247, s[44:47], s72 offen offset:0 sc1\n\t"
      "buffer_load_dwordx4 v[80:83], v248, s[44:47], s72 offen offset:0 sc1\n\t"
      "buffer_load_dwordx4 v[88:91], v249, s[44:47], s72 offen offset:0 sc1\n\t"
      "buffer_load_dwordx4 v[68:71], v246, s[44:47], s72 offen offset:1024 sc1\n\t"
      "buffer_load_dwordx4 v[76:79], v247, s[44:47], s72 offen offset:1024 sc1\n\t"
      "buffer_load_dwordx4 v[84:87], v248, s[44:47], s72 offen offset:1024 sc1\n\t"
      "buffer_load_dwordx4 v[92:95], v249, s[44:47], s72 offen offset:1024 sc1\n\t"
      "s_add_u32 s72, s72, 0x800\n\t"
      "buffer_load_dwordx4 v[96:99], v246, s[44:47], s72 offen offset:0 sc1\n\t"
      "buffer_load_dwordx4 v[104:107], v247, s[44:47], s72 offen offset:0 sc1\n\t"
      "buffer_load_dwordx4 v[112:115], v248, s[44:47], s72 offen offset:0 sc1\n\t"
      "buffer_load_dwordx4 v[120:123], v249, s[44:47], s72 offen offset:0 sc1\n\t"
      "buffer_load_dwordx4 v[100:103], v246, s[44:47], s72 offen offset:1024 sc1\n\t"
      "buffer_load_dwordx4 v[108:111], v247, s[44:47], s72 offen offset:1024 sc1\n\t"
      "buffer_load_dwordx4 v[116:119], v248, s[44:47], s72 offen offset:1024 sc1\n\t"
      "buffer_load_dwordx4 v[124:127], v249, s[44:47], s72 offen offset:1024 sc1\n\t"
      "s_add_u32 s72, s72, 0x800\n\t"
      "s_lshl_b32 s70, s58, 2\n\t"
      "s_add_u32 s70, s70, s56\n\t"
      "s_lshl_b32 s70, s70, 19\n\t"
      "s_lshl_b32 s69, s57, 12\n\t"
      "s_add_u32 s70, s70, s69\n\t"
      "s_nop 0\n\t"
      "s_sub_u32 s67, 0x80000, s69\n\t"
      "s_mov_b64 s[48:49], %1\n\t"
      "s_nop 0\n\t"
      "s_add_u32 s48, s48, 33554432\n\t"
      "s_addc_u32 s49, s49, 0\n\t"
      "s_add_u32 s48, s48, s70\n\t"
      "s_addc_u32 s49, s49, 0\n\t"
      "s_nop 0\n\t"
      "s_and_b32 s49, s49, 0xffff\n\t"
      "s_mov_b32 s50, s67\n\t"
      "s_nop 0\n\t"
      "s_mov_b32 s51, 0x20000\n\t"
      "v_add_u32_e64 v218, s68, v246\n\t"
      "v_add_u32_e32 v218, 0x20000, v218\n\t"
      "v_add_u32_e32 v219, s59, v252\n\t"
      "s_waitcnt vmcnt(16)\n\t"
      "ds_write_b128 v218, v[0:3]\n\t"
      "ds_write_b128 v219, v[4:7] offset:0\n\t"
      "ds_write_b128 v219, v[8:11] offset:1024\n\t"
      "ds_write_b128 v219, v[12:15] offset:2048\n\t"
      "ds_write_b128 v219, v[16:19] offset:3072\n\t"
      "s_waitcnt lgkmcnt(0)\n\t"
      "s_barrier\n\t"
      "ds_read_b64 v[160:161], v252 offset:0\n\t"
      "ds_read_b64 v[164:165], v252 offset:8\n\t"
      "ds_read_b128 v[168:171], v250 offset:32\n\t"
      "ds_read_b128 v[172:175], v250 offset:48\n\t"
      "ds_read_b64 v[176:177], v252 offset:1024\n\t"
      "ds_read_b64 v[180:181], v252 offset:1032\n\t"
      "ds_read_b128 v[184:187], v250 offset:288\n\t"
      "ds_read_b128 v[188:191], v250 offset:304\n\t"
      "ds_read_b64_tr_b16 v[0:1], v242 offset:0\n\t"
      "ds_read_b64_tr_b16 v[2:3], v242 offset:4096\n\t"
      "ds_read_b64_tr_b16 v[4:5], v242 offset:8\n\t"
      "ds_read_b64_tr_b16 v[6:7], v242 offset:4104\n\t"
      "ds_read_b64_tr_b16 v[8:9], v242 offset:512\n\t"
      "ds_read_b64_tr_b16 v[10:11], v242 offset:4608\n\t"
      "s_waitcnt vmcnt(12) lgkmcnt(6)\n\t"
      "s_nop 0\n\t"
      "v_mfma_f32_16x16x32_bf16 v[192:195], v[160:163], v[64:67], v[168:171]\n\t"
      "v_mfma_f32_16x16x32_bf16 v[196:199], v[162:165], v[64:67], v[172:175]\n\t"
      "buffer_load_dwordx4 v[128:131], v246, s[44:47], s72 offen offset:0 sc1\n\t"
      "v_mfma_f32_16x16x32_bf16 v[200:203], v[160:163], v[72:75], v[168:171]\n\t"
      "v_mfma_f32_16x16x32_bf16 v[204:207], v[162:165], v[72:75], v[172:175]\n\t"
      "buffer_load_dwordx4 v[136:139], v247, s[44:47], s72 offen offset:0 sc1\n\t"
      "v_mfma_f32_16x16x32_bf16 v[208:211], v[160:163], v[80:83], v[168:171]\n\t"
      "v_mfma_f32_16x16x32_bf16 v[212:215], v[162:165], v[80:83], v[172:175]\n\t"
      "buffer_load_dwordx4 v[144:147], v248, s[44:47], s72 offen offset:0 sc1\n\t"
      "v_mfma_f32_16x16x32_bf16 v[216:219], v[160:163], v[88:91], v[168:171]\n\t"
      "v_mfma_f32_16x16x32_bf16 v[220:223], v[162:165], v[88:91], v[172:175]\n\t"
      "buffer_load_dwordx4 v[152:155], v249, s[44:47], s72 offen offset:0 sc1\n\t"
      "ds_read_b64_tr_b16 v[12:13], v242 offset:520\n\t"
      "ds_read_b64_tr_b16 v[14:15], v242 offset:4616\n\t"
      "ds_read_b64_tr_b16 v[16:17], v242 offset:1024\n\t"
      "ds_read_b64_tr_b16 v[18:19], v242 offset:5120\n\t"
      "ds_read_b64_tr_b16 v[20:21], v242 offset:1032\n\t"
      "ds_read_b64_tr_b16 v[22:23], v242 offset:5128\n\t"
      "ds_read_b64_tr_b16 v[24:25], v242 offset:1536\n\t"
      "ds_read_b64_tr_b16 v[26:27], v242 offset:5632\n\t"
      "ds_read_b64_tr_b16 v[28:29], v242 offset:1544\n\t"
      "ds_read_b64_tr_b16 v[30:31], v242 offset:5640\n\t"
      "v_cvt_pk_bf16_f32 v64, v192, v193\n\t"
      "v_cvt_pk_bf16_f32 v65, v194, v195\n\t"
      "v_cvt_pk_bf16_f32 v66, v196, v197\n\t"
      "v_cvt_pk_bf16_f32 v67, v198, v199\n\t"
      "v_cvt_pk_bf16_f32 v72, v200, v201\n\t"
      "v_cvt_pk_bf16_f32 v73, v202, v203\n\t"
      "v_cvt_pk_bf16_f32 v74, v204, v205\n\t"
      "v_cvt_pk_bf16_f32 v75, v206, v207\n\t"
      "v_cvt_pk_bf16_f32 v80, v208, v209\n\t"
      "v_cvt_pk_bf16_f32 v81, v210, v211\n\t"
      "v_cvt_pk_bf16_f32 v82, v212, v213\n\t"
      "v_cvt_pk_bf16_f32 v83, v214, v215\n\t"
      "v_cvt_pk_bf16_f32 v88, v216, v217\n\t"
      "v_cvt_pk_bf16_f32 v89, v218, v219\n\t"
      "v_cvt_pk_bf16_f32 v90, v220, v221\n\t"
      "v_cvt_pk_bf16_f32 v91, v222, v223\n\t"
      "ds_read_b64_tr_b16 v[32:33], v242 offset:8192\n\t"
      "ds_read_b64_tr_b16 v[34:35], v242 offset:12288\n\t"
      "ds_read_b64_tr_b16 v[36:37], v242 offset:8200\n\t"
      "ds_read_b64_tr_b16 v[38:39], v242 offset:12296\n\t"
      "ds_read_b64_tr_b16 v[40:41], v242 offset:8704\n\t"
      "ds_read_b64_tr_b16 v[42:43], v242 offset:12800\n\t"
      "s_waitcnt vmcnt(12) lgkmcnt(15)\n\t"
      "s_nop 0\n\t"
      "v_mfma_f32_16x16x32_bf16 v[192:195], v[176:179], v[68:71], v[184:187]\n\t"
      "v_mfma_f32_16x16x32_bf16 v[196:199], v[178:181], v[68:71], v[188:191]\n\t"
      "buffer_load_dwordx4 v[132:135], v246, s[44:47], s72 offen offset:1024 sc1\n\t"
      "v_mfma_f32_16x16x32_bf16 v[200:203], v[176:179], v[76:79], v[184:187]\n\t"
      "v_mfma_f32_16x16x32_bf16 v[204:207], v[178:181], v[76:79], v[188:191]\n\t"
      "buffer_load_dwordx4 v[140:143], v247, s[44:47], s72 offen offset:1024 sc1\n\t"
      "v_mfma_f32_16x16x32_bf16 v[208:211], v[176:179], v[84:87], v[184:187]\n\t"
      "v_mfma_f32_16x16x32_bf16 v[212:215], v[178:181], v[84:87], v[188:191]\n\t"
      "buffer_load_dwordx4 v[148:151], v248, s[44:47], s72 offen offset:1024 sc1\n\t"
      "v_mfma_f32_16x16x32_bf16 v[216:219], v[176:179], v[92:95], v[184:187]\n\t"
      "v_mfma_f32_16x16x32_bf16 v[220:223], v[178:181], v[92:95], v[188:191]\n\t"
      "buffer_load_dwordx4 v[156:159], v249, s[44:47], s72 offen offset:1024 sc1\n\t"
      "ds_read_b64_tr_b16 v[44:45], v242 offset:8712\n\t"
      "ds_read_b64_tr_b16 v[46:47], v242 offset:12808\n\t"
      "ds_read_b64_tr_b16 v[48:49], v242 offset:9216\n\t"
      "ds_read_b64_tr_b16 v[50:51], v242 offset:13312\n\t"
      "ds_read_b64_tr_b16 v[52:53], v242 offset:9224\n\t"
      "ds_read_b64_tr_b16 v[54:55], v242 offset:13320\n\t"
      "ds_read_b64_tr_b16 v[56:57], v242 offset:9728\n\t"
      "ds_read_b64_tr_b16 v[58:59], v242 offset:13824\n\t"
      "ds_read_b64_tr_b16 v[60:61], v242 offset:9736\n\t"
      "ds_read_b64_tr_b16 v[62:63], v242 offset:13832\n\t"
      "v_cvt_pk_bf16_f32 v68, v192, v193\n\t"
      "v_cvt_pk_bf16_f32 v69, v194, v195\n\t"
      "v_cvt_pk_bf16_f32 v70, v196, v197\n\t"
      "v_cvt_pk_bf16_f32 v71, v198, v199\n\t"
      "v_cvt_pk_bf16_f32 v76, v200, v201\n\t"
      "v_cvt_pk_bf16_f32 v77, v202, v203\n\t"
      "v_cvt_pk_bf16_f32 v78, v204, v205\n\t"
      "v_cvt_pk_bf16_f32 v79, v206, v207\n\t"
      "v_cvt_pk_bf16_f32 v84, v208, v209\n\t"
      "v_cvt_pk_bf16_f32 v85, v210, v211\n\t"
      "v_cvt_pk_bf16_f32 v86, v212, v213\n\t"
      "v_cvt_pk_bf16_f32 v87, v214, v215\n\t"
      "v_cvt_pk_bf16_f32 v92, v216, v217\n\t"
      "v_cvt_pk_bf16_f32 v93, v218, v219\n\t"
      "v_cvt_pk_bf16_f32 v94, v220, v221\n\t"
      "v_cvt_pk_bf16_f32 v95, v222, v223\n\t"
      "s_add_u32 s72, s72, 0x800\n\t"
      "s_waitcnt lgkmcnt(0)\n\t"
      "s_nop 0\n\t"
      "v_mfma_f32_16x16x32_bf16 a[128:131], v[0:3], v[64:67], 0\n\t"
      "v_mfma_f32_16x16x32_bf16 a[132:135], v[0:3], v[72:75], 0\n\t"
      "v_mfma_f32_16x16x32_bf16 a[136:139], v[0:3], v[80:83], 0\n\t"
      "ds_read_b64 v[160:161], v252 offset:2048\n\t"
      "v_mfma_f32_16x16x32_bf16 a[140:143], v[0:3], v[88:91], 0\n\t"
      "s_barrier\n\t"
      "s_nop 0\n\t"
      "ds_read_b64 v[164:165], v252 offset:2056\n\t"
      "v_mfma_f32_16x16x32_bf16 a[144:147], v[4:7], v[64:67], 0\n\t"
      "ds_read_b64_tr_b16 v[0:1], v243 offset:0\n\t"
      "v_mfma_f32_16x16x32_bf16 a[148:151], v[4:7], v[72:75], 0\n\t"
      "s_add_u32 m0, s59, 49152\n\t"
      "ds_read_b128 v[168:171], v250 offset:544\n\t"
      "v_mfma_f32_16x16x32_bf16 a[152:155], v[4:7], v[80:83], 0\n\t"
      "s_mov_b32 s73, 3145728\n\t"
      "ds_read_b64_tr_b16 v[2:3], v243 offset:4096\n\t"
      "v_mfma_f32_16x16x32_bf16 a[156:159], v[4:7], v[88:91], 0\n\t"
      "ds_read_b128 v[172:175], v250 offset:560\n\t"
      "v_mfma_f32_16x16x32_bf16 a[160:163], v[8:11], v[64:67], 0\n\t"
      "ds_read_b64_tr_b16 v[4:5], v243 offset:8\n\t"
      "s_waitcnt vmcnt(12) lgkmcnt(1)\n\t"
      "s_nop 0\n\t"
      "v_mfma_f32_16x16x32_bf16 v[192:195], v[160:163], v[96:99], v[168:171]\n\t"
      "v_mfma_f32_16x16x32_bf16 a[164:167], v[8:11], v[72:75], 0\n\t"
      "ds_read_b64 v[176:177], v252 offset:3072\n\t"
      "v_mfma_f32_16x16x32_bf16 a[168:171], v[8:11], v[80:83], 0\n\t"
      "ds_read_b64_tr_b16 v[6:7], v243 offset:4104\n\t"
      "v_mfma_f32_16x16x32_bf16 a[172:175], v[8:11], v[88:91], 0\n\t"
      "ds_read_b64 v[180:181], v252 offset:3080\n\t"
      "v_mfma_f32_16x16x32_bf16 v[196:199], v[162:165], v[96:99], v[172:175]\n\t"
      "v_mfma_f32_16x16x32_bf16 a[176:179], v[12:15], v[64:67], 0\n\t"
      "ds_read_b64_tr_b16 v[8:9], v243 offset:512\n\t"
      "v_mfma_f32_16x16x32_bf16 a[180:183], v[12:15], v[72:75], 0\n\t"
      "ds_read_b128 v[184:187], v250 offset:800\n\t"
      "v_mfma_f32_16x16x32_bf16 a[184:187], v[12:15], v[80:83], 0\n\t"
      "ds_read_b64_tr_b16 v[10:11], v243 offset:4608\n\t"
      "v_mfma_f32_16x16x32_bf16 v[200:203], v[160:163], v[104:107], v[168:171]\n\t"
      "v_mfma_f32_16x16x32_bf16 a[188:191], v[12:15], v[88:91], 0\n\t"
      "ds_read_b128 v[188:191], v250 offset:816\n\t"
      "v_cvt_pk_bf16_f32 v96, v192, v193\n\t"
      "v_cvt_pk_bf16_f32 v97, v194, v195\n\t"
      "v_mfma_f32_16x16x32_bf16 a[192:195], v[16:19], v[64:67], 0\n\t"
      "ds_read_b64_tr_b16 v[12:13], v243 offset:520\n\t"
      "v_cvt_pk_bf16_f32 v98, v196, v197\n\t"
      "v_cvt_pk_bf16_f32 v99, v198, v199\n\t"
      "v_mfma_f32_16x16x32_bf16 a[196:199], v[16:19], v[72:75], 0\n\t"
      "buffer_load_dwordx4 v244, s[40:43], s73 offen lds\n\t"
      "v_mfma_f32_16x16x32_bf16 v[204:207], v[162:165], v[104:107], v[172:175]\n\t"
      "v_mfma_f32_16x16x32_bf16 a[200:203], v[16:19], v[80:83], 0\n\t"
      "s_add_u32 m0, s59, 50176\n\t"
      "ds_read_b64_tr_b16 v[14:15], v243 offset:4616\n\t"
      "v_mfma_f32_16x16x32_bf16 a[204:207], v[16:19], v[88:91], 0\n\t"
      "buffer_load_dwordx4 v245, s[40:43], s73 offen lds\n\t"
      "v_mfma_f32_16x16x32_bf16 a[208:211], v[20:23], v[64:67], 0\n\t"
      "s_add_u32 m0, s59, 51200\n\t"
      "ds_read_b64_tr_b16 v[16:17], v243 offset:1024\n\t"
      "v_mfma_f32_16x16x32_bf16 v[208:211], v[160:163], v[112:115], v[168:171]\n\t"
      "v_mfma_f32_16x16x32_bf16 a[212:215], v[20:23], v[72:75], 0\n\t"
      "s_mov_b32 s73, 3211264\n\t"
      "buffer_load_dwordx4 v244, s[40:43], s73 offen lds\n\t"
      "v_cvt_pk_bf16_f32 v104, v200, v201\n\t"
      "v_cvt_pk_bf16_f32 v105, v202, v203\n\t"
      "v_mfma_f32_16x16x32_bf16 a[216:219], v[20:23], v[80:83], 0\n\t"
      "s_add_u32 m0, s59, 52224\n\t"
      "ds_read_b64_tr_b16 v[18:19], v243 offset:5120\n\t"
      "v_cvt_pk_bf16_f32 v106, v204, v205\n\t"
      "v_cvt_pk_bf16_f32 v107, v206, v207\n\t"
      "v_mfma_f32_16x16x32_bf16 a[220:223], v[20:23], v[88:91], 0\n\t"
      "buffer_load_dwordx4 v245, s[40:43], s73 offen lds\n\t"
      "v_mfma_f32_16x16x32_bf16 v[212:215], v[162:165], v[112:115], v[172:175]\n\t"
      "v_mfma_f32_16x16x32_bf16 a[224:227], v[24:27], v[64:67], 0\n\t"
      "s_add_u32 m0, s59, 114688\n\t"
      "ds_read_b64_tr_b16 v[20:21], v243 offset:1032\n\t"
      "v_mfma_f32_16x16x32_bf16 a[228:231], v[24:27], v[72:75], 0\n\t"
      "s_mov_b32 s73, 3670016\n\t"
      "buffer_load_dwordx4 v244, s[40:43], s73 offen lds\n\t"
      "v_mfma_f32_16x16x32_bf16 a[232:235], v[24:27], v[80:83], 0\n\t"
      "s_add_u32 m0, s59, 115712\n\t"
      "ds_read_b64_tr_b16 v[22:23], v243 offset:5128\n\t"
      "v_mfma_f32_16x16x32_bf16 v[216:219], v[160:163], v[120:123], v[168:171]\n\t"
      "v_mfma_f32_16x16x32_bf16 a[236:239], v[24:27], v[88:91], 0\n\t"
      "buffer_load_dwordx4 v245, s[40:43], s73 offen lds\n\t"
      "v_cvt_pk_bf16_f32 v112, v208, v209\n\t"
      "v_cvt_pk_bf16_f32 v113, v210, v211\n\t"
      "v_mfma_f32_16x16x32_bf16 a[240:243], v[28:31], v[64:67], 0\n\t"
      "s_add_u32 m0, s59, 116736\n\t"
      "ds_read_b64_tr_b16 v[24:25], v243 offset:1536\n\t"
      "v_cvt_pk_bf16_f32 v114, v212, v213\n\t"
      "v_cvt_pk_bf16_f32 v115, v214, v215\n\t"
      "v_mfma_f32_16x16x32_bf16 a[244:247], v[28:31], v[72:75], 0\n\t"
      "s_mov_b32 s73, 3735552\n\t"
      "buffer_load_dwordx4 v244, s[40:43], s73 offen lds\n\t"
      "v_mfma_f32_16x16x32_bf16 v[220:223], v[162:165], v[120:123], v[172:175]\n\t"
      "v_mfma_f32_16x16x32_bf16 a[248:251], v[28:31], v[80:83], 0\n\t"
      "s_add_u32 m0, s59, 117760\n\t"
      "ds_read_b64_tr_b16 v[26:27], v243 offset:5632\n\t"
      "v_mfma_f32_16x16x32_bf16 a[252:255], v[28:31], v[88:91], 0\n\t"
      "buffer_load_dwordx4 v245, s[40:43], s73 offen lds\n\t"
      "v_mfma_f32_16x16x32_bf16 a[128:131], v[32:35], v[68:71], a[128:131]\n\t"
      "ds_read_b64_tr_b16 v[28:29], v243 offset:1544\n\t"
      "s_waitcnt vmcnt(16) lgkmcnt(9)\n\t"
      "s_nop 0\n\t"
      "v_mfma_f32_16x16x32_bf16 v[192:195], v[176:179], v[100:103], v[184:187]\n\t"
      "v_mfma_f32_16x16x32_bf16 a[132:135], v[32:35], v[76:79], a[132:135]\n\t"
      "buffer_load_dwordx4 v[64:67], v246, s[44:47], s72 offen offset:0 sc1\n\t"
      "v_cvt_pk_bf16_f32 v120, v216, v217\n\t"
      "v_cvt_pk_bf16_f32 v121, v218, v219\n\t"
      "v_mfma_f32_16x16x32_bf16 a[136:139], v[32:35], v[84:87], a[136:139]\n\t"
      "ds_read_b64_tr_b16 v[30:31], v243 offset:5640\n\t"
      "v_cvt_pk_bf16_f32 v122, v220, v221\n\t"
      "v_cvt_pk_bf16_f32 v123, v222, v223\n\t"
      "v_mfma_f32_16x16x32_bf16 a[140:143], v[32:35], v[92:95], a[140:143]\n\t"
      "buffer_load_dwordx4 v[72:75], v247, s[44:47], s72 offen offset:0 sc1\n\t"
      "v_mfma_f32_16x16x32_bf16 v[196:199], v[178:181], v[100:103], v[188:191]\n\t"
      "v_mfma_f32_16x16x32_bf16 a[144:147], v[36:39], v[68:71], a[144:147]\n\t"
      "ds_read_b64_tr_b16 v[32:33], v243 offset:8192\n\t"
      "v_mfma_f32_16x16x32_bf16 a[148:151], v[36:39], v[76:79], a[148:151]\n\t"
      "buffer_load_dwordx4 v[80:83], v248, s[44:47], s72 offen offset:0 sc1\n\t"
      "v_mfma_f32_16x16x32_bf16 a[152:155], v[36:39], v[84:87], a[152:155]\n\t"
      "ds_read_b64_tr_b16 v[34:35], v243 offset:12288\n\t"
      "v_mfma_f32_16x16x32_bf16 v[200:203], v[176:179], v[108:111], v[184:187]\n\t"
      "v_mfma_f32_16x16x32_bf16 a[156:159], v[36:39], v[92:95], a[156:159]\n\t"
      "buffer_load_dwordx4 v[88:91], v249, s[44:47], s72 offen offset:0 sc1\n\t"
      "v_cvt_pk_bf16_f32 v100, v192, v193\n\t"
      "v_cvt_pk_bf16_f32 v101, v194, v195\n\t"
      "v_mfma_f32_16x16x32_bf16 a[160:163], v[40:43], v[68:71], a[160:163]\n\t"
      "ds_read_b64_tr_b16 v[36:37], v243 offset:8200\n\t"
      "v_cvt_pk_bf16_f32 v102, v196, v197\n\t"
      "v_cvt_pk_bf16_f32 v103, v198, v199\n\t"
      "v_mfma_f32_16x16x32_bf16 a[164:167], v[40:43], v[76:79], a[164:167]\n\t"
      "v_mfma_f32_16x16x32_bf16 v[204:207], v[178:181], v[108:111], v[188:191]\n\t"
      "v_mfma_f32_16x16x32_bf16 a[168:171], v[40:43], v[84:87], a[168:171]\n\t"
      "ds_read_b64_tr_b16 v[38:39], v243 offset:12296\n\t"
      "v_mfma_f32_16x16x32_bf16 a[172:175], v[40:43], v[92:95], a[172:175]\n\t"
      "v_mfma_f32_16x16x32_bf16 a[176:179], v[44:47], v[68:71], a[176:179]\n\t"
      "ds_read_b64_tr_b16 v[40:41], v243 offset:8704\n\t"
      "v_mfma_f32_16x16x32_bf16 v[208:211], v[176:179], v[116:119], v[184:187]\n\t"
      "v_mfma_f32_16x16x32_bf16 a[180:183], v[44:47], v[76:79], a[180:183]\n\t"
      "v_cvt_pk_bf16_f32 v108, v200, v201\n\t"
      "v_cvt_pk_bf16_f32 v109, v202, v203\n\t"
      "v_mfma_f32_16x16x32_bf16 a[184:187], v[44:47], v[84:87], a[184:187]\n\t"
      "ds_read_b64_tr_b16 v[42:43], v243 offset:12800\n\t"
      "v_cvt_pk_bf16_f32 v110, v204, v205\n\t"
      "v_cvt_pk_bf16_f32 v111, v206, v207\n\t"
      "v_mfma_f32_16x16x32_bf16 a[188:191], v[44:47], v[92:95], a[188:191]\n\t"
      "v_mfma_f32_16x16x32_bf16 v[212:215], v[178:181], v[116:119], v[188:191]\n\t"
      "v_mfma_f32_16x16x32_bf16 a[192:195], v[48:51], v[68:71], a[192:195]\n\t"
      "ds_read_b64_tr_b16 v[44:45], v243 offset:8712\n\t"
      "v_mfma_f32_16x16x32_bf16 a[196:199], v[48:51], v[76:79], a[196:199]\n\t"
      "v_mfma_f32_16x16x32_bf16 a[200:203], v[48:51], v[84:87], a[200:203]\n\t"
      "ds_read_b64_tr_b16 v[46:47], v243 offset:12808\n\t"
      "v_mfma_f32_16x16x32_bf16 v[216:219], v[176:179], v[124:127], v[184:187]\n\t"
      "v_mfma_f32_16x16x32_bf16 a[204:207], v[48:51], v[92:95], a[204:207]\n\t"
      "v_cvt_pk_bf16_f32 v116, v208, v209\n\t"
      "v_cvt_pk_bf16_f32 v117, v210, v211\n\t"
      "v_mfma_f32_16x16x32_bf16 a[208:211], v[52:55], v[68:71], a[208:211]\n\t"
      "ds_read_b64_tr_b16 v[48:49], v243 offset:9216\n\t"
      "v_cvt_pk_bf16_f32 v118, v212, v213\n\t"
      "v_cvt_pk_bf16_f32 v119, v214, v215\n\t"
      "v_mfma_f32_16x16x32_bf16 a[212:215], v[52:55], v[76:79], a[212:215]\n\t"
      "v_mfma_f32_16x16x32_bf16 v[220:223], v[178:181], v[124:127], v[188:191]\n\t"
      "v_mfma_f32_16x16x32_bf16 a[216:219], v[52:55], v[84:87], a[216:219]\n\t"
      "ds_read_b64_tr_b16 v[50:51], v243 offset:13312\n\t"
      "v_mfma_f32_16x16x32_bf16 a[220:223], v[52:55], v[92:95], a[220:223]\n\t"
      "v_mfma_f32_16x16x32_bf16 a[224:227], v[56:59], v[68:71], a[224:227]\n\t"
      "ds_read_b64_tr_b16 v[52:53], v243 offset:9224\n\t"
      "v_mfma_f32_16x16x32_bf16 a[228:231], v[56:59], v[76:79], a[228:231]\n\t"
      "v_mfma_f32_16x16x32_bf16 a[232:235], v[56:59], v[84:87], a[232:235]\n\t"
      "ds_read_b64_tr_b16 v[54:55], v243 offset:13320\n\t"
      "v_cvt_pk_bf16_f32 v124, v216, v217\n\t"
      "v_cvt_pk_bf16_f32 v125, v218, v219\n\t"
      "v_mfma_f32_16x16x32_bf16 a[236:239], v[56:59], v[92:95], a[236:239]\n\t"
      "v_cvt_pk_bf16_f32 v126, v220, v221\n\t"
      "v_cvt_pk_bf16_f32 v127, v222, v223\n\t"
      "v_mfma_f32_16x16x32_bf16 a[240:243], v[60:63], v[68:71], a[240:243]\n\t"
      "ds_read_b64_tr_b16 v[56:57], v243 offset:9728\n\t"
      "v_mfma_f32_16x16x32_bf16 a[244:247], v[60:63], v[76:79], a[244:247]\n\t"
      "v_mfma_f32_16x16x32_bf16 a[248:251], v[60:63], v[84:87], a[248:251]\n\t"
      "ds_read_b64_tr_b16 v[58:59], v243 offset:13824\n\t"
      "v_mfma_f32_16x16x32_bf16 a[252:255], v[60:63], v[92:95], a[252:255]\n\t"
      "buffer_load_dwordx4 v[68:71], v246, s[44:47], s72 offen offset:1024 sc1\n\t"
      "buffer_load_dwordx4 v[76:79], v247, s[44:47], s72 offen offset:1024 sc1\n\t"
      "buffer_load_dwordx4 v[84:87], v248, s[44:47], s72 offen offset:1024 sc1\n\t"
      "buffer_load_dwordx4 v[92:95], v249, s[44:47], s72 offen offset:1024 sc1\n\t"
      "s_add_u32 s72, s72, 0x800\n\t"
      "s_waitcnt lgkmcnt(2)\n\t"
      "s_nop 0\n\t"
      "v_mfma_f32_16x16x32_bf16 a[128:131], v[0:3], v[96:99], a[128:131]\n\t"
      "ds_read_b64_tr_b16 v[60:61], v243 offset:9736\n\t"
      "v_mfma_f32_16x16x32_bf16 a[132:135], v[0:3], v[104:107], a[132:135]\n\t"
      "ds_read_b64_tr_b16 v[62:63], v243 offset:13832\n\t"
      "v_mfma_f32_16x16x32_bf16 a[136:139], v[0:3], v[112:115], a[136:139]\n\t"
      "ds_read_b64 v[160:161], v252 offset:4096\n\t"
      "v_mfma_f32_16x16x32_bf16 a[140:143], v[0:3], v[120:123], a[140:143]\n\t"
      "s_barrier\n\t"
      "s_nop 0\n\t"
      "ds_read_b64 v[164:165], v252 offset:4104\n\t"
      "v_mfma_f32_16x16x32_bf16 a[144:147], v[4:7], v[96:99], a[144:147]\n\t"
      "ds_read_b64_tr_b16 v[0:1], v242 offset:16384\n\t"
      "v_mfma_f32_16x16x32_bf16 a[148:151], v[4:7], v[104:107], a[148:151]\n\t"
      "s_add_u32 m0, s59, 0\n\t"
      "s_nop 0\n\t"
      "ds_read_b128 v[168:171], v250 offset:1056\n\t"
      "v_mfma_f32_16x16x32_bf16 a[152:155], v[4:7], v[112:115], a[152:155]\n\t"
      "ds_read_b64_tr_b16 v[2:3], v242 offset:20480\n\t"
      "v_mfma_f32_16x16x32_bf16 a[156:159], v[4:7], v[120:123], a[156:159]\n\t"
      "ds_read_b128 v[172:175], v250 offset:1072\n\t"
      "v_mfma_f32_16x16x32_bf16 a[160:163], v[8:11], v[96:99], a[160:163]\n\t"
      "ds_read_b64_tr_b16 v[4:5], v242 offset:16392\n\t"
      "s_waitcnt vmcnt(20) lgkmcnt(1)\n\t"
      "s_nop 0\n\t"
      "v_mfma_f32_16x16x32_bf16 v[192:195], v[160:163], v[128:131], v[168:171]\n\t"
      "v_mfma_f32_16x16x32_bf16 a[164:167], v[8:11], v[104:107], a[164:167]\n\t"
      "ds_read_b64 v[176:177], v252 offset:5120\n\t"
      "v_mfma_f32_16x16x32_bf16 a[168:171], v[8:11], v[112:115], a[168:171]\n\t"
      "ds_read_b64_tr_b16 v[6:7], v242 offset:20488\n\t"
      "v_mfma_f32_16x16x32_bf16 a[172:175], v[8:11], v[120:123], a[172:175]\n\t"
      "ds_read_b64 v[180:181], v252 offset:5128\n\t"
      "v_mfma_f32_16x16x32_bf16 v[196:199], v[162:165], v[128:131], v[172:175]\n\t"
      "v_mfma_f32_16x16x32_bf16 a[176:179], v[12:15], v[96:99], a[176:179]\n\t"
      "ds_read_b64_tr_b16 v[8:9], v242 offset:16896\n\t"
      "v_mfma_f32_16x16x32_bf16 a[180:183], v[12:15], v[104:107], a[180:183]\n\t"
      "ds_read_b128 v[184:187], v250 offset:1312\n\t"
      "v_mfma_f32_16x16x32_bf16 a[184:187], v[12:15], v[112:115], a[184:187]\n\t"
      "ds_read_b64_tr_b16 v[10:11], v242 offset:20992\n\t"
      "v_mfma_f32_16x16x32_bf16 v[200:203], v[160:163], v[136:139], v[168:171]\n\t"
      "v_mfma_f32_16x16x32_bf16 a[188:191], v[12:15], v[120:123], a[188:191]\n\t"
      "s_waitcnt lgkmcnt(13)\n\t"
      "s_nop 0\n\t"
      "ds_read_b128 v[188:191], v250 offset:1328\n\t"
      "v_cvt_pk_bf16_f32 v128, v192, v193\n\t"
      "v_cvt_pk_bf16_f32 v129, v194, v195\n\t"
      "v_mfma_f32_16x16x32_bf16 a[192:195], v[16:19], v[96:99], a[192:195]\n\t"
      "ds_read_b64_tr_b16 v[12:13], v242 offset:16904\n\t"
      "v_cvt_pk_bf16_f32 v130, v196, v197\n\t"
      "v_cvt_pk_bf16_f32 v131, v198, v199\n\t"
      "v_mfma_f32_16x16x32_bf16 a[196:199], v[16:19], v[104:107], a[196:199]\n\t"
      "buffer_load_dwordx4 v235, s[76:79], 0 offen lds\n\t"
      "v_mfma_f32_16x16x32_bf16 v[204:207], v[162:165], v[136:139], v[172:175]\n\t"
      "v_mfma_f32_16x16x32_bf16 a[200:203], v[16:19], v[112:115], a[200:203]\n\t"
      "ds_read_b64_tr_b16 v[14:15], v242 offset:21000\n\t"
      "v_mfma_f32_16x16x32_bf16 a[204:207], v[16:19], v[120:123], a[204:207]\n\t"
      "buffer_load_dwordx4 v236, s[76:79], 0 offen offset:1024 lds\n\t"
      "v_mfma_f32_16x16x32_bf16 a[208:211], v[20:23], v[96:99], a[208:211]\n\t"
      "ds_read_b64_tr_b16 v[16:17], v242 offset:17408\n\t"
      "v_mfma_f32_16x16x32_bf16 v[208:211], v[160:163], v[144:147], v[168:171]\n\t"
      "v_mfma_f32_16x16x32_bf16 a[212:215], v[20:23], v[104:107], a[212:215]\n\t"
      "buffer_load_dwordx4 v237, s[76:79], 0 offen offset:2048 lds\n\t"
      "v_cvt_pk_bf16_f32 v136, v200, v201\n\t"
      "v_cvt_pk_bf16_f32 v137, v202, v203\n\t"
      "v_mfma_f32_16x16x32_bf16 a[216:219], v[20:23], v[112:115], a[216:219]\n\t"
      "ds_read_b64_tr_b16 v[18:19], v242 offset:21504\n\t"
      "v_cvt_pk_bf16_f32 v138, v204, v205\n\t"
      "v_cvt_pk_bf16_f32 v139, v206, v207\n\t"
      "v_mfma_f32_16x16x32_bf16 a[220:223], v[20:23], v[120:123], a[220:223]\n\t"
      "buffer_load_dwordx4 v238, s[76:79], 0 offen offset:3072 lds\n\t"
      "v_mfma_f32_16x16x32_bf16 v[212:215], v[162:165], v[144:147], v[172:175]\n\t"
      "v_mfma_f32_16x16x32_bf16 a[224:227], v[24:27], v[96:99], a[224:227]\n\t"
      "ds_read_b64_tr_b16 v[20:21], v242 offset:17416\n\t"
      "v_mfma_f32_16x16x32_bf16 a[228:231], v[24:27], v[104:107], a[228:231]\n\t"
      "v_mfma_f32_16x16x32_bf16 a[232:235], v[24:27], v[112:115], a[232:235]\n\t"
      "ds_read_b64_tr_b16 v[22:23], v242 offset:21512\n\t"
      "v_mfma_f32_16x16x32_bf16 v[216:219], v[160:163], v[152:155], v[168:171]\n\t"
      "v_mfma_f32_16x16x32_bf16 a[236:239], v[24:27], v[120:123], a[236:239]\n\t"
      "v_cvt_pk_bf16_f32 v144, v208, v209\n\t"
      "v_cvt_pk_bf16_f32 v145, v210, v211\n\t"
      "v_mfma_f32_16x16x32_bf16 a[240:243], v[28:31], v[96:99], a[240:243]\n\t"
      "ds_read_b64_tr_b16 v[24:25], v242 offset:17920\n\t"
      "v_cvt_pk_bf16_f32 v146, v212, v213\n\t"
      "v_cvt_pk_bf16_f32 v147, v214, v215\n\t"
      "v_mfma_f32_16x16x32_bf16 a[244:247], v[28:31], v[104:107], a[244:247]\n\t"
      "v_mfma_f32_16x16x32_bf16 v[220:223], v[162:165], v[152:155], v[172:175]\n\t"
      "v_mfma_f32_16x16x32_bf16 a[248:251], v[28:31], v[112:115], a[248:251]\n\t"
      "ds_read_b64_tr_b16 v[26:27], v242 offset:22016\n\t"
      "v_mfma_f32_16x16x32_bf16 a[252:255], v[28:31], v[120:123], a[252:255]\n\t"
      "v_mfma_f32_16x16x32_bf16 a[128:131], v[32:35], v[100:103], a[128:131]\n\t"
      "ds_read_b64_tr_b16 v[28:29], v242 offset:17928\n\t"
      "s_waitcnt vmcnt(20) lgkmcnt(9)\n\t"
      "s_nop 0\n\t"
      "v_mfma_f32_16x16x32_bf16 v[192:195], v[176:179], v[132:135], v[184:187]\n\t"
      "v_mfma_f32_16x16x32_bf16 a[132:135], v[32:35], v[108:111], a[132:135]\n\t"
      "buffer_load_dwordx4 v[96:99], v246, s[44:47], s72 offen offset:0 sc1\n\t"
      "v_cvt_pk_bf16_f32 v152, v216, v217\n\t"
      "v_cvt_pk_bf16_f32 v153, v218, v219\n\t"
      "v_mfma_f32_16x16x32_bf16 a[136:139], v[32:35], v[116:119], a[136:139]\n\t"
      "ds_read_b64_tr_b16 v[30:31], v242 offset:22024\n\t"
      "v_cvt_pk_bf16_f32 v154, v220, v221\n\t"
      "v_cvt_pk_bf16_f32 v155, v222, v223\n\t"
      "v_mfma_f32_16x16x32_bf16 a[140:143], v[32:35], v[124:127], a[140:143]\n\t"
      "buffer_load_dwordx4 v[104:107], v247, s[44:47], s72 offen offset:0 sc1\n\t"
      "v_mfma_f32_16x16x32_bf16 v[196:199], v[178:181], v[132:135], v[188:191]\n\t"
      "v_mfma_f32_16x16x32_bf16 a[144:147], v[36:39], v[100:103], a[144:147]\n\t"
      "ds_read_b64_tr_b16 v[32:33], v242 offset:24576\n\t"
      "v_mfma_f32_16x16x32_bf16 a[148:151], v[36:39], v[108:111], a[148:151]\n\t"
      "buffer_load_dwordx4 v[112:115], v248, s[44:47], s72 offen offset:0 sc1\n\t"
      "v_mfma_f32_16x16x32_bf16 a[152:155], v[36:39], v[116:119], a[152:155]\n\t"
      "ds_read_b64_tr_b16 v[34:35], v242 offset:28672\n\t"
      "v_mfma_f32_16x16x32_bf16 v[200:203], v[176:179], v[140:143], v[184:187]\n\t"
      "v_mfma_f32_16x16x32_bf16 a[156:159], v[36:39], v[124:127], a[156:159]\n\t"
      "buffer_load_dwordx4 v[120:123], v249, s[44:47], s72 offen offset:0 sc1\n\t"
      "v_cvt_pk_bf16_f32 v132, v192, v193\n\t"
      "v_cvt_pk_bf16_f32 v133, v194, v195\n\t"
      "v_mfma_f32_16x16x32_bf16 a[160:163], v[40:43], v[100:103], a[160:163]\n\t"
      "ds_read_b64_tr_b16 v[36:37], v242 offset:24584\n\t"
      "v_cvt_pk_bf16_f32 v134, v196, v197\n\t"
      "v_cvt_pk_bf16_f32 v135, v198, v199\n\t"
      "v_mfma_f32_16x16x32_bf16 a[164:167], v[40:43], v[108:111], a[164:167]\n\t"
      "v_mfma_f32_16x16x32_bf16 v[204:207], v[178:181], v[140:143], v[188:191]\n\t"
      "v_mfma_f32_16x16x32_bf16 a[168:171], v[40:43], v[116:119], a[168:171]\n\t"
      "ds_read_b64_tr_b16 v[38:39], v242 offset:28680\n\t"
      "v_mfma_f32_16x16x32_bf16 a[172:175], v[40:43], v[124:127], a[172:175]\n\t"
      "v_mfma_f32_16x16x32_bf16 a[176:179], v[44:47], v[100:103], a[176:179]\n\t"
      "ds_read_b64_tr_b16 v[40:41], v242 offset:25088\n\t"
      "v_mfma_f32_16x16x32_bf16 v[208:211], v[176:179], v[148:151], v[184:187]\n\t"
      "v_mfma_f32_16x16x32_bf16 a[180:183], v[44:47], v[108:111], a[180:183]\n\t"
      "v_cvt_pk_bf16_f32 v140, v200, v201\n\t"
      "v_cvt_pk_bf16_f32 v141, v202, v203\n\t"
      "v_mfma_f32_16x16x32_bf16 a[184:187], v[44:47], v[116:119], a[184:187]\n\t"
      "ds_read_b64_tr_b16 v[42:43], v242 offset:29184\n\t"
      "v_cvt_pk_bf16_f32 v142, v204, v205\n\t"
      "v_cvt_pk_bf16_f32 v143, v206, v207\n\t"
      "v_mfma_f32_16x16x32_bf16 a[188:191], v[44:47], v[124:127], a[188:191]\n\t"
      "v_mfma_f32_16x16x32_bf16 v[212:215], v[178:181], v[148:151], v[188:191]\n\t"
      "v_mfma_f32_16x16x32_bf16 a[192:195], v[48:51], v[100:103], a[192:195]\n\t"
      "ds_read_b64_tr_b16 v[44:45], v242 offset:25096\n\t"
      "v_mfma_f32_16x16x32_bf16 a[196:199], v[48:51], v[108:111], a[196:199]\n\t"
      "v_mfma_f32_16x16x32_bf16 a[200:203], v[48:51], v[116:119], a[200:203]\n\t"
      "ds_read_b64_tr_b16 v[46:47], v242 offset:29192\n\t"
      "v_mfma_f32_16x16x32_bf16 v[216:219], v[176:179], v[156:159], v[184:187]\n\t"
      "v_mfma_f32_16x16x32_bf16 a[204:207], v[48:51], v[124:127], a[204:207]\n\t"
      "v_cvt_pk_bf16_f32 v148, v208, v209\n\t"
      "v_cvt_pk_bf16_f32 v149, v210, v211\n\t"
      "v_mfma_f32_16x16x32_bf16 a[208:211], v[52:55], v[100:103], a[208:211]\n\t"
      "ds_read_b64_tr_b16 v[48:49], v242 offset:25600\n\t"
      "v_cvt_pk_bf16_f32 v150, v212, v213\n\t"
      "v_cvt_pk_bf16_f32 v151, v214, v215\n\t"
      "v_mfma_f32_16x16x32_bf16 a[212:215], v[52:55], v[108:111], a[212:215]\n\t"
      "v_mfma_f32_16x16x32_bf16 v[220:223], v[178:181], v[156:159], v[188:191]\n\t"
      "v_mfma_f32_16x16x32_bf16 a[216:219], v[52:55], v[116:119], a[216:219]\n\t"
      "ds_read_b64_tr_b16 v[50:51], v242 offset:29696\n\t"
      "v_mfma_f32_16x16x32_bf16 a[220:223], v[52:55], v[124:127], a[220:223]\n\t"
      "v_mfma_f32_16x16x32_bf16 a[224:227], v[56:59], v[100:103], a[224:227]\n\t"
      "ds_read_b64_tr_b16 v[52:53], v242 offset:25608\n\t"
      "v_mfma_f32_16x16x32_bf16 a[228:231], v[56:59], v[108:111], a[228:231]\n\t"
      "v_mfma_f32_16x16x32_bf16 a[232:235], v[56:59], v[116:119], a[232:235]\n\t"
      "ds_read_b64_tr_b16 v[54:55], v242 offset:29704\n\t"
      "v_cvt_pk_bf16_f32 v156, v216, v217\n\t"
      "v_cvt_pk_bf16_f32 v157, v218, v219\n\t"
      "v_mfma_f32_16x16x32_bf16 a[236:239], v[56:59], v[124:127], a[236:239]\n\t"
      "v_cvt_pk_bf16_f32 v158, v220, v221\n\t"
      "v_cvt_pk_bf16_f32 v159, v222, v223\n\t"
      "v_mfma_f32_16x16x32_bf16 a[240:243], v[60:63], v[100:103], a[240:243]\n\t"
      "ds_read_b64_tr_b16 v[56:57], v242 offset:26112\n\t"
      "v_mfma_f32_16x16x32_bf16 a[244:247], v[60:63], v[108:111], a[244:247]\n\t"
      "v_mfma_f32_16x16x32_bf16 a[248:251], v[60:63], v[116:119], a[248:251]\n\t"
      "ds_read_b64_tr_b16 v[58:59], v242 offset:30208\n\t"
      "v_mfma_f32_16x16x32_bf16 a[252:255], v[60:63], v[124:127], a[252:255]\n\t"
      "buffer_load_dwordx4 v[100:103], v246, s[44:47], s72 offen offset:1024 sc1\n\t"
      "buffer_load_dwordx4 v[108:111], v247, s[44:47], s72 offen offset:1024 sc1\n\t"
      "buffer_load_dwordx4 v[116:119], v248, s[44:47], s72 offen offset:1024 sc1\n\t"
      "buffer_load_dwordx4 v[124:127], v249, s[44:47], s72 offen offset:1024 sc1\n\t"
      "s_add_u32 s72, s72, 0x800\n\t"
      "s_waitcnt lgkmcnt(2)\n\t"
      "s_nop 0\n\t"
      "v_mfma_f32_16x16x32_bf16 a[128:131], v[0:3], v[128:131], a[128:131]\n\t"
      "ds_read_b64_tr_b16 v[60:61], v242 offset:26120\n\t"
      "v_mfma_f32_16x16x32_bf16 a[132:135], v[0:3], v[136:139], a[132:135]\n\t"
      "ds_read_b64_tr_b16 v[62:63], v242 offset:30216\n\t"
      "v_mfma_f32_16x16x32_bf16 a[136:139], v[0:3], v[144:147], a[136:139]\n\t"
      "ds_read_b64 v[160:161], v252 offset:6144\n\t"
      "v_mfma_f32_16x16x32_bf16 a[140:143], v[0:3], v[152:155], a[140:143]\n\t"
      "s_barrier\n\t"
      "s_nop 0\n\t"
      "ds_read_b64 v[164:165], v252 offset:6152\n\t"
      "v_mfma_f32_16x16x32_bf16 a[144:147], v[4:7], v[128:131], a[144:147]\n\t"
      "ds_read_b64_tr_b16 v[0:1], v243 offset:16384\n\t"
      "v_mfma_f32_16x16x32_bf16 a[148:151], v[4:7], v[136:139], a[148:151]\n\t"
      "s_add_u32 m0, s60, 0\n\t"
      "s_nop 0\n\t"
      "ds_read_b128 v[168:171], v250 offset:1568\n\t"
      "v_mfma_f32_16x16x32_bf16 a[152:155], v[4:7], v[144:147], a[152:155]\n\t"
      "ds_read_b64_tr_b16 v[2:3], v243 offset:20480\n\t"
      "v_mfma_f32_16x16x32_bf16 a[156:159], v[4:7], v[152:155], a[156:159]\n\t"
      "ds_read_b128 v[172:175], v250 offset:1584\n\t"
      "v_mfma_f32_16x16x32_bf16 a[160:163], v[8:11], v[128:131], a[160:163]\n\t"
      "ds_read_b64_tr_b16 v[4:5], v243 offset:16392\n\t"
      "s_waitcnt vmcnt(16) lgkmcnt(1)\n\t"
      "s_nop 0\n\t"
      "v_mfma_f32_16x16x32_bf16 v[192:195], v[160:163], v[64:67], v[168:171]\n\t"
      "v_mfma_f32_16x16x32_bf16 a[164:167], v[8:11], v[136:139], a[164:167]\n\t"
      "ds_read_b64 v[176:177], v252 offset:7168\n\t"
      "v_mfma_f32_16x16x32_bf16 a[168:171], v[8:11], v[144:147], a[168:171]\n\t"
      "ds_read_b64_tr_b16 v[6:7], v243 offset:20488\n\t"
      "v_mfma_f32_16x16x32_bf16 a[172:175], v[8:11], v[152:155], a[172:175]\n\t"
      "ds_read_b64 v[180:181], v252 offset:7176\n\t"
      "v_mfma_f32_16x16x32_bf16 v[196:199], v[162:165], v[64:67], v[172:175]\n\t"
      "v_mfma_f32_16x16x32_bf16 a[176:179], v[12:15], v[128:131], a[176:179]\n\t"
      "ds_read_b64_tr_b16 v[8:9], v243 offset:16896\n\t"
      "v_mfma_f32_16x16x32_bf16 a[180:183], v[12:15], v[136:139], a[180:183]\n\t"
      "ds_read_b128 v[184:187], v250 offset:1824\n\t"
      "v_mfma_f32_16x16x32_bf16 a[184:187], v[12:15], v[144:147], a[184:187]\n\t"
      "ds_read_b64_tr_b16 v[10:11], v243 offset:20992\n\t"
      "v_mfma_f32_16x16x32_bf16 v[200:203], v[160:163], v[72:75], v[168:171]\n\t"
      "v_mfma_f32_16x16x32_bf16 a[188:191], v[12:15], v[152:155], a[188:191]\n\t"
      "s_waitcnt lgkmcnt(13)\n\t"
      "s_nop 0\n\t"
      "ds_read_b128 v[188:191], v250 offset:1840\n\t"
      "v_cvt_pk_bf16_f32 v64, v192, v193\n\t"
      "v_cvt_pk_bf16_f32 v65, v194, v195\n\t"
      "v_mfma_f32_16x16x32_bf16 a[192:195], v[16:19], v[128:131], a[192:195]\n\t"
      "ds_read_b64_tr_b16 v[12:13], v243 offset:16904\n\t"
      "v_cvt_pk_bf16_f32 v66, v196, v197\n\t"
      "v_cvt_pk_bf16_f32 v67, v198, v199\n\t"
      "v_mfma_f32_16x16x32_bf16 a[196:199], v[16:19], v[136:139], a[196:199]\n\t"
      "buffer_load_dwordx4 v239, s[36:39], 0 offen lds\n\t"
      "v_mfma_f32_16x16x32_bf16 v[204:207], v[162:165], v[72:75], v[172:175]\n\t"
      "v_mfma_f32_16x16x32_bf16 a[200:203], v[16:19], v[144:147], a[200:203]\n\t"
      "ds_read_b64_tr_b16 v[14:15], v243 offset:21000\n\t"
      "v_mfma_f32_16x16x32_bf16 a[204:207], v[16:19], v[152:155], a[204:207]\n\t"
      "buffer_load_dwordx4 v240, s[36:39], 0 offen offset:1024 lds\n\t"
      "v_mfma_f32_16x16x32_bf16 a[208:211], v[20:23], v[128:131], a[208:211]\n\t"
      "ds_read_b64_tr_b16 v[16:17], v243 offset:17408\n\t"
      "v_mfma_f32_16x16x32_bf16 v[208:211], v[160:163], v[80:83], v[168:171]\n\t"
      "v_mfma_f32_16x16x32_bf16 a[212:215], v[20:23], v[136:139], a[212:215]\n\t"
      "buffer_load_dwordx4 v239, s[36:39], 0 offen offset:2048 lds\n\t"
      "v_cvt_pk_bf16_f32 v72, v200, v201\n\t"
      "v_cvt_pk_bf16_f32 v73, v202, v203\n\t"
      "v_mfma_f32_16x16x32_bf16 a[216:219], v[20:23], v[144:147], a[216:219]\n\t"
      "ds_read_b64_tr_b16 v[18:19], v243 offset:21504\n\t"
      "v_cvt_pk_bf16_f32 v74, v204, v205\n\t"
      "v_cvt_pk_bf16_f32 v75, v206, v207\n\t"
      "v_mfma_f32_16x16x32_bf16 a[220:223], v[20:23], v[152:155], a[220:223]\n\t"
      "buffer_load_dwordx4 v240, s[36:39], 0 offen offset:3072 lds\n\t"
      "v_mfma_f32_16x16x32_bf16 v[212:215], v[162:165], v[80:83], v[172:175]\n\t"
      "v_mfma_f32_16x16x32_bf16 a[224:227], v[24:27], v[128:131], a[224:227]\n\t"
      "ds_read_b64_tr_b16 v[20:21], v243 offset:17416\n\t"
      "v_mfma_f32_16x16x32_bf16 a[228:231], v[24:27], v[136:139], a[228:231]\n\t"
      "v_mfma_f32_16x16x32_bf16 a[232:235], v[24:27], v[144:147], a[232:235]\n\t"
      "ds_read_b64_tr_b16 v[22:23], v243 offset:21512\n\t"
      "v_mfma_f32_16x16x32_bf16 v[216:219], v[160:163], v[88:91], v[168:171]\n\t"
      "v_mfma_f32_16x16x32_bf16 a[236:239], v[24:27], v[152:155], a[236:239]\n\t"
      "v_cvt_pk_bf16_f32 v80, v208, v209\n\t"
      "v_cvt_pk_bf16_f32 v81, v210, v211\n\t"
      "v_mfma_f32_16x16x32_bf16 a[240:243], v[28:31], v[128:131], a[240:243]\n\t"
      "ds_read_b64_tr_b16 v[24:25], v243 offset:17920\n\t"
      "v_cvt_pk_bf16_f32 v82, v212, v213\n\t"
      "v_cvt_pk_bf16_f32 v83, v214, v215\n\t"
      "v_mfma_f32_16x16x32_bf16 a[244:247], v[28:31], v[136:139], a[244:247]\n\t"
      "v_mfma_f32_16x16x32_bf16 v[220:223], v[162:165], v[88:91], v[172:175]\n\t"
      "v_mfma_f32_16x16x32_bf16 a[248:251], v[28:31], v[144:147], a[248:251]\n\t"
      "ds_read_b64_tr_b16 v[26:27], v243 offset:22016\n\t"
      "v_mfma_f32_16x16x32_bf16 a[252:255], v[28:31], v[152:155], a[252:255]\n\t"
      "v_mfma_f32_16x16x32_bf16 a[128:131], v[32:35], v[132:135], a[128:131]\n\t"
      "ds_read_b64_tr_b16 v[28:29], v243 offset:17928\n\t"
      "s_waitcnt vmcnt(16) lgkmcnt(9)\n\t"
      "s_nop 0\n\t"
      "v_mfma_f32_16x16x32_bf16 v[192:195], v[176:179], v[68:71], v[184:187]\n\t"
      "v_mfma_f32_16x16x32_bf16 a[132:135], v[32:35], v[140:143], a[132:135]\n\t"
      "buffer_load_dwordx4 v[128:131], v246, s[44:47], s72 offen offset:0 sc1\n\t"
      "v_cvt_pk_bf16_f32 v88, v216, v217\n\t"
      "v_cvt_pk_bf16_f32 v89, v218, v219\n\t"
      "v_mfma_f32_16x16x32_bf16 a[136:139], v[32:35], v[148:151], a[136:139]\n\t"
      "ds_read_b64_tr_b16 v[30:31], v243 offset:22024\n\t"
      "v_cvt_pk_bf16_f32 v90, v220, v221\n\t"
      "v_cvt_pk_bf16_f32 v91, v222, v223\n\t"
      "v_mfma_f32_16x16x32_bf16 a[140:143], v[32:35], v[156:159], a[140:143]\n\t"
      "buffer_load_dwordx4 v[136:139], v247, s[44:47], s72 offen offset:0 sc1\n\t"
      "v_mfma_f32_16x16x32_bf16 v[196:199], v[178:181], v[68:71], v[188:191]\n\t"
      "v_mfma_f32_16x16x32_bf16 a[144:147], v[36:39], v[132:135], a[144:147]\n\t"
      "ds_read_b64_tr_b16 v[32:33], v243 offset:24576\n\t"
      "v_mfma_f32_16x16x32_bf16 a[148:151], v[36:39], v[140:143], a[148:151]\n\t"
      "buffer_load_dwordx4 v[144:147], v248, s[44:47], s72 offen offset:0 sc1\n\t"
      "v_mfma_f32_16x16x32_bf16 a[152:155], v[36:39], v[148:151], a[152:155]\n\t"
      "ds_read_b64_tr_b16 v[34:35], v243 offset:28672\n\t"
      "v_mfma_f32_16x16x32_bf16 v[200:203], v[176:179], v[76:79], v[184:187]\n\t"
      "v_mfma_f32_16x16x32_bf16 a[156:159], v[36:39], v[156:159], a[156:159]\n\t"
      "buffer_load_dwordx4 v[152:155], v249, s[44:47], s72 offen offset:0 sc1\n\t"
      "v_cvt_pk_bf16_f32 v68, v192, v193\n\t"
      "v_cvt_pk_bf16_f32 v69, v194, v195\n\t"
      "v_mfma_f32_16x16x32_bf16 a[160:163], v[40:43], v[132:135], a[160:163]\n\t"
      "ds_read_b64_tr_b16 v[36:37], v243 offset:24584\n\t"
      "v_cvt_pk_bf16_f32 v70, v196, v197\n\t"
      "v_cvt_pk_bf16_f32 v71, v198, v199\n\t"
      "v_mfma_f32_16x16x32_bf16 a[164:167], v[40:43], v[140:143], a[164:167]\n\t"
      "v_mfma_f32_16x16x32_bf16 v[204:207], v[178:181], v[76:79], v[188:191]\n\t"
      "v_mfma_f32_16x16x32_bf16 a[168:171], v[40:43], v[148:151], a[168:171]\n\t"
      "ds_read_b64_tr_b16 v[38:39], v243 offset:28680\n\t"
      "v_mfma_f32_16x16x32_bf16 a[172:175], v[40:43], v[156:159], a[172:175]\n\t"
      "v_mfma_f32_16x16x32_bf16 a[176:179], v[44:47], v[132:135], a[176:179]\n\t"
      "ds_read_b64_tr_b16 v[40:41], v243 offset:25088\n\t"
      "v_mfma_f32_16x16x32_bf16 v[208:211], v[176:179], v[84:87], v[184:187]\n\t"
      "v_mfma_f32_16x16x32_bf16 a[180:183], v[44:47], v[140:143], a[180:183]\n\t"
      "v_cvt_pk_bf16_f32 v76, v200, v201\n\t"
      "v_cvt_pk_bf16_f32 v77, v202, v203\n\t"
      "v_mfma_f32_16x16x32_bf16 a[184:187], v[44:47], v[148:151], a[184:187]\n\t"
      "ds_read_b64_tr_b16 v[42:43], v243 offset:29184\n\t"
      "v_cvt_pk_bf16_f32 v78, v204, v205\n\t"
      "v_cvt_pk_bf16_f32 v79, v206, v207\n\t"
      "v_mfma_f32_16x16x32_bf16 a[188:191], v[44:47], v[156:159], a[188:191]\n\t"
      "v_mfma_f32_16x16x32_bf16 v[212:215], v[178:181], v[84:87], v[188:191]\n\t"
      "v_mfma_f32_16x16x32_bf16 a[192:195], v[48:51], v[132:135], a[192:195]\n\t"
      "ds_read_b64_tr_b16 v[44:45], v243 offset:25096\n\t"
      "v_mfma_f32_16x16x32_bf16 a[196:199], v[48:51], v[140:143], a[196:199]\n\t"
      "v_mfma_f32_16x16x32_bf16 a[200:203], v[48:51], v[148:151], a[200:203]\n\t"
      "ds_read_b64_tr_b16 v[46:47], v243 offset:29192\n\t"
      "v_mfma_f32_16x16x32_bf16 v[216:219], v[176:179], v[92:95], v[184:187]\n\t"
      "v_mfma_f32_16x16x32_bf16 a[204:207], v[48:51], v[156:159], a[204:207]\n\t"
      "v_cvt_pk_bf16_f32 v84, v208, v209\n\t"
      "v_cvt_pk_bf16_f32 v85, v210, v211\n\t"
      "v_mfma_f32_16x16x32_bf16 a[208:211], v[52:55], v[132:135], a[208:211]\n\t"
      "ds_read_b64_tr_b16 v[48:49], v243 offset:25600\n\t"
      "v_cvt_pk_bf16_f32 v86, v212, v213\n\t"
      "v_cvt_pk_bf16_f32 v87, v214, v215\n\t"
      "v_mfma_f32_16x16x32_bf16 a[212:215], v[52:55], v[140:143], a[212:215]\n\t"
      "v_mfma_f32_16x16x32_bf16 v[220:223], v[178:181], v[92:95], v[188:191]\n\t"
      "v_mfma_f32_16x16x32_bf16 a[216:219], v[52:55], v[148:151], a[216:219]\n\t"
      "ds_read_b64_tr_b16 v[50:51], v243 offset:29696\n\t"
      "v_mfma_f32_16x16x32_bf16 a[220:223], v[52:55], v[156:159], a[220:223]\n\t"
      "v_mfma_f32_16x16x32_bf16 a[224:227], v[56:59], v[132:135], a[224:227]\n\t"
      "ds_read_b64_tr_b16 v[52:53], v243 offset:25608\n\t"
      "v_mfma_f32_16x16x32_bf16 a[228:231], v[56:59], v[140:143], a[228:231]\n\t"
      "v_mfma_f32_16x16x32_bf16 a[232:235], v[56:59], v[148:151], a[232:235]\n\t"
      "ds_read_b64_tr_b16 v[54:55], v243 offset:29704\n\t"
      "v_cvt_pk_bf16_f32 v92, v216, v217\n\t"
      "v_cvt_pk_bf16_f32 v93, v218, v219\n\t"
      "v_mfma_f32_16x16x32_bf16 a[236:239], v[56:59], v[156:159], a[236:239]\n\t"
      "v_cvt_pk_bf16_f32 v94, v220, v221\n\t"
      "v_cvt_pk_bf16_f32 v95, v222, v223\n\t"
      "v_mfma_f32_16x16x32_bf16 a[240:243], v[60:63], v[132:135], a[240:243]\n\t"
      "ds_read_b64_tr_b16 v[56:57], v243 offset:26112\n\t"
      "v_mfma_f32_16x16x32_bf16 a[244:247], v[60:63], v[140:143], a[244:247]\n\t"
      "v_mfma_f32_16x16x32_bf16 a[248:251], v[60:63], v[148:151], a[248:251]\n\t"
      "ds_read_b64_tr_b16 v[58:59], v243 offset:30208\n\t"
      "v_mfma_f32_16x16x32_bf16 a[252:255], v[60:63], v[156:159], a[252:255]\n\t"
      "buffer_load_dwordx4 v[132:135], v246, s[44:47], s72 offen offset:1024 sc1\n\t"
      "buffer_load_dwordx4 v[140:143], v247, s[44:47], s72 offen offset:1024 sc1\n\t"
      "buffer_load_dwordx4 v[148:151], v248, s[44:47], s72 offen offset:1024 sc1\n\t"
      "buffer_load_dwordx4 v[156:159], v249, s[44:47], s72 offen offset:1024 sc1\n\t"
      "s_add_u32 s72, s72, 0x800\n\t"
      "s_waitcnt lgkmcnt(2)\n\t"
      "s_nop 0\n\t"
      "v_mfma_f32_16x16x32_bf16 a[128:131], v[0:3], v[64:67], a[128:131]\n\t"
      "ds_read_b64_tr_b16 v[60:61], v243 offset:26120\n\t"
      "v_mfma_f32_16x16x32_bf16 a[132:135], v[0:3], v[72:75], a[132:135]\n\t"
      "ds_read_b64_tr_b16 v[62:63], v243 offset:30216\n\t"
      "v_mfma_f32_16x16x32_bf16 a[136:139], v[0:3], v[80:83], a[136:139]\n\t"
      "ds_read_b64 v[160:161], v252 offset:8192\n\t"
      "v_mfma_f32_16x16x32_bf16 a[140:143], v[0:3], v[88:91], a[140:143]\n\t"
      "s_barrier\n\t"
      "s_nop 0\n\t"
      "ds_read_b64 v[164:165], v252 offset:8200\n\t"
      "v_mfma_f32_16x16x32_bf16 a[144:147], v[4:7], v[64:67], a[144:147]\n\t"
      "ds_read_b64_tr_b16 v[0:1], v242 offset:32768\n\t"
      "v_mfma_f32_16x16x32_bf16 a[148:151], v[4:7], v[72:75], a[148:151]\n\t"
      "s_add_u32 m0, s59, 16384\n\t"
      "ds_read_b128 v[168:171], v250 offset:2080\n\t"
      "v_mfma_f32_16x16x32_bf16 a[152:155], v[4:7], v[80:83], a[152:155]\n\t"
      "s_mov_b32 s61, 16384\n\t"
      "ds_read_b64_tr_b16 v[2:3], v242 offset:36864\n\t"
      "v_mfma_f32_16x16x32_bf16 a[156:159], v[4:7], v[88:91], a[156:159]\n\t"
      "ds_read_b128 v[172:175], v250 offset:2096\n\t"
      "v_mfma_f32_16x16x32_bf16 a[160:163], v[8:11], v[64:67], a[160:163]\n\t"
      "ds_read_b64_tr_b16 v[4:5], v242 offset:32776\n\t"
      "s_waitcnt vmcnt(16) lgkmcnt(1)\n\t"
      "s_nop 0\n\t"
      "v_mfma_f32_16x16x32_bf16 v[192:195], v[160:163], v[96:99], v[168:171]\n\t"
      "v_mfma_f32_16x16x32_bf16 a[164:167], v[8:11], v[72:75], a[164:167]\n\t"
      "ds_read_b64 v[176:177], v252 offset:9216\n\t"
      "v_mfma_f32_16x16x32_bf16 a[168:171], v[8:11], v[80:83], a[168:171]\n\t"
      "ds_read_b64_tr_b16 v[6:7], v242 offset:36872\n\t"
      "v_mfma_f32_16x16x32_bf16 a[172:175], v[8:11], v[88:91], a[172:175]\n\t"
      "ds_read_b64 v[180:181], v252 offset:9224\n\t"
      "v_mfma_f32_16x16x32_bf16 v[196:199], v[162:165], v[96:99], v[172:175]\n\t"
      "v_mfma_f32_16x16x32_bf16 a[176:179], v[12:15], v[64:67], a[176:179]\n\t"
      "ds_read_b64_tr_b16 v[8:9], v242 offset:33280\n\t"
      "v_mfma_f32_16x16x32_bf16 a[180:183], v[12:15], v[72:75], a[180:183]\n\t"
      "ds_read_b128 v[184:187], v250 offset:2336\n\t"
      "v_mfma_f32_16x16x32_bf16 a[184:187], v[12:15], v[80:83], a[184:187]\n\t"
      "ds_read_b64_tr_b16 v[10:11], v242 offset:37376\n\t"
      "v_mfma_f32_16x16x32_bf16 v[200:203], v[160:163], v[104:107], v[168:171]\n\t"
      "v_mfma_f32_16x16x32_bf16 a[188:191], v[12:15], v[88:91], a[188:191]\n\t"
      "s_waitcnt lgkmcnt(13)\n\t"
      "s_nop 0\n\t"
      "ds_read_b128 v[188:191], v250 offset:2352\n\t"
      "v_cvt_pk_bf16_f32 v96, v192, v193\n\t"
      "v_cvt_pk_bf16_f32 v97, v194, v195\n\t"
      "v_mfma_f32_16x16x32_bf16 a[192:195], v[16:19], v[64:67], a[192:195]\n\t"
      "ds_read_b64_tr_b16 v[12:13], v242 offset:33288\n\t"
      "v_cvt_pk_bf16_f32 v98, v196, v197\n\t"
      "v_cvt_pk_bf16_f32 v99, v198, v199\n\t"
      "v_mfma_f32_16x16x32_bf16 a[196:199], v[16:19], v[72:75], a[196:199]\n\t"
      "buffer_load_dwordx4 v235, s[76:79], s61 offen lds\n\t"
      "v_mfma_f32_16x16x32_bf16 v[204:207], v[162:165], v[104:107], v[172:175]\n\t"
      "v_mfma_f32_16x16x32_bf16 a[200:203], v[16:19], v[80:83], a[200:203]\n\t"
      "ds_read_b64_tr_b16 v[14:15], v242 offset:37384\n\t"
      "v_mfma_f32_16x16x32_bf16 a[204:207], v[16:19], v[88:91], a[204:207]\n\t"
      "buffer_load_dwordx4 v236, s[76:79], s61 offen offset:1024 lds\n\t"
      "v_mfma_f32_16x16x32_bf16 a[208:211], v[20:23], v[64:67], a[208:211]\n\t"
      "ds_read_b64_tr_b16 v[16:17], v242 offset:33792\n\t"
      "v_mfma_f32_16x16x32_bf16 v[208:211], v[160:163], v[112:115], v[168:171]\n\t"
      "v_mfma_f32_16x16x32_bf16 a[212:215], v[20:23], v[72:75], a[212:215]\n\t"
      "buffer_load_dwordx4 v237, s[76:79], s61 offen offset:2048 lds\n\t"
      "v_cvt_pk_bf16_f32 v104, v200, v201\n\t"
      "v_cvt_pk_bf16_f32 v105, v202, v203\n\t"
      "v_mfma_f32_16x16x32_bf16 a[216:219], v[20:23], v[80:83], a[216:219]\n\t"
      "ds_read_b64_tr_b16 v[18:19], v242 offset:37888\n\t"
      "v_cvt_pk_bf16_f32 v106, v204, v205\n\t"
      "v_cvt_pk_bf16_f32 v107, v206, v207\n\t"
      "v_mfma_f32_16x16x32_bf16 a[220:223], v[20:23], v[88:91], a[220:223]\n\t"
      "buffer_load_dwordx4 v238, s[76:79], s61 offen offset:3072 lds\n\t"
      "v_mfma_f32_16x16x32_bf16 v[212:215], v[162:165], v[112:115], v[172:175]\n\t"
      "v_mfma_f32_16x16x32_bf16 a[224:227], v[24:27], v[64:67], a[224:227]\n\t"
      "ds_read_b64_tr_b16 v[20:21], v242 offset:33800\n\t"
      "v_mfma_f32_16x16x32_bf16 a[228:231], v[24:27], v[72:75], a[228:231]\n\t"
      "v_mfma_f32_16x16x32_bf16 a[232:235], v[24:27], v[80:83], a[232:235]\n\t"
      "ds_read_b64_tr_b16 v[22:23], v242 offset:37896\n\t"
      "v_mfma_f32_16x16x32_bf16 v[216:219], v[160:163], v[120:123], v[168:171]\n\t"
      "v_mfma_f32_16x16x32_bf16 a[236:239], v[24:27], v[88:91], a[236:239]\n\t"
      "v_cvt_pk_bf16_f32 v112, v208, v209\n\t"
      "v_cvt_pk_bf16_f32 v113, v210, v211\n\t"
      "v_mfma_f32_16x16x32_bf16 a[240:243], v[28:31], v[64:67], a[240:243]\n\t"
      "ds_read_b64_tr_b16 v[24:25], v242 offset:34304\n\t"
      "v_cvt_pk_bf16_f32 v114, v212, v213\n\t"
      "v_cvt_pk_bf16_f32 v115, v214, v215\n\t"
      "v_mfma_f32_16x16x32_bf16 a[244:247], v[28:31], v[72:75], a[244:247]\n\t"
      "v_mfma_f32_16x16x32_bf16 v[220:223], v[162:165], v[120:123], v[172:175]\n\t"
      "v_mfma_f32_16x16x32_bf16 a[248:251], v[28:31], v[80:83], a[248:251]\n\t"
      "ds_read_b64_tr_b16 v[26:27], v242 offset:38400\n\t"
      "v_mfma_f32_16x16x32_bf16 a[252:255], v[28:31], v[88:91], a[252:255]\n\t"
      "v_mfma_f32_16x16x32_bf16 a[128:131], v[32:35], v[68:71], a[128:131]\n\t"
      "ds_read_b64_tr_b16 v[28:29], v242 offset:34312\n\t"
      "s_waitcnt vmcnt(16) lgkmcnt(9)\n\t"
      "s_nop 0\n\t"
      "v_mfma_f32_16x16x32_bf16 v[192:195], v[176:179], v[100:103], v[184:187]\n\t"
      "v_mfma_f32_16x16x32_bf16 a[132:135], v[32:35], v[76:79], a[132:135]\n\t"
      "buffer_load_dwordx4 v[64:67], v246, s[44:47], s72 offen offset:0 sc1\n\t"
      "v_cvt_pk_bf16_f32 v120, v216, v217\n\t"
      "v_cvt_pk_bf16_f32 v121, v218, v219\n\t"
      "v_mfma_f32_16x16x32_bf16 a[136:139], v[32:35], v[84:87], a[136:139]\n\t"
      "ds_read_b64_tr_b16 v[30:31], v242 offset:38408\n\t"
      "v_cvt_pk_bf16_f32 v122, v220, v221\n\t"
      "v_cvt_pk_bf16_f32 v123, v222, v223\n\t"
      "v_mfma_f32_16x16x32_bf16 a[140:143], v[32:35], v[92:95], a[140:143]\n\t"
      "buffer_load_dwordx4 v[72:75], v247, s[44:47], s72 offen offset:0 sc1\n\t"
      "v_mfma_f32_16x16x32_bf16 v[196:199], v[178:181], v[100:103], v[188:191]\n\t"
      "v_mfma_f32_16x16x32_bf16 a[144:147], v[36:39], v[68:71], a[144:147]\n\t"
      "ds_read_b64_tr_b16 v[32:33], v242 offset:40960\n\t"
      "v_mfma_f32_16x16x32_bf16 a[148:151], v[36:39], v[76:79], a[148:151]\n\t"
      "buffer_load_dwordx4 v[80:83], v248, s[44:47], s72 offen offset:0 sc1\n\t"
      "v_mfma_f32_16x16x32_bf16 a[152:155], v[36:39], v[84:87], a[152:155]\n\t"
      "ds_read_b64_tr_b16 v[34:35], v242 offset:45056\n\t"
      "v_mfma_f32_16x16x32_bf16 v[200:203], v[176:179], v[108:111], v[184:187]\n\t"
      "v_mfma_f32_16x16x32_bf16 a[156:159], v[36:39], v[92:95], a[156:159]\n\t"
      "buffer_load_dwordx4 v[88:91], v249, s[44:47], s72 offen offset:0 sc1\n\t"
      "v_cvt_pk_bf16_f32 v100, v192, v193\n\t"
      "v_cvt_pk_bf16_f32 v101, v194, v195\n\t"
      "v_mfma_f32_16x16x32_bf16 a[160:163], v[40:43], v[68:71], a[160:163]\n\t"
      "ds_read_b64_tr_b16 v[36:37], v242 offset:40968\n\t"
      "v_cvt_pk_bf16_f32 v102, v196, v197\n\t"
      "v_cvt_pk_bf16_f32 v103, v198, v199\n\t"
      "v_mfma_f32_16x16x32_bf16 a[164:167], v[40:43], v[76:79], a[164:167]\n\t"
      "v_mfma_f32_16x16x32_bf16 v[204:207], v[178:181], v[108:111], v[188:191]\n\t"
      "v_mfma_f32_16x16x32_bf16 a[168:171], v[40:43], v[84:87], a[168:171]\n\t"
      "ds_read_b64_tr_b16 v[38:39], v242 offset:45064\n\t"
      "v_mfma_f32_16x16x32_bf16 a[172:175], v[40:43], v[92:95], a[172:175]\n\t"
      "v_mfma_f32_16x16x32_bf16 a[176:179], v[44:47], v[68:71], a[176:179]\n\t"
      "ds_read_b64_tr_b16 v[40:41], v242 offset:41472\n\t"
      "v_mfma_f32_16x16x32_bf16 v[208:211], v[176:179], v[116:119], v[184:187]\n\t"
      "v_mfma_f32_16x16x32_bf16 a[180:183], v[44:47], v[76:79], a[180:183]\n\t"
      "v_cvt_pk_bf16_f32 v108, v200, v201\n\t"
      "v_cvt_pk_bf16_f32 v109, v202, v203\n\t"
      "v_mfma_f32_16x16x32_bf16 a[184:187], v[44:47], v[84:87], a[184:187]\n\t"
      "ds_read_b64_tr_b16 v[42:43], v242 offset:45568\n\t"
      "v_cvt_pk_bf16_f32 v110, v204, v205\n\t"
      "v_cvt_pk_bf16_f32 v111, v206, v207\n\t"
      "v_mfma_f32_16x16x32_bf16 a[188:191], v[44:47], v[92:95], a[188:191]\n\t"
      "v_mfma_f32_16x16x32_bf16 v[212:215], v[178:181], v[116:119], v[188:191]\n\t"
      "v_mfma_f32_16x16x32_bf16 a[192:195], v[48:51], v[68:71], a[192:195]\n\t"
      "ds_read_b64_tr_b16 v[44:45], v242 offset:41480\n\t"
      "v_mfma_f32_16x16x32_bf16 a[196:199], v[48:51], v[76:79], a[196:199]\n\t"
      "v_mfma_f32_16x16x32_bf16 a[200:203], v[48:51], v[84:87], a[200:203]\n\t"
      "ds_read_b64_tr_b16 v[46:47], v242 offset:45576\n\t"
      "v_mfma_f32_16x16x32_bf16 v[216:219], v[176:179], v[124:127], v[184:187]\n\t"
      "v_mfma_f32_16x16x32_bf16 a[204:207], v[48:51], v[92:95], a[204:207]\n\t"
      "v_cvt_pk_bf16_f32 v116, v208, v209\n\t"
      "v_cvt_pk_bf16_f32 v117, v210, v211\n\t"
      "v_mfma_f32_16x16x32_bf16 a[208:211], v[52:55], v[68:71], a[208:211]\n\t"
      "ds_read_b64_tr_b16 v[48:49], v242 offset:41984\n\t"
      "v_cvt_pk_bf16_f32 v118, v212, v213\n\t"
      "v_cvt_pk_bf16_f32 v119, v214, v215\n\t"
      "v_mfma_f32_16x16x32_bf16 a[212:215], v[52:55], v[76:79], a[212:215]\n\t"
      "v_mfma_f32_16x16x32_bf16 v[220:223], v[178:181], v[124:127], v[188:191]\n\t"
      "v_mfma_f32_16x16x32_bf16 a[216:219], v[52:55], v[84:87], a[216:219]\n\t"
      "ds_read_b64_tr_b16 v[50:51], v242 offset:46080\n\t"
      "v_mfma_f32_16x16x32_bf16 a[220:223], v[52:55], v[92:95], a[220:223]\n\t"
      "v_mfma_f32_16x16x32_bf16 a[224:227], v[56:59], v[68:71], a[224:227]\n\t"
      "ds_read_b64_tr_b16 v[52:53], v242 offset:41992\n\t"
      "v_mfma_f32_16x16x32_bf16 a[228:231], v[56:59], v[76:79], a[228:231]\n\t"
      "v_mfma_f32_16x16x32_bf16 a[232:235], v[56:59], v[84:87], a[232:235]\n\t"
      "ds_read_b64_tr_b16 v[54:55], v242 offset:46088\n\t"
      "v_cvt_pk_bf16_f32 v124, v216, v217\n\t"
      "v_cvt_pk_bf16_f32 v125, v218, v219\n\t"
      "v_mfma_f32_16x16x32_bf16 a[236:239], v[56:59], v[92:95], a[236:239]\n\t"
      "v_cvt_pk_bf16_f32 v126, v220, v221\n\t"
      "v_cvt_pk_bf16_f32 v127, v222, v223\n\t"
      "v_mfma_f32_16x16x32_bf16 a[240:243], v[60:63], v[68:71], a[240:243]\n\t"
      "ds_read_b64_tr_b16 v[56:57], v242 offset:42496\n\t"
      "v_mfma_f32_16x16x32_bf16 a[244:247], v[60:63], v[76:79], a[244:247]\n\t"
      "v_mfma_f32_16x16x32_bf16 a[248:251], v[60:63], v[84:87], a[248:251]\n\t"
      "ds_read_b64_tr_b16 v[58:59], v242 offset:46592\n\t"
      "v_mfma_f32_16x16x32_bf16 a[252:255], v[60:63], v[92:95], a[252:255]\n\t"
      "buffer_load_dwordx4 v[68:71], v246, s[44:47], s72 offen offset:1024 sc1\n\t"
      "buffer_load_dwordx4 v[76:79], v247, s[44:47], s72 offen offset:1024 sc1\n\t"
      "buffer_load_dwordx4 v[84:87], v248, s[44:47], s72 offen offset:1024 sc1\n\t"
      "buffer_load_dwordx4 v[92:95], v249, s[44:47], s72 offen offset:1024 sc1\n\t"
      "s_add_u32 s72, s72, 0x800\n\t"
      "s_waitcnt lgkmcnt(2)\n\t"
      "s_nop 0\n\t"
      "v_mfma_f32_16x16x32_bf16 a[128:131], v[0:3], v[96:99], a[128:131]\n\t"
      "ds_read_b64_tr_b16 v[60:61], v242 offset:42504\n\t"
      "v_mfma_f32_16x16x32_bf16 a[132:135], v[0:3], v[104:107], a[132:135]\n\t"
      "ds_read_b64_tr_b16 v[62:63], v242 offset:46600\n\t"
      "v_mfma_f32_16x16x32_bf16 a[136:139], v[0:3], v[112:115], a[136:139]\n\t"
      "ds_read_b64 v[160:161], v252 offset:10240\n\t"
      "v_mfma_f32_16x16x32_bf16 a[140:143], v[0:3], v[120:123], a[140:143]\n\t"
      "s_barrier\n\t"
      "s_nop 0\n\t"
      "ds_read_b64 v[164:165], v252 offset:10248\n\t"
      "v_mfma_f32_16x16x32_bf16 a[144:147], v[4:7], v[96:99], a[144:147]\n\t"
      "ds_read_b64_tr_b16 v[0:1], v243 offset:32768\n\t"
      "v_mfma_f32_16x16x32_bf16 a[148:151], v[4:7], v[104:107], a[148:151]\n\t"
      "s_add_u32 m0, s60, 16384\n\t"
      "ds_read_b128 v[168:171], v250 offset:2592\n\t"
      "v_mfma_f32_16x16x32_bf16 a[152:155], v[4:7], v[112:115], a[152:155]\n\t"
      "s_mov_b32 s62, 16384\n\t"
      "ds_read_b64_tr_b16 v[2:3], v243 offset:36864\n\t"
      "v_mfma_f32_16x16x32_bf16 a[156:159], v[4:7], v[120:123], a[156:159]\n\t"
      "ds_read_b128 v[172:175], v250 offset:2608\n\t"
      "v_mfma_f32_16x16x32_bf16 a[160:163], v[8:11], v[96:99], a[160:163]\n\t"
      "ds_read_b64_tr_b16 v[4:5], v243 offset:32776\n\t"
      "s_waitcnt vmcnt(16) lgkmcnt(1)\n\t"
      "s_nop 0\n\t"
      "v_mfma_f32_16x16x32_bf16 v[192:195], v[160:163], v[128:131], v[168:171]\n\t"
      "v_mfma_f32_16x16x32_bf16 a[164:167], v[8:11], v[104:107], a[164:167]\n\t"
      "ds_read_b64 v[176:177], v252 offset:11264\n\t"
      "v_mfma_f32_16x16x32_bf16 a[168:171], v[8:11], v[112:115], a[168:171]\n\t"
      "ds_read_b64_tr_b16 v[6:7], v243 offset:36872\n\t"
      "v_mfma_f32_16x16x32_bf16 a[172:175], v[8:11], v[120:123], a[172:175]\n\t"
      "ds_read_b64 v[180:181], v252 offset:11272\n\t"
      "v_mfma_f32_16x16x32_bf16 v[196:199], v[162:165], v[128:131], v[172:175]\n\t"
      "v_mfma_f32_16x16x32_bf16 a[176:179], v[12:15], v[96:99], a[176:179]\n\t"
      "ds_read_b64_tr_b16 v[8:9], v243 offset:33280\n\t"
      "v_mfma_f32_16x16x32_bf16 a[180:183], v[12:15], v[104:107], a[180:183]\n\t"
      "ds_read_b128 v[184:187], v250 offset:2848\n\t"
      "v_mfma_f32_16x16x32_bf16 a[184:187], v[12:15], v[112:115], a[184:187]\n\t"
      "ds_read_b64_tr_b16 v[10:11], v243 offset:37376\n\t"
      "v_mfma_f32_16x16x32_bf16 v[200:203], v[160:163], v[136:139], v[168:171]\n\t"
      "v_mfma_f32_16x16x32_bf16 a[188:191], v[12:15], v[120:123], a[188:191]\n\t"
      "s_waitcnt lgkmcnt(13)\n\t"
      "s_nop 0\n\t"
      "ds_read_b128 v[188:191], v250 offset:2864\n\t"
      "v_cvt_pk_bf16_f32 v128, v192, v193\n\t"
      "v_cvt_pk_bf16_f32 v129, v194, v195\n\t"
      "v_mfma_f32_16x16x32_bf16 a[192:195], v[16:19], v[96:99], a[192:195]\n\t"
      "ds_read_b64_tr_b16 v[12:13], v243 offset:33288\n\t"
      "v_cvt_pk_bf16_f32 v130, v196, v197\n\t"
      "v_cvt_pk_bf16_f32 v131, v198, v199\n\t"
      "v_mfma_f32_16x16x32_bf16 a[196:199], v[16:19], v[104:107], a[196:199]\n\t"
      "buffer_load_dwordx4 v239, s[36:39], s62 offen lds\n\t"
      "v_mfma_f32_16x16x32_bf16 v[204:207], v[162:165], v[136:139], v[172:175]\n\t"
      "v_mfma_f32_16x16x32_bf16 a[200:203], v[16:19], v[112:115], a[200:203]\n\t"
      "ds_read_b64_tr_b16 v[14:15], v243 offset:37384\n\t"
      "v_mfma_f32_16x16x32_bf16 a[204:207], v[16:19], v[120:123], a[204:207]\n\t"
      "buffer_load_dwordx4 v240, s[36:39], s62 offen offset:1024 lds\n\t"
      "v_mfma_f32_16x16x32_bf16 a[208:211], v[20:23], v[96:99], a[208:211]\n\t"
      "ds_read_b64_tr_b16 v[16:17], v243 offset:33792\n\t"
      "v_mfma_f32_16x16x32_bf16 v[208:211], v[160:163], v[144:147], v[168:171]\n\t"
      "v_mfma_f32_16x16x32_bf16 a[212:215], v[20:23], v[104:107], a[212:215]\n\t"
      "buffer_load_dwordx4 v239, s[36:39], s62 offen offset:2048 lds\n\t"
      "v_cvt_pk_bf16_f32 v136, v200, v201\n\t"
      "v_cvt_pk_bf16_f32 v137, v202, v203\n\t"
      "v_mfma_f32_16x16x32_bf16 a[216:219], v[20:23], v[112:115], a[216:219]\n\t"
      "ds_read_b64_tr_b16 v[18:19], v243 offset:37888\n\t"
      "v_cvt_pk_bf16_f32 v138, v204, v205\n\t"
      "v_cvt_pk_bf16_f32 v139, v206, v207\n\t"
      "v_mfma_f32_16x16x32_bf16 a[220:223], v[20:23], v[120:123], a[220:223]\n\t"
      "buffer_load_dwordx4 v240, s[36:39], s62 offen offset:3072 lds\n\t"
      "v_mfma_f32_16x16x32_bf16 v[212:215], v[162:165], v[144:147], v[172:175]\n\t"
      "v_mfma_f32_16x16x32_bf16 a[224:227], v[24:27], v[96:99], a[224:227]\n\t"
      "ds_read_b64_tr_b16 v[20:21], v243 offset:33800\n\t"
      "v_mfma_f32_16x16x32_bf16 a[228:231], v[24:27], v[104:107], a[228:231]\n\t"
      "v_mfma_f32_16x16x32_bf16 a[232:235], v[24:27], v[112:115], a[232:235]\n\t"
      "ds_read_b64_tr_b16 v[22:23], v243 offset:37896\n\t"
      "v_mfma_f32_16x16x32_bf16 v[216:219], v[160:163], v[152:155], v[168:171]\n\t"
      "v_mfma_f32_16x16x32_bf16 a[236:239], v[24:27], v[120:123], a[236:239]\n\t"
      "v_cvt_pk_bf16_f32 v144, v208, v209\n\t"
      "v_cvt_pk_bf16_f32 v145, v210, v211\n\t"
      "v_mfma_f32_16x16x32_bf16 a[240:243], v[28:31], v[96:99], a[240:243]\n\t"
      "ds_read_b64_tr_b16 v[24:25], v243 offset:34304\n\t"
      "v_cvt_pk_bf16_f32 v146, v212, v213\n\t"
      "v_cvt_pk_bf16_f32 v147, v214, v215\n\t"
      "v_mfma_f32_16x16x32_bf16 a[244:247], v[28:31], v[104:107], a[244:247]\n\t"
      "v_mfma_f32_16x16x32_bf16 v[220:223], v[162:165], v[152:155], v[172:175]\n\t"
      "v_mfma_f32_16x16x32_bf16 a[248:251], v[28:31], v[112:115], a[248:251]\n\t"
      "ds_read_b64_tr_b16 v[26:27], v243 offset:38400\n\t"
      "v_mfma_f32_16x16x32_bf16 a[252:255], v[28:31], v[120:123], a[252:255]\n\t"
      "v_mfma_f32_16x16x32_bf16 a[128:131], v[32:35], v[100:103], a[128:131]\n\t"
      "ds_read_b64_tr_b16 v[28:29], v243 offset:34312\n\t"
      "s_waitcnt vmcnt(16) lgkmcnt(9)\n\t"
      "s_nop 0\n\t"
      "v_mfma_f32_16x16x32_bf16 v[192:195], v[176:179], v[132:135], v[184:187]\n\t"
      "v_mfma_f32_16x16x32_bf16 a[132:135], v[32:35], v[108:111], a[132:135]\n\t"
      "buffer_load_dwordx4 v[96:99], v246, s[44:47], s72 offen offset:0 sc1\n\t"
      "v_cvt_pk_bf16_f32 v152, v216, v217\n\t"
      "v_cvt_pk_bf16_f32 v153, v218, v219\n\t"
      "v_mfma_f32_16x16x32_bf16 a[136:139], v[32:35], v[116:119], a[136:139]\n\t"
      "ds_read_b64_tr_b16 v[30:31], v243 offset:38408\n\t"
      "v_cvt_pk_bf16_f32 v154, v220, v221\n\t"
      "v_cvt_pk_bf16_f32 v155, v222, v223\n\t"
      "v_mfma_f32_16x16x32_bf16 a[140:143], v[32:35], v[124:127], a[140:143]\n\t"
      "buffer_load_dwordx4 v[104:107], v247, s[44:47], s72 offen offset:0 sc1\n\t"
      "v_mfma_f32_16x16x32_bf16 v[196:199], v[178:181], v[132:135], v[188:191]\n\t"
      "v_mfma_f32_16x16x32_bf16 a[144:147], v[36:39], v[100:103], a[144:147]\n\t"
      "ds_read_b64_tr_b16 v[32:33], v243 offset:40960\n\t"
      "v_mfma_f32_16x16x32_bf16 a[148:151], v[36:39], v[108:111], a[148:151]\n\t"
      "buffer_load_dwordx4 v[112:115], v248, s[44:47], s72 offen offset:0 sc1\n\t"
      "v_mfma_f32_16x16x32_bf16 a[152:155], v[36:39], v[116:119], a[152:155]\n\t"
      "ds_read_b64_tr_b16 v[34:35], v243 offset:45056\n\t"
      "v_mfma_f32_16x16x32_bf16 v[200:203], v[176:179], v[140:143], v[184:187]\n\t"
      "v_mfma_f32_16x16x32_bf16 a[156:159], v[36:39], v[124:127], a[156:159]\n\t"
      "buffer_load_dwordx4 v[120:123], v249, s[44:47], s72 offen offset:0 sc1\n\t"
      "v_cvt_pk_bf16_f32 v132, v192, v193\n\t"
      "v_cvt_pk_bf16_f32 v133, v194, v195\n\t"
      "v_mfma_f32_16x16x32_bf16 a[160:163], v[40:43], v[100:103], a[160:163]\n\t"
      "ds_read_b64_tr_b16 v[36:37], v243 offset:40968\n\t"
      "v_cvt_pk_bf16_f32 v134, v196, v197\n\t"
      "v_cvt_pk_bf16_f32 v135, v198, v199\n\t"
      "v_mfma_f32_16x16x32_bf16 a[164:167], v[40:43], v[108:111], a[164:167]\n\t"
      "v_mfma_f32_16x16x32_bf16 v[204:207], v[178:181], v[140:143], v[188:191]\n\t"
      "v_mfma_f32_16x16x32_bf16 a[168:171], v[40:43], v[116:119], a[168:171]\n\t"
      "ds_read_b64_tr_b16 v[38:39], v243 offset:45064\n\t"
      "v_mfma_f32_16x16x32_bf16 a[172:175], v[40:43], v[124:127], a[172:175]\n\t"
      "v_mfma_f32_16x16x32_bf16 a[176:179], v[44:47], v[100:103], a[176:179]\n\t"
      "ds_read_b64_tr_b16 v[40:41], v243 offset:41472\n\t"
      "v_mfma_f32_16x16x32_bf16 v[208:211], v[176:179], v[148:151], v[184:187]\n\t"
      "v_mfma_f32_16x16x32_bf16 a[180:183], v[44:47], v[108:111], a[180:183]\n\t"
      "v_cvt_pk_bf16_f32 v140, v200, v201\n\t"
      "v_cvt_pk_bf16_f32 v141, v202, v203\n\t"
      "v_mfma_f32_16x16x32_bf16 a[184:187], v[44:47], v[116:119], a[184:187]\n\t"
      "ds_read_b64_tr_b16 v[42:43], v243 offset:45568\n\t"
      "v_cvt_pk_bf16_f32 v142, v204, v205\n\t"
      "v_cvt_pk_bf16_f32 v143, v206, v207\n\t"
      "v_mfma_f32_16x16x32_bf16 a[188:191], v[44:47], v[124:127], a[188:191]\n\t"
      "v_mfma_f32_16x16x32_bf16 v[212:215], v[178:181], v[148:151], v[188:191]\n\t"
      "v_mfma_f32_16x16x32_bf16 a[192:195], v[48:51], v[100:103], a[192:195]\n\t"
      "ds_read_b64_tr_b16 v[44:45], v243 offset:41480\n\t"
      "v_mfma_f32_16x16x32_bf16 a[196:199], v[48:51], v[108:111], a[196:199]\n\t"
      "v_mfma_f32_16x16x32_bf16 a[200:203], v[48:51], v[116:119], a[200:203]\n\t"
      "ds_read_b64_tr_b16 v[46:47], v243 offset:45576\n\t"
      "v_mfma_f32_16x16x32_bf16 v[216:219], v[176:179], v[156:159], v[184:187]\n\t"
      "v_mfma_f32_16x16x32_bf16 a[204:207], v[48:51], v[124:127], a[204:207]\n\t"
      "v_cvt_pk_bf16_f32 v148, v208, v209\n\t"
      "v_cvt_pk_bf16_f32 v149, v210, v211\n\t"
      "v_mfma_f32_16x16x32_bf16 a[208:211], v[52:55], v[100:103], a[208:211]\n\t"
      "ds_read_b64_tr_b16 v[48:49], v243 offset:41984\n\t"
      "v_cvt_pk_bf16_f32 v150, v212, v213\n\t"
      "v_cvt_pk_bf16_f32 v151, v214, v215\n\t"
      "v_mfma_f32_16x16x32_bf16 a[212:215], v[52:55], v[108:111], a[212:215]\n\t"
      "v_mfma_f32_16x16x32_bf16 v[220:223], v[178:181], v[156:159], v[188:191]\n\t"
      "v_mfma_f32_16x16x32_bf16 a[216:219], v[52:55], v[116:119], a[216:219]\n\t"
      "ds_read_b64_tr_b16 v[50:51], v243 offset:46080\n\t"
      "v_mfma_f32_16x16x32_bf16 a[220:223], v[52:55], v[124:127], a[220:223]\n\t"
      "v_mfma_f32_16x16x32_bf16 a[224:227], v[56:59], v[100:103], a[224:227]\n\t"
      "ds_read_b64_tr_b16 v[52:53], v243 offset:41992\n\t"
      "v_mfma_f32_16x16x32_bf16 a[228:231], v[56:59], v[108:111], a[228:231]\n\t"
      "v_mfma_f32_16x16x32_bf16 a[232:235], v[56:59], v[116:119], a[232:235]\n\t"
      "ds_read_b64_tr_b16 v[54:55], v243 offset:46088\n\t"
      "v_cvt_pk_bf16_f32 v156, v216, v217\n\t"
      "v_cvt_pk_bf16_f32 v157, v218, v219\n\t"
      "v_mfma_f32_16x16x32_bf16 a[236:239], v[56:59], v[124:127], a[236:239]\n\t"
      "v_cvt_pk_bf16_f32 v158, v220, v221\n\t"
      "v_cvt_pk_bf16_f32 v159, v222, v223\n\t"
      "v_mfma_f32_16x16x32_bf16 a[240:243], v[60:63], v[100:103], a[240:243]\n\t"
      "ds_read_b64_tr_b16 v[56:57], v243 offset:42496\n\t"
      "v_mfma_f32_16x16x32_bf16 a[244:247], v[60:63], v[108:111], a[244:247]\n\t"
      "v_mfma_f32_16x16x32_bf16 a[248:251], v[60:63], v[116:119], a[248:251]\n\t"
      "ds_read_b64_tr_b16 v[58:59], v243 offset:46592\n\t"
      "v_mfma_f32_16x16x32_bf16 a[252:255], v[60:63], v[124:127], a[252:255]\n\t"
      "buffer_load_dwordx4 v[100:103], v246, s[44:47], s72 offen offset:1024 sc1\n\t"
      "buffer_load_dwordx4 v[108:111], v247, s[44:47], s72 offen offset:1024 sc1\n\t"
      "buffer_load_dwordx4 v[116:119], v248, s[44:47], s72 offen offset:1024 sc1\n\t"
      "buffer_load_dwordx4 v[124:127], v249, s[44:47], s72 offen offset:1024 sc1\n\t"
      "s_add_u32 s72, s72, 0x800\n\t"
      "s_waitcnt lgkmcnt(2)\n\t"
      "s_nop 0\n\t"
      "v_mfma_f32_16x16x32_bf16 a[128:131], v[0:3], v[128:131], a[128:131]\n\t"
      "ds_read_b64_tr_b16 v[60:61], v243 offset:42504\n\t"
      "v_mfma_f32_16x16x32_bf16 a[132:135], v[0:3], v[136:139], a[132:135]\n\t"
      "ds_read_b64_tr_b16 v[62:63], v243 offset:46600\n\t"
      "v_mfma_f32_16x16x32_bf16 a[136:139], v[0:3], v[144:147], a[136:139]\n\t"
      "s_waitcnt vmcnt(60)\n\t"
      "s_nop 0\n\t"
      "ds_read_b64 v[160:161], v252 offset:12288\n\t"
      "v_mfma_f32_16x16x32_bf16 a[140:143], v[0:3], v[152:155], a[140:143]\n\t"
      "s_barrier\n\t"
      "s_nop 0\n\t"
      "ds_read_b64 v[164:165], v252 offset:12296\n\t"
      "v_mfma_f32_16x16x32_bf16 a[144:147], v[4:7], v[128:131], a[144:147]\n\t"
      "ds_read_b64_tr_b16 v[0:1], v242 offset:49152\n\t"
      "v_mfma_f32_16x16x32_bf16 a[148:151], v[4:7], v[136:139], a[148:151]\n\t"
      "s_add_u32 m0, s59, 32768\n\t"
      "ds_read_b128 v[168:171], v250 offset:3104\n\t"
      "v_mfma_f32_16x16x32_bf16 a[152:155], v[4:7], v[144:147], a[152:155]\n\t"
      "s_mov_b32 s61, 32768\n\t"
      "ds_read_b64_tr_b16 v[2:3], v242 offset:53248\n\t"
      "v_mfma_f32_16x16x32_bf16 a[156:159], v[4:7], v[152:155], a[156:159]\n\t"
      "ds_read_b128 v[172:175], v250 offset:3120\n\t"
      "v_mfma_f32_16x16x32_bf16 a[160:163], v[8:11], v[128:131], a[160:163]\n\t"
      "ds_read_b64_tr_b16 v[4:5], v242 offset:49160\n\t"
      "s_waitcnt vmcnt(16) lgkmcnt(1)\n\t"
      "s_nop 0\n\t"
      "v_mfma_f32_16x16x32_bf16 v[192:195], v[160:163], v[64:67], v[168:171]\n\t"
      "v_mfma_f32_16x16x32_bf16 a[164:167], v[8:11], v[136:139], a[164:167]\n\t"
      "ds_read_b64 v[176:177], v252 offset:13312\n\t"
      "v_mfma_f32_16x16x32_bf16 a[168:171], v[8:11], v[144:147], a[168:171]\n\t"
      "ds_read_b64_tr_b16 v[6:7], v242 offset:53256\n\t"
      "v_mfma_f32_16x16x32_bf16 a[172:175], v[8:11], v[152:155], a[172:175]\n\t"
      "ds_read_b64 v[180:181], v252 offset:13320\n\t"
      "v_mfma_f32_16x16x32_bf16 v[196:199], v[162:165], v[64:67], v[172:175]\n\t"
      "v_mfma_f32_16x16x32_bf16 a[176:179], v[12:15], v[128:131], a[176:179]\n\t"
      "ds_read_b64_tr_b16 v[8:9], v242 offset:49664\n\t"
      "v_mfma_f32_16x16x32_bf16 a[180:183], v[12:15], v[136:139], a[180:183]\n\t"
      "ds_read_b128 v[184:187], v250 offset:3360\n\t"
      "v_mfma_f32_16x16x32_bf16 a[184:187], v[12:15], v[144:147], a[184:187]\n\t"
      "ds_read_b64_tr_b16 v[10:11], v242 offset:53760\n\t"
      "v_mfma_f32_16x16x32_bf16 v[200:203], v[160:163], v[72:75], v[168:171]\n\t"
      "v_mfma_f32_16x16x32_bf16 a[188:191], v[12:15], v[152:155], a[188:191]\n\t"
      "s_waitcnt lgkmcnt(13)\n\t"
      "s_nop 0\n\t"
      "ds_read_b128 v[188:191], v250 offset:3376\n\t"
      "v_cvt_pk_bf16_f32 v64, v192, v193\n\t"
      "v_cvt_pk_bf16_f32 v65, v194, v195\n\t"
      "v_mfma_f32_16x16x32_bf16 a[192:195], v[16:19], v[128:131], a[192:195]\n\t"
      "ds_read_b64_tr_b16 v[12:13], v242 offset:49672\n\t"
      "v_cvt_pk_bf16_f32 v66, v196, v197\n\t"
      "v_cvt_pk_bf16_f32 v67, v198, v199\n\t"
      "v_mfma_f32_16x16x32_bf16 a[196:199], v[16:19], v[136:139], a[196:199]\n\t"
      "buffer_load_dwordx4 v235, s[76:79], s61 offen lds\n\t"
      "v_mfma_f32_16x16x32_bf16 v[204:207], v[162:165], v[72:75], v[172:175]\n\t"
      "v_mfma_f32_16x16x32_bf16 a[200:203], v[16:19], v[144:147], a[200:203]\n\t"
      "ds_read_b64_tr_b16 v[14:15], v242 offset:53768\n\t"
      "v_mfma_f32_16x16x32_bf16 a[204:207], v[16:19], v[152:155], a[204:207]\n\t"
      "buffer_load_dwordx4 v236, s[76:79], s61 offen offset:1024 lds\n\t"
      "v_mfma_f32_16x16x32_bf16 a[208:211], v[20:23], v[128:131], a[208:211]\n\t"
      "ds_read_b64_tr_b16 v[16:17], v242 offset:50176\n\t"
      "v_mfma_f32_16x16x32_bf16 v[208:211], v[160:163], v[80:83], v[168:171]\n\t"
      "v_mfma_f32_16x16x32_bf16 a[212:215], v[20:23], v[136:139], a[212:215]\n\t"
      "buffer_load_dwordx4 v237, s[76:79], s61 offen offset:2048 lds\n\t"
      "v_cvt_pk_bf16_f32 v72, v200, v201\n\t"
      "v_cvt_pk_bf16_f32 v73, v202, v203\n\t"
      "v_mfma_f32_16x16x32_bf16 a[216:219], v[20:23], v[144:147], a[216:219]\n\t"
      "ds_read_b64_tr_b16 v[18:19], v242 offset:54272\n\t"
      "v_cvt_pk_bf16_f32 v74, v204, v205\n\t"
      "v_cvt_pk_bf16_f32 v75, v206, v207\n\t"
      "v_mfma_f32_16x16x32_bf16 a[220:223], v[20:23], v[152:155], a[220:223]\n\t"
      "buffer_load_dwordx4 v238, s[76:79], s61 offen offset:3072 lds\n\t"
      "v_mfma_f32_16x16x32_bf16 v[212:215], v[162:165], v[80:83], v[172:175]\n\t"
      "v_mfma_f32_16x16x32_bf16 a[224:227], v[24:27], v[128:131], a[224:227]\n\t"
      "ds_read_b64_tr_b16 v[20:21], v242 offset:50184\n\t"
      "v_mfma_f32_16x16x32_bf16 a[228:231], v[24:27], v[136:139], a[228:231]\n\t"
      "v_mfma_f32_16x16x32_bf16 a[232:235], v[24:27], v[144:147], a[232:235]\n\t"
      "ds_read_b64_tr_b16 v[22:23], v242 offset:54280\n\t"
      "v_mfma_f32_16x16x32_bf16 v[216:219], v[160:163], v[88:91], v[168:171]\n\t"
      "v_mfma_f32_16x16x32_bf16 a[236:239], v[24:27], v[152:155], a[236:239]\n\t"
      "v_cvt_pk_bf16_f32 v80, v208, v209\n\t"
      "v_cvt_pk_bf16_f32 v81, v210, v211\n\t"
      "v_mfma_f32_16x16x32_bf16 a[240:243], v[28:31], v[128:131], a[240:243]\n\t"
      "ds_read_b64_tr_b16 v[24:25], v242 offset:50688\n\t"
      "v_cvt_pk_bf16_f32 v82, v212, v213\n\t"
      "v_cvt_pk_bf16_f32 v83, v214, v215\n\t"
      "v_mfma_f32_16x16x32_bf16 a[244:247], v[28:31], v[136:139], a[244:247]\n\t"
      "v_mfma_f32_16x16x32_bf16 v[220:223], v[162:165], v[88:91], v[172:175]\n\t"
      "v_mfma_f32_16x16x32_bf16 a[248:251], v[28:31], v[144:147], a[248:251]\n\t"
      "ds_read_b64_tr_b16 v[26:27], v242 offset:54784\n\t"
      "v_mfma_f32_16x16x32_bf16 a[252:255], v[28:31], v[152:155], a[252:255]\n\t"
      "v_mfma_f32_16x16x32_bf16 a[128:131], v[32:35], v[132:135], a[128:131]\n\t"
      "ds_read_b64_tr_b16 v[28:29], v242 offset:50696\n\t"
      "s_waitcnt vmcnt(16) lgkmcnt(9)\n\t"
      "s_nop 0\n\t"
      "v_mfma_f32_16x16x32_bf16 v[192:195], v[176:179], v[68:71], v[184:187]\n\t"
      "v_mfma_f32_16x16x32_bf16 a[132:135], v[32:35], v[140:143], a[132:135]\n\t"
      "v_cvt_pk_bf16_f32 v88, v216, v217\n\t"
      "v_cvt_pk_bf16_f32 v89, v218, v219\n\t"
      "v_mfma_f32_16x16x32_bf16 a[136:139], v[32:35], v[148:151], a[136:139]\n\t"
      "ds_read_b64_tr_b16 v[30:31], v242 offset:54792\n\t"
      "v_cvt_pk_bf16_f32 v90, v220, v221\n\t"
      "v_cvt_pk_bf16_f32 v91, v222, v223\n\t"
      "v_mfma_f32_16x16x32_bf16 a[140:143], v[32:35], v[156:159], a[140:143]\n\t"
      "v_mfma_f32_16x16x32_bf16 v[196:199], v[178:181], v[68:71], v[188:191]\n\t"
      "v_mfma_f32_16x16x32_bf16 a[144:147], v[36:39], v[132:135], a[144:147]\n\t"
      "ds_read_b64_tr_b16 v[32:33], v242 offset:57344\n\t"
      "v_mfma_f32_16x16x32_bf16 a[148:151], v[36:39], v[140:143], a[148:151]\n\t"
      "v_mfma_f32_16x16x32_bf16 a[152:155], v[36:39], v[148:151], a[152:155]\n\t"
      "ds_read_b64_tr_b16 v[34:35], v242 offset:61440\n\t"
      "v_mfma_f32_16x16x32_bf16 v[200:203], v[176:179], v[76:79], v[184:187]\n\t"
      "v_mfma_f32_16x16x32_bf16 a[156:159], v[36:39], v[156:159], a[156:159]\n\t"
      "v_cvt_pk_bf16_f32 v68, v192, v193\n\t"
      "v_cvt_pk_bf16_f32 v69, v194, v195\n\t"
      "v_mfma_f32_16x16x32_bf16 a[160:163], v[40:43], v[132:135], a[160:163]\n\t"
      "ds_read_b64_tr_b16 v[36:37], v242 offset:57352\n\t"
      "v_cvt_pk_bf16_f32 v70, v196, v197\n\t"
      "v_cvt_pk_bf16_f32 v71, v198, v199\n\t"
      "v_mfma_f32_16x16x32_bf16 a[164:167], v[40:43], v[140:143], a[164:167]\n\t"
      "v_mfma_f32_16x16x32_bf16 v[204:207], v[178:181], v[76:79], v[188:191]\n\t"
      "v_mfma_f32_16x16x32_bf16 a[168:171], v[40:43], v[148:151], a[168:171]\n\t"
      "ds_read_b64_tr_b16 v[38:39], v242 offset:61448\n\t"
      "v_mfma_f32_16x16x32_bf16 a[172:175], v[40:43], v[156:159], a[172:175]\n\t"
      "v_mfma_f32_16x16x32_bf16 a[176:179], v[44:47], v[132:135], a[176:179]\n\t"
      "ds_read_b64_tr_b16 v[40:41], v242 offset:57856\n\t"
      "v_mfma_f32_16x16x32_bf16 v[208:211], v[176:179], v[84:87], v[184:187]\n\t"
      "v_mfma_f32_16x16x32_bf16 a[180:183], v[44:47], v[140:143], a[180:183]\n\t"
      "v_cvt_pk_bf16_f32 v76, v200, v201\n\t"
      "v_cvt_pk_bf16_f32 v77, v202, v203\n\t"
      "v_mfma_f32_16x16x32_bf16 a[184:187], v[44:47], v[148:151], a[184:187]\n\t"
      "ds_read_b64_tr_b16 v[42:43], v242 offset:61952\n\t"
      "v_cvt_pk_bf16_f32 v78, v204, v205\n\t"
      "v_cvt_pk_bf16_f32 v79, v206, v207\n\t"
      "v_mfma_f32_16x16x32_bf16 a[188:191], v[44:47], v[156:159], a[188:191]\n\t"
      "v_mfma_f32_16x16x32_bf16 v[212:215], v[178:181], v[84:87], v[188:191]\n\t"
      "v_mfma_f32_16x16x32_bf16 a[192:195], v[48:51], v[132:135], a[192:195]\n\t"
      "ds_read_b64_tr_b16 v[44:45], v242 offset:57864\n\t"
      "v_mfma_f32_16x16x32_bf16 a[196:199], v[48:51], v[140:143], a[196:199]\n\t"
      "v_mfma_f32_16x16x32_bf16 a[200:203], v[48:51], v[148:151], a[200:203]\n\t"
      "ds_read_b64_tr_b16 v[46:47], v242 offset:61960\n\t"
      "v_mfma_f32_16x16x32_bf16 v[216:219], v[176:179], v[92:95], v[184:187]\n\t"
      "v_mfma_f32_16x16x32_bf16 a[204:207], v[48:51], v[156:159], a[204:207]\n\t"
      "v_cvt_pk_bf16_f32 v84, v208, v209\n\t"
      "v_cvt_pk_bf16_f32 v85, v210, v211\n\t"
      "v_mfma_f32_16x16x32_bf16 a[208:211], v[52:55], v[132:135], a[208:211]\n\t"
      "ds_read_b64_tr_b16 v[48:49], v242 offset:58368\n\t"
      "v_cvt_pk_bf16_f32 v86, v212, v213\n\t"
      "v_cvt_pk_bf16_f32 v87, v214, v215\n\t"
      "v_mfma_f32_16x16x32_bf16 a[212:215], v[52:55], v[140:143], a[212:215]\n\t"
      "v_mfma_f32_16x16x32_bf16 v[220:223], v[178:181], v[92:95], v[188:191]\n\t"
      "v_mfma_f32_16x16x32_bf16 a[216:219], v[52:55], v[148:151], a[216:219]\n\t"
      "ds_read_b64_tr_b16 v[50:51], v242 offset:62464\n\t"
      "v_mfma_f32_16x16x32_bf16 a[220:223], v[52:55], v[156:159], a[220:223]\n\t"
      "v_mfma_f32_16x16x32_bf16 a[224:227], v[56:59], v[132:135], a[224:227]\n\t"
      "ds_read_b64_tr_b16 v[52:53], v242 offset:58376\n\t"
      "v_mfma_f32_16x16x32_bf16 a[228:231], v[56:59], v[140:143], a[228:231]\n\t"
      "v_mfma_f32_16x16x32_bf16 a[232:235], v[56:59], v[148:151], a[232:235]\n\t"
      "ds_read_b64_tr_b16 v[54:55], v242 offset:62472\n\t"
      "v_cvt_pk_bf16_f32 v92, v216, v217\n\t"
      "v_cvt_pk_bf16_f32 v93, v218, v219\n\t"
      "v_mfma_f32_16x16x32_bf16 a[236:239], v[56:59], v[156:159], a[236:239]\n\t"
      "v_cvt_pk_bf16_f32 v94, v220, v221\n\t"
      "v_cvt_pk_bf16_f32 v95, v222, v223\n\t"
      "v_mfma_f32_16x16x32_bf16 a[240:243], v[60:63], v[132:135], a[240:243]\n\t"
      "ds_read_b64_tr_b16 v[56:57], v242 offset:58880\n\t"
      "v_mfma_f32_16x16x32_bf16 a[244:247], v[60:63], v[140:143], a[244:247]\n\t"
      "v_mfma_f32_16x16x32_bf16 a[248:251], v[60:63], v[148:151], a[248:251]\n\t"
      "ds_read_b64_tr_b16 v[58:59], v242 offset:62976\n\t"
      "v_mfma_f32_16x16x32_bf16 a[252:255], v[60:63], v[156:159], a[252:255]\n\t"
      "s_waitcnt lgkmcnt(2)\n\t"
      "s_nop 0\n\t"
      "v_mfma_f32_16x16x32_bf16 a[128:131], v[0:3], v[64:67], a[128:131]\n\t"
      "ds_read_b64_tr_b16 v[60:61], v242 offset:58888\n\t"
      "v_mfma_f32_16x16x32_bf16 a[132:135], v[0:3], v[72:75], a[132:135]\n\t"
      "ds_read_b64_tr_b16 v[62:63], v242 offset:62984\n\t"
      "v_mfma_f32_16x16x32_bf16 a[136:139], v[0:3], v[80:83], a[136:139]\n\t"
      "s_waitcnt vmcnt(60)\n\t"
      "s_nop 0\n\t"
      "ds_read_b64 v[160:161], v252 offset:14336\n\t"
      "v_mfma_f32_16x16x32_bf16 a[140:143], v[0:3], v[88:91], a[140:143]\n\t"
      "s_barrier\n\t"
      "s_nop 0\n\t"
      "ds_read_b64 v[164:165], v252 offset:14344\n\t"
      "v_mfma_f32_16x16x32_bf16 a[144:147], v[4:7], v[64:67], a[144:147]\n\t"
      "ds_read_b64_tr_b16 v[0:1], v243 offset:49152\n\t"
      "v_mfma_f32_16x16x32_bf16 a[148:151], v[4:7], v[72:75], a[148:151]\n\t"
      "s_add_u32 m0, s60, 32768\n\t"
      "ds_read_b128 v[168:171], v250 offset:3616\n\t"
      "v_mfma_f32_16x16x32_bf16 a[152:155], v[4:7], v[80:83], a[152:155]\n\t"
      "s_mov_b32 s62, 32768\n\t"
      "ds_read_b64_tr_b16 v[2:3], v243 offset:53248\n\t"
      "v_mfma_f32_16x16x32_bf16 a[156:159], v[4:7], v[88:91], a[156:159]\n\t"
      "ds_read_b128 v[172:175], v250 offset:3632\n\t"
      "v_mfma_f32_16x16x32_bf16 a[160:163], v[8:11], v[64:67], a[160:163]\n\t"
      "ds_read_b64_tr_b16 v[4:5], v243 offset:49160\n\t"
      "s_waitcnt vmcnt(8) lgkmcnt(1)\n\t"
      "s_nop 0\n\t"
      "v_mfma_f32_16x16x32_bf16 v[192:195], v[160:163], v[96:99], v[168:171]\n\t"
      "v_mfma_f32_16x16x32_bf16 a[164:167], v[8:11], v[72:75], a[164:167]\n\t"
      "ds_read_b64 v[176:177], v252 offset:15360\n\t"
      "v_mfma_f32_16x16x32_bf16 a[168:171], v[8:11], v[80:83], a[168:171]\n\t"
      "ds_read_b64_tr_b16 v[6:7], v243 offset:53256\n\t"
      "v_mfma_f32_16x16x32_bf16 a[172:175], v[8:11], v[88:91], a[172:175]\n\t"
      "ds_read_b64 v[180:181], v252 offset:15368\n\t"
      "v_mfma_f32_16x16x32_bf16 v[196:199], v[162:165], v[96:99], v[172:175]\n\t"
      "v_mfma_f32_16x16x32_bf16 a[176:179], v[12:15], v[64:67], a[176:179]\n\t"
      "ds_read_b64_tr_b16 v[8:9], v243 offset:49664\n\t"
      "v_mfma_f32_16x16x32_bf16 a[180:183], v[12:15], v[72:75], a[180:183]\n\t"
      "ds_read_b128 v[184:187], v250 offset:3872\n\t"
      "v_mfma_f32_16x16x32_bf16 a[184:187], v[12:15], v[80:83], a[184:187]\n\t"
      "ds_read_b64_tr_b16 v[10:11], v243 offset:53760\n\t"
      "v_mfma_f32_16x16x32_bf16 v[200:203], v[160:163], v[104:107], v[168:171]\n\t"
      "v_mfma_f32_16x16x32_bf16 a[188:191], v[12:15], v[88:91], a[188:191]\n\t"
      "s_waitcnt lgkmcnt(13)\n\t"
      "s_nop 0\n\t"
      "ds_read_b128 v[188:191], v250 offset:3888\n\t"
      "v_cvt_pk_bf16_f32 v96, v192, v193\n\t"
      "v_cvt_pk_bf16_f32 v97, v194, v195\n\t"
      "v_mfma_f32_16x16x32_bf16 a[192:195], v[16:19], v[64:67], a[192:195]\n\t"
      "ds_read_b64_tr_b16 v[12:13], v243 offset:49672\n\t"
      "v_cvt_pk_bf16_f32 v98, v196, v197\n\t"
      "v_cvt_pk_bf16_f32 v99, v198, v199\n\t"
      "v_mfma_f32_16x16x32_bf16 a[196:199], v[16:19], v[72:75], a[196:199]\n\t"
      "buffer_load_dwordx4 v239, s[36:39], s62 offen lds\n\t"
      "v_mfma_f32_16x16x32_bf16 v[204:207], v[162:165], v[104:107], v[172:175]\n\t"
      "v_mfma_f32_16x16x32_bf16 a[200:203], v[16:19], v[80:83], a[200:203]\n\t"
      "ds_read_b64_tr_b16 v[14:15], v243 offset:53768\n\t"
      "v_mfma_f32_16x16x32_bf16 a[204:207], v[16:19], v[88:91], a[204:207]\n\t"
      "buffer_load_dwordx4 v240, s[36:39], s62 offen offset:1024 lds\n\t"
      "v_mfma_f32_16x16x32_bf16 a[208:211], v[20:23], v[64:67], a[208:211]\n\t"
      "ds_read_b64_tr_b16 v[16:17], v243 offset:50176\n\t"
      "v_mfma_f32_16x16x32_bf16 v[208:211], v[160:163], v[112:115], v[168:171]\n\t"
      "v_mfma_f32_16x16x32_bf16 a[212:215], v[20:23], v[72:75], a[212:215]\n\t"
      "buffer_load_dwordx4 v239, s[36:39], s62 offen offset:2048 lds\n\t"
      "v_cvt_pk_bf16_f32 v104, v200, v201\n\t"
      "v_cvt_pk_bf16_f32 v105, v202, v203\n\t"
      "v_mfma_f32_16x16x32_bf16 a[216:219], v[20:23], v[80:83], a[216:219]\n\t"
      "ds_read_b64_tr_b16 v[18:19], v243 offset:54272\n\t"
      "v_cvt_pk_bf16_f32 v106, v204, v205\n\t"
      "v_cvt_pk_bf16_f32 v107, v206, v207\n\t"
      "v_mfma_f32_16x16x32_bf16 a[220:223], v[20:23], v[88:91], a[220:223]\n\t"
      "buffer_load_dwordx4 v240, s[36:39], s62 offen offset:3072 lds\n\t"
      "v_mfma_f32_16x16x32_bf16 v[212:215], v[162:165], v[112:115], v[172:175]\n\t"
      "v_mfma_f32_16x16x32_bf16 a[224:227], v[24:27], v[64:67], a[224:227]\n\t"
      "ds_read_b64_tr_b16 v[20:21], v243 offset:50184\n\t"
      "v_mfma_f32_16x16x32_bf16 a[228:231], v[24:27], v[72:75], a[228:231]\n\t"
      "v_mfma_f32_16x16x32_bf16 a[232:235], v[24:27], v[80:83], a[232:235]\n\t"
      "ds_read_b64_tr_b16 v[22:23], v243 offset:54280\n\t"
      "v_mfma_f32_16x16x32_bf16 v[216:219], v[160:163], v[120:123], v[168:171]\n\t"
      "v_mfma_f32_16x16x32_bf16 a[236:239], v[24:27], v[88:91], a[236:239]\n\t"
      "v_cvt_pk_bf16_f32 v112, v208, v209\n\t"
      "v_cvt_pk_bf16_f32 v113, v210, v211\n\t"
      "v_mfma_f32_16x16x32_bf16 a[240:243], v[28:31], v[64:67], a[240:243]\n\t"
      "ds_read_b64_tr_b16 v[24:25], v243 offset:50688\n\t"
      "v_cvt_pk_bf16_f32 v114, v212, v213\n\t"
      "v_cvt_pk_bf16_f32 v115, v214, v215\n\t"
      "v_mfma_f32_16x16x32_bf16 a[244:247], v[28:31], v[72:75], a[244:247]\n\t"
      "v_mfma_f32_16x16x32_bf16 v[220:223], v[162:165], v[120:123], v[172:175]\n\t"
      "v_mfma_f32_16x16x32_bf16 a[248:251], v[28:31], v[80:83], a[248:251]\n\t"
      "ds_read_b64_tr_b16 v[26:27], v243 offset:54784\n\t"
      "v_mfma_f32_16x16x32_bf16 a[252:255], v[28:31], v[88:91], a[252:255]\n\t"
      "v_mfma_f32_16x16x32_bf16 a[128:131], v[32:35], v[68:71], a[128:131]\n\t"
      "ds_read_b64_tr_b16 v[28:29], v243 offset:50696\n\t"
      "s_waitcnt vmcnt(8) lgkmcnt(9)\n\t"
      "s_nop 0\n\t"
      "v_mfma_f32_16x16x32_bf16 v[192:195], v[176:179], v[100:103], v[184:187]\n\t"
      "v_mfma_f32_16x16x32_bf16 a[132:135], v[32:35], v[76:79], a[132:135]\n\t"
      "v_cvt_pk_bf16_f32 v120, v216, v217\n\t"
      "v_cvt_pk_bf16_f32 v121, v218, v219\n\t"
      "v_mfma_f32_16x16x32_bf16 a[136:139], v[32:35], v[84:87], a[136:139]\n\t"
      "ds_read_b64_tr_b16 v[30:31], v243 offset:54792\n\t"
      "v_cvt_pk_bf16_f32 v122, v220, v221\n\t"
      "v_cvt_pk_bf16_f32 v123, v222, v223\n\t"
      "v_mfma_f32_16x16x32_bf16 a[140:143], v[32:35], v[92:95], a[140:143]\n\t"
      "v_mfma_f32_16x16x32_bf16 v[196:199], v[178:181], v[100:103], v[188:191]\n\t"
      "v_mfma_f32_16x16x32_bf16 a[144:147], v[36:39], v[68:71], a[144:147]\n\t"
      "ds_read_b64_tr_b16 v[32:33], v243 offset:57344\n\t"
      "v_mfma_f32_16x16x32_bf16 a[148:151], v[36:39], v[76:79], a[148:151]\n\t"
      "v_mfma_f32_16x16x32_bf16 a[152:155], v[36:39], v[84:87], a[152:155]\n\t"
      "ds_read_b64_tr_b16 v[34:35], v243 offset:61440\n\t"
      "v_mfma_f32_16x16x32_bf16 v[200:203], v[176:179], v[108:111], v[184:187]\n\t"
      "v_mfma_f32_16x16x32_bf16 a[156:159], v[36:39], v[92:95], a[156:159]\n\t"
      "v_cvt_pk_bf16_f32 v100, v192, v193\n\t"
      "v_cvt_pk_bf16_f32 v101, v194, v195\n\t"
      "v_mfma_f32_16x16x32_bf16 a[160:163], v[40:43], v[68:71], a[160:163]\n\t"
      "ds_read_b64_tr_b16 v[36:37], v243 offset:57352\n\t"
      "v_cvt_pk_bf16_f32 v102, v196, v197\n\t"
      "v_cvt_pk_bf16_f32 v103, v198, v199\n\t"
      "v_mfma_f32_16x16x32_bf16 a[164:167], v[40:43], v[76:79], a[164:167]\n\t"
      "v_mfma_f32_16x16x32_bf16 v[204:207], v[178:181], v[108:111], v[188:191]\n\t"
      "v_mfma_f32_16x16x32_bf16 a[168:171], v[40:43], v[84:87], a[168:171]\n\t"
      "ds_read_b64_tr_b16 v[38:39], v243 offset:61448\n\t"
      "v_mfma_f32_16x16x32_bf16 a[172:175], v[40:43], v[92:95], a[172:175]\n\t"
      "v_mfma_f32_16x16x32_bf16 a[176:179], v[44:47], v[68:71], a[176:179]\n\t"
      "ds_read_b64_tr_b16 v[40:41], v243 offset:57856\n\t"
      "v_mfma_f32_16x16x32_bf16 v[208:211], v[176:179], v[116:119], v[184:187]\n\t"
      "v_mfma_f32_16x16x32_bf16 a[180:183], v[44:47], v[76:79], a[180:183]\n\t"
      "v_cvt_pk_bf16_f32 v108, v200, v201\n\t"
      "v_cvt_pk_bf16_f32 v109, v202, v203\n\t"
      "v_mfma_f32_16x16x32_bf16 a[184:187], v[44:47], v[84:87], a[184:187]\n\t"
      "ds_read_b64_tr_b16 v[42:43], v243 offset:61952\n\t"
      "v_cvt_pk_bf16_f32 v110, v204, v205\n\t"
      "v_cvt_pk_bf16_f32 v111, v206, v207\n\t"
      "v_mfma_f32_16x16x32_bf16 a[188:191], v[44:47], v[92:95], a[188:191]\n\t"
      "v_mfma_f32_16x16x32_bf16 v[212:215], v[178:181], v[116:119], v[188:191]\n\t"
      "v_mfma_f32_16x16x32_bf16 a[192:195], v[48:51], v[68:71], a[192:195]\n\t"
      "ds_read_b64_tr_b16 v[44:45], v243 offset:57864\n\t"
      "v_mfma_f32_16x16x32_bf16 a[196:199], v[48:51], v[76:79], a[196:199]\n\t"
      "v_mfma_f32_16x16x32_bf16 a[200:203], v[48:51], v[84:87], a[200:203]\n\t"
      "ds_read_b64_tr_b16 v[46:47], v243 offset:61960\n\t"
      "v_mfma_f32_16x16x32_bf16 v[216:219], v[176:179], v[124:127], v[184:187]\n\t"
      "v_mfma_f32_16x16x32_bf16 a[204:207], v[48:51], v[92:95], a[204:207]\n\t"
      "v_cvt_pk_bf16_f32 v116, v208, v209\n\t"
      "v_cvt_pk_bf16_f32 v117, v210, v211\n\t"
      "v_mfma_f32_16x16x32_bf16 a[208:211], v[52:55], v[68:71], a[208:211]\n\t"
      "ds_read_b64_tr_b16 v[48:49], v243 offset:58368\n\t"
      "v_cvt_pk_bf16_f32 v118, v212, v213\n\t"
      "v_cvt_pk_bf16_f32 v119, v214, v215\n\t"
      "v_mfma_f32_16x16x32_bf16 a[212:215], v[52:55], v[76:79], a[212:215]\n\t"
      "v_mfma_f32_16x16x32_bf16 v[220:223], v[178:181], v[124:127], v[188:191]\n\t"
      "v_mfma_f32_16x16x32_bf16 a[216:219], v[52:55], v[84:87], a[216:219]\n\t"
      "ds_read_b64_tr_b16 v[50:51], v243 offset:62464\n\t"
      "v_mfma_f32_16x16x32_bf16 a[220:223], v[52:55], v[92:95], a[220:223]\n\t"
      "v_mfma_f32_16x16x32_bf16 a[224:227], v[56:59], v[68:71], a[224:227]\n\t"
      "ds_read_b64_tr_b16 v[52:53], v243 offset:58376\n\t"
      "v_mfma_f32_16x16x32_bf16 a[228:231], v[56:59], v[76:79], a[228:231]\n\t"
      "v_mfma_f32_16x16x32_bf16 a[232:235], v[56:59], v[84:87], a[232:235]\n\t"
      "ds_read_b64_tr_b16 v[54:55], v243 offset:62472\n\t"
      "v_cvt_pk_bf16_f32 v124, v216, v217\n\t"
      "v_cvt_pk_bf16_f32 v125, v218, v219\n\t"
      "v_mfma_f32_16x16x32_bf16 a[236:239], v[56:59], v[92:95], a[236:239]\n\t"
      "v_cvt_pk_bf16_f32 v126, v220, v221\n\t"
      "v_cvt_pk_bf16_f32 v127, v222, v223\n\t"
      "v_mfma_f32_16x16x32_bf16 a[240:243], v[60:63], v[68:71], a[240:243]\n\t"
      "ds_read_b64_tr_b16 v[56:57], v243 offset:58880\n\t"
      "v_mfma_f32_16x16x32_bf16 a[244:247], v[60:63], v[76:79], a[244:247]\n\t"
      "v_mfma_f32_16x16x32_bf16 a[248:251], v[60:63], v[84:87], a[248:251]\n\t"
      "ds_read_b64_tr_b16 v[58:59], v243 offset:62976\n\t"
      "v_mfma_f32_16x16x32_bf16 a[252:255], v[60:63], v[92:95], a[252:255]\n\t"
      "s_waitcnt lgkmcnt(2)\n\t"
      "s_nop 0\n\t"
      "v_mfma_f32_16x16x32_bf16 a[128:131], v[0:3], v[96:99], a[128:131]\n\t"
      "ds_read_b64_tr_b16 v[60:61], v243 offset:58888\n\t"
      "v_mfma_f32_16x16x32_bf16 a[132:135], v[0:3], v[104:107], a[132:135]\n\t"
      "ds_read_b64_tr_b16 v[62:63], v243 offset:62984\n\t"
      "v_mfma_f32_16x16x32_bf16 a[136:139], v[0:3], v[112:115], a[136:139]\n\t"
      "s_waitcnt vmcnt(40)\n\t"
      "s_nop 0\n\t"
      "v_mfma_f32_16x16x32_bf16 a[140:143], v[0:3], v[120:123], a[140:143]\n\t"
      "s_barrier\n\t"
      "s_nop 0\n\t"
      "v_mfma_f32_16x16x32_bf16 a[144:147], v[4:7], v[96:99], a[144:147]\n\t"
      "s_add_u32 m0, s59, 49152\n\t"
      "s_mov_b32 s61, 49152\n\t"
      "buffer_load_dwordx4 v235, s[76:79], s61 offen lds\n\t"
      "buffer_load_dwordx4 v236, s[76:79], s61 offen offset:1024 lds\n\t"
      "buffer_load_dwordx4 v237, s[76:79], s61 offen offset:2048 lds\n\t"
      "buffer_load_dwordx4 v238, s[76:79], s61 offen offset:3072 lds\n\t"
      "s_lshl_b32 s69, %6, 14\n\t"
      "s_nop 0\n\t"
      "v_mfma_f32_16x16x32_bf16 a[148:151], v[4:7], v[104:107], a[148:151]\n\t"
      "s_lshl_b32 s70, s56, 13\n\t"
      "s_add_u32 s69, s69, s70\n\t"
      "s_mul_i32 s68, %6, 7168\n\t"
      "s_movk_i32 s70, 0x2000\n\t"
      "s_movk_i32 s67, 7168\n\t"
      "s_cmp_lt_u32 s56, 2\n\t"
      "s_cselect_b32 s69, s69, s68\n\t"
      "v_mfma_f32_16x16x32_bf16 a[152:155], v[4:7], v[112:115], a[152:155]\n\t"
      "s_cselect_b32 s54, s70, s67\n\t"
      "s_cselect_b64 s[52:53], %5, %9\n\t"
      "s_movk_i32 s75, 0x400\n\t"
      "s_cmp_eq_u32 s56, 3\n\t"
      "s_cselect_b32 s54, 0, s54\n\t"
      "v_mov_b32_e64 v250, v246\n\t"
      "s_add_u32 s52, s52, s69\n\t"
      "v_mfma_f32_16x16x32_bf16 a[156:159], v[4:7], v[120:123], a[156:159]\n\t"
      "s_addc_u32 s53, s53, 0\n\t"
      "s_nop 0\n\t"
      "s_and_b32 s53, s53, 0xffff\n\t"
      "s_mov_b32 s71, 0\n\t"
      "s_lshr_b32 s70, s57, 2\n\t"
      "s_lshl_b32 s68, s70, 19\n\t"
      "s_lshl_b32 s69, s74, 8\n\t"
      "s_add_u32 s68, s68, s69\n\t"
      "s_nop 0\n\t"
      "v_mfma_f32_16x16x32_bf16 a[128:131], v[32:35], v[100:103], a[128:131]\n\t"
      "s_sub_u32 s69, 0x400000, s68\n\t"
      "s_mov_b64 s[40:41], %5\n\t"
      "s_add_u32 s40, s40, s68\n\t"
      "s_addc_u32 s41, s41, 0\n\t"
      "s_nop 0\n\t"
      "s_and_b32 s41, s41, 0xffff\n\t"
      "s_mov_b32 s42, s69\n\t"
      "s_nop 0\n\t"
      "s_mov_b32 s43, 0x20000\n\t"
      "v_mfma_f32_16x16x32_bf16 a[132:135], v[32:35], v[108:111], a[132:135]\n\t"
      "s_lshl_b32 s67, s58, 2\n\t"
      "s_add_u32 s67, s67, s56\n\t"
      "s_lshl_b32 s68, s67, 19\n\t"
      "s_lshl_b32 s69, s70, 14\n\t"
      "s_add_u32 s68, s68, s69\n\t"
      "s_nop 0\n\t"
      "s_sub_u32 s69, 0x80000, s69\n\t"
      "s_mov_b64 s[44:45], %1\n\t"
      "s_nop 0\n\t"
      "v_mfma_f32_16x16x32_bf16 a[136:139], v[32:35], v[116:119], a[136:139]\n\t"
      "s_add_u32 s44, s44, 33554432\n\t"
      "s_addc_u32 s45, s45, 0\n\t"
      "s_add_u32 s44, s44, s68\n\t"
      "s_addc_u32 s45, s45, 0\n\t"
      "s_nop 0\n\t"
      "s_and_b32 s45, s45, 0xffff\n\t"
      "s_mov_b32 s46, s69\n\t"
      "s_nop 0\n\t"
      "s_mov_b32 s47, 0x20000\n\t"
      "v_mfma_f32_16x16x32_bf16 a[140:143], v[32:35], v[124:127], a[140:143]\n\t"
      "s_lshl_b32 s68, s58, 3\n\t"
      "s_add_u32 s68, s68, s70\n\t"
      "s_lshl_b32 s68, s68, 7\n\t"
      "s_mov_b64 s[80:81], %1\n\t"
      "s_add_u32 s80, s80, 24576\n\t"
      "s_addc_u32 s81, s81, 0\n\t"
      "s_add_u32 s80, s80, s68\n\t"
      "v_mfma_f32_16x16x32_bf16 a[144:147], v[36:39], v[100:103], a[144:147]\n\t"
      "s_addc_u32 s81, s81, 0\n\t"
      "s_nop 0\n\t"
      "s_and_b32 s81, s81, 0xffff\n\t"
      "s_mov_b32 s82, 16\n\t"
      "s_mov_b32 s83, 0x20000\n\t"
      "v_lshlrev_b32_e64 v246, 4, v241\n\t"
      "v_add_u32_e32 v247, 0x20000, v246\n\t"
      "v_add_u32_e32 v248, 0x40000, v246\n\t"
      "v_mfma_f32_16x16x32_bf16 a[148:151], v[36:39], v[108:111], a[148:151]\n\t"
      "v_add_u32_e32 v249, 0x60000, v246\n\t"
      "v_bfe_u32 v218, v241, 1, 3\n\t"
      "v_lshrrev_b32_e32 v219, 4, v241\n\t"
      "v_and_b32_e32 v220, 1, v241\n\t"
      "v_lshlrev_b32_e64 v221, 5, v219\n\t"
      "v_lshl_add_u32 v221, v220, 4, v221\n\t"
      "v_lshl_add_u32 v244, v218, 10, v221\n\t"
      "v_mfma_f32_16x16x32_bf16 a[152:155], v[36:39], v[116:119], a[152:155]\n\t"
      "s_lshl_b32 s68, s56, 14\n\t"
      "v_add_u32_e32 v244, s68, v244\n\t"
      "v_add_u32_e32 v245, 0x80, v244\n\t"
      "v_lshrrev_b32_e32 v218, 5, v241\n\t"
      "v_lshlrev_b32_e32 v218, 11, v218\n\t"
      "v_bfe_u32 v220, v241, 2, 3\n\t"
      "v_lshl_add_u32 v218, v220, 5, v218\n\t"
      "v_mfma_f32_16x16x32_bf16 a[156:159], v[36:39], v[124:127], a[156:159]\n\t"
      "s_waitcnt lgkmcnt(0)\n\t"
      "v_and_b32_e32 v220, 1, v241\n\t"
      "v_lshl_add_u32 v242, v220, 4, v218\n\t"
      "v_bfe_u32 v220, v241, 1, 1\n\t"
      "v_lshl_add_u32 v242, v220, 8, v242\n\t"
      "v_add_u32_e32 v243, 0x10000, v242\n\t"
      "s_lshl_b32 s72, s74, 12\n\t"
      "s_nop 0\n\t"
      "s_add_u32 s72, s72, 0x1000\n\t"
      "v_mfma_f32_16x16x32_bf16 a[160:163], v[8:11], v[96:99], a[160:163]\n\t"
      "s_and_b32 s72, s72, 0x3fff\n\t"
      "s_mov_b32 s61, 65536\n\t"
      "s_mov_b32 s62, 49152\n\t"
      "v_and_b32_e32 v222, 15, v241\n\t"
      "v_cmp_eq_u32_e32 vcc, 0, v222\n\t"
      "v_mov_b32_e32 v223, 0x3f803f80\n\t"
      "s_nop 0\n\t"
      "s_nop 0\n\t"
      "v_mfma_f32_16x16x32_bf16 a[164:167], v[8:11], v[104:107], a[164:167]\n\t"
      "v_cndmask_b32_e32 v222, 0, v223, vcc\n\t"
      "s_nop 0\n\t"
      "v_mfma_f32_16x16x32_bf16 a[168:171], v[8:11], v[112:115], a[168:171]\n\t"
      "v_mfma_f32_16x16x32_bf16 a[172:175], v[8:11], v[120:123], a[172:175]\n\t"
      "v_accvgpr_read_b32 v64, a128\n\t"
      "v_accvgpr_read_b32 v65, a129\n\t"
      "v_accvgpr_read_b32 v66, a130\n\t"
      "v_accvgpr_read_b32 v67, a131\n\t"
      "v_accvgpr_read_b32 v68, a144\n\t"
      "v_mfma_f32_16x16x32_bf16 a[176:179], v[12:15], v[96:99], a[176:179]\n\t"
      "v_accvgpr_read_b32 v69, a145\n\t"
      "v_accvgpr_read_b32 v70, a146\n\t"
      "v_accvgpr_read_b32 v71, a147\n\t"
      "v_accvgpr_read_b32 v72, a132\n\t"
      "v_accvgpr_read_b32 v73, a133\n\t"
      "v_mfma_f32_16x16x32_bf16 a[180:183], v[12:15], v[104:107], a[180:183]\n\t"
      "v_accvgpr_read_b32 v74, a134\n\t"
      "v_accvgpr_read_b32 v75, a135\n\t"
      "v_accvgpr_read_b32 v76, a148\n\t"
      "v_accvgpr_read_b32 v77, a149\n\t"
      "v_accvgpr_read_b32 v78, a150\n\t"
      "v_mfma_f32_16x16x32_bf16 a[184:187], v[12:15], v[112:115], a[184:187]\n\t"
      "v_accvgpr_read_b32 v79, a151\n\t"
      "v_accvgpr_read_b32 v80, a136\n\t"
      "v_accvgpr_read_b32 v81, a137\n\t"
      "v_accvgpr_read_b32 v82, a138\n\t"
      "v_accvgpr_read_b32 v83, a139\n\t"
      "v_mfma_f32_16x16x32_bf16 a[188:191], v[12:15], v[120:123], a[188:191]\n\t"
      "v_accvgpr_read_b32 v84, a152\n\t"
      "v_accvgpr_read_b32 v85, a153\n\t"
      "v_accvgpr_read_b32 v86, a154\n\t"
      "v_accvgpr_read_b32 v87, a155\n\t"
      "v_accvgpr_read_b32 v88, a140\n\t"
      "v_mfma_f32_16x16x32_bf16 a[160:163], v[40:43], v[100:103], a[160:163]\n\t"
      "v_accvgpr_read_b32 v89, a141\n\t"
      "v_accvgpr_read_b32 v90, a142\n\t"
      "v_accvgpr_read_b32 v91, a143\n\t"
      "v_accvgpr_read_b32 v92, a156\n\t"
      "v_accvgpr_read_b32 v93, a157\n\t"
      "v_mfma_f32_16x16x32_bf16 a[164:167], v[40:43], v[108:111], a[164:167]\n\t"
      "v_accvgpr_read_b32 v94, a158\n\t"
      "v_accvgpr_read_b32 v95, a159\n\t"
      "ds_read_b128 a[128:131], v230 offset:0\n\t"
      "ds_read_b128 a[132:135], v231 offset:0\n\t"
      "ds_read_b128 a[136:139], v232 offset:0\n\t"
      "v_mfma_f32_16x16x32_bf16 a[168:171], v[40:43], v[116:119], a[168:171]\n\t"
      "ds_read_b128 a[140:143], v233 offset:0\n\t"
      "ds_read_b128 a[144:147], v230 offset:4096\n\t"
      "ds_read_b128 a[148:151], v231 offset:4096\n\t"
      "ds_read_b128 a[152:155], v232 offset:4096\n\t"
      "ds_read_b128 a[156:159], v233 offset:4096\n\t"
      "v_mfma_f32_16x16x32_bf16 a[172:175], v[40:43], v[124:127], a[172:175]\n\t"
      "v_mul_f32_e32 v64, s66, v64\n\t"
      "v_mul_f32_e32 v65, s66, v65\n\t"
      "v_mul_f32_e32 v66, s66, v66\n\t"
      "v_mul_f32_e32 v67, s66, v67\n\t"
      "v_mul_f32_e64 v68, s66, v68\n\t"
      "v_mfma_f32_16x16x32_bf16 a[176:179], v[44:47], v[100:103], a[176:179]\n\t"
      "v_mul_f32_e32 v69, s66, v69\n\t"
      "v_mul_f32_e32 v70, s66, v70\n\t"
      "v_mul_f32_e64 v71, s66, v71\n\t"
      "v_cvt_pk_bf16_f32 v0, v64, v65\n\t"
      "v_cvt_pk_bf16_f32 v1, v66, v67\n\t"
      "v_mfma_f32_16x16x32_bf16 a[180:183], v[44:47], v[108:111], a[180:183]\n\t"
      "v_cvt_pk_bf16_f32 v2, v68, v69\n\t"
      "v_cvt_pk_bf16_f32 v3, v70, v71\n\t"
      "v_mul_f32_e32 v72, s66, v72\n\t"
      "v_mul_f32_e32 v73, s66, v73\n\t"
      "v_mul_f32_e64 v74, s66, v74\n\t"
      "v_mfma_f32_16x16x32_bf16 a[184:187], v[44:47], v[116:119], a[184:187]\n\t"
      "v_mul_f32_e32 v75, s66, v75\n\t"
      "v_mul_f32_e32 v76, s66, v76\n\t"
      "v_mul_f32_e32 v77, s66, v77\n\t"
      "v_mul_f32_e32 v78, s66, v78\n\t"
      "v_mul_f32_e64 v79, s66, v79\n\t"
      "v_mfma_f32_16x16x32_bf16 a[188:191], v[44:47], v[124:127], a[188:191]\n\t"
      "v_cvt_pk_bf16_f32 v4, v72, v73\n\t"
      "v_cvt_pk_bf16_f32 v5, v74, v75\n\t"
      "v_cvt_pk_bf16_f32 v6, v76, v77\n\t"
      "v_cvt_pk_bf16_f32 v7, v78, v79\n\t"
      "v_mul_f32_e64 v80, s66, v80\n\t"
      "v_mfma_f32_16x16x32_bf16 a[192:195], v[16:19], v[96:99], a[192:195]\n\t"
      "v_mul_f32_e32 v81, s66, v81\n\t"
      "v_mul_f32_e32 v82, s66, v82\n\t"
      "v_mul_f32_e32 v83, s66, v83\n\t"
      "v_mul_f32_e32 v84, s66, v84\n\t"
      "v_mul_f32_e64 v85, s66, v85\n\t"
      "v_mfma_f32_16x16x32_bf16 a[196:199], v[16:19], v[104:107], a[196:199]\n\t"
      "v_mul_f32_e32 v86, s66, v86\n\t"
      "v_mul_f32_e32 v87, s66, v87\n\t"
      "v_cvt_pk_bf16_f32 v32, v80, v81\n\t"
      "v_cvt_pk_bf16_f32 v33, v82, v83\n\t"
      "v_cvt_pk_bf16_f32 v34, v84, v85\n\t"
      "v_mfma_f32_16x16x32_bf16 a[200:203], v[16:19], v[112:115], a[200:203]\n\t"
      "v_cvt_pk_bf16_f32 v35, v86, v87\n\t"
      "v_mul_f32_e32 v88, s66, v88\n\t"
      "v_mul_f32_e32 v89, s66, v89\n\t"
      "v_mul_f32_e32 v90, s66, v90\n\t"
      "v_mul_f32_e32 v91, s66, v91\n\t"
      "v_mfma_f32_16x16x32_bf16 a[204:207], v[16:19], v[120:123], a[204:207]\n\t"
      "v_mul_f32_e32 v92, s66, v92\n\t"
      "v_mul_f32_e32 v93, s66, v93\n\t"
      "v_mul_f32_e32 v94, s66, v94\n\t"
      "v_mul_f32_e32 v95, s66, v95\n\t"
      "v_cvt_pk_bf16_f32 v36, v88, v89\n\t"
      "v_mfma_f32_16x16x32_bf16 a[208:211], v[20:23], v[96:99], a[208:211]\n\t"
      "v_cvt_pk_bf16_f32 v37, v90, v91\n\t"
      "v_cvt_pk_bf16_f32 v38, v92, v93\n\t"
      "v_cvt_pk_bf16_f32 v39, v94, v95\n\t"
      "v_accvgpr_read_b32 v128, a160\n\t"
      "v_accvgpr_read_b32 v129, a161\n\t"
      "v_mfma_f32_16x16x32_bf16 a[212:215], v[20:23], v[104:107], a[212:215]\n\t"
      "v_accvgpr_read_b32 v130, a162\n\t"
      "v_accvgpr_read_b32 v131, a163\n\t"
      "v_accvgpr_read_b32 v132, a176\n\t"
      "v_accvgpr_read_b32 v133, a177\n\t"
      "v_accvgpr_read_b32 v134, a178\n\t"
      "v_mfma_f32_16x16x32_bf16 a[216:219], v[20:23], v[112:115], a[216:219]\n\t"
      "v_accvgpr_read_b32 v135, a179\n\t"
      "v_accvgpr_read_b32 v136, a164\n\t"
      "v_accvgpr_read_b32 v137, a165\n\t"
      "v_accvgpr_read_b32 v138, a166\n\t"
      "v_accvgpr_read_b32 v139, a167\n\t"
      "v_mfma_f32_16x16x32_bf16 a[220:223], v[20:23], v[120:123], a[220:223]\n\t"
      "v_accvgpr_read_b32 v140, a180\n\t"
      "v_accvgpr_read_b32 v141, a181\n\t"
      "v_accvgpr_read_b32 v142, a182\n\t"
      "v_accvgpr_read_b32 v143, a183\n\t"
      "v_accvgpr_read_b32 v144, a168\n\t"
      "v_mfma_f32_16x16x32_bf16 a[192:195], v[48:51], v[100:103], a[192:195]\n\t"
      "v_accvgpr_read_b32 v145, a169\n\t"
      "v_accvgpr_read_b32 v146, a170\n\t"
      "v_accvgpr_read_b32 v147, a171\n\t"
      "v_accvgpr_read_b32 v148, a184\n\t"
      "v_accvgpr_read_b32 v149, a185\n\t"
      "v_mfma_f32_16x16x32_bf16 a[196:199], v[48:51], v[108:111], a[196:199]\n\t"
      "v_accvgpr_read_b32 v150, a186\n\t"
      "v_accvgpr_read_b32 v151, a187\n\t"
      "v_accvgpr_read_b32 v152, a172\n\t"
      "v_accvgpr_read_b32 v153, a173\n\t"
      "v_accvgpr_read_b32 v154, a174\n\t"
      "v_mfma_f32_16x16x32_bf16 a[200:203], v[48:51], v[116:119], a[200:203]\n\t"
      "v_accvgpr_read_b32 v155, a175\n\t"
      "v_accvgpr_read_b32 v156, a188\n\t"
      "v_accvgpr_read_b32 v157, a189\n\t"
      "v_accvgpr_read_b32 v158, a190\n\t"
      "v_accvgpr_read_b32 v159, a191\n\t"
      "v_mfma_f32_16x16x32_bf16 a[204:207], v[48:51], v[124:127], a[204:207]\n\t"
      "v_mul_f32_e32 v128, s66, v128\n\t"
      "v_mul_f32_e32 v129, s66, v129\n\t"
      "v_mul_f32_e32 v130, s66, v130\n\t"
      "v_mul_f32_e32 v131, s66, v131\n\t"
      "v_mul_f32_e64 v132, s66, v132\n\t"
      "v_mfma_f32_16x16x32_bf16 a[208:211], v[52:55], v[100:103], a[208:211]\n\t"
      "v_mul_f32_e32 v133, s66, v133\n\t"
      "v_mul_f32_e32 v134, s66, v134\n\t"
      "v_mul_f32_e64 v135, s66, v135\n\t"
      "v_cvt_pk_bf16_f32 v8, v128, v129\n\t"
      "v_cvt_pk_bf16_f32 v9, v130, v131\n\t"
      "v_mfma_f32_16x16x32_bf16 a[212:215], v[52:55], v[108:111], a[212:215]\n\t"
      "v_cvt_pk_bf16_f32 v10, v132, v133\n\t"
      "v_cvt_pk_bf16_f32 v11, v134, v135\n\t"
      "v_mul_f32_e32 v136, s66, v136\n\t"
      "v_mul_f32_e32 v137, s66, v137\n\t"
      "v_mul_f32_e64 v138, s66, v138\n\t"
      "v_mfma_f32_16x16x32_bf16 a[216:219], v[52:55], v[116:119], a[216:219]\n\t"
      "v_mul_f32_e32 v139, s66, v139\n\t"
      "v_mul_f32_e32 v140, s66, v140\n\t"
      "v_mul_f32_e32 v141, s66, v141\n\t"
      "v_mul_f32_e32 v142, s66, v142\n\t"
      "v_mul_f32_e64 v143, s66, v143\n\t"
      "v_mfma_f32_16x16x32_bf16 a[220:223], v[52:55], v[124:127], a[220:223]\n\t"
      "v_cvt_pk_bf16_f32 v12, v136, v137\n\t"
      "v_cvt_pk_bf16_f32 v13, v138, v139\n\t"
      "v_cvt_pk_bf16_f32 v14, v140, v141\n\t"
      "v_cvt_pk_bf16_f32 v15, v142, v143\n\t"
      "v_mul_f32_e64 v144, s66, v144\n\t"
      "v_mfma_f32_16x16x32_bf16 a[224:227], v[24:27], v[96:99], a[224:227]\n\t"
      "v_mul_f32_e32 v145, s66, v145\n\t"
      "v_mul_f32_e32 v146, s66, v146\n\t"
      "v_mul_f32_e32 v147, s66, v147\n\t"
      "v_mul_f32_e32 v148, s66, v148\n\t"
      "v_mul_f32_e64 v149, s66, v149\n\t"
      "v_mfma_f32_16x16x32_bf16 a[228:231], v[24:27], v[104:107], a[228:231]\n\t"
      "v_mul_f32_e32 v150, s66, v150\n\t"
      "v_mul_f32_e32 v151, s66, v151\n\t"
      "v_cvt_pk_bf16_f32 v40, v144, v145\n\t"
      "v_cvt_pk_bf16_f32 v41, v146, v147\n\t"
      "v_cvt_pk_bf16_f32 v42, v148, v149\n\t"
      "v_mfma_f32_16x16x32_bf16 a[232:235], v[24:27], v[112:115], a[232:235]\n\t"
      "v_cvt_pk_bf16_f32 v43, v150, v151\n\t"
      "v_mul_f32_e32 v152, s66, v152\n\t"
      "v_mul_f32_e32 v153, s66, v153\n\t"
      "v_mul_f32_e32 v154, s66, v154\n\t"
      "v_mul_f32_e32 v155, s66, v155\n\t"
      "v_mfma_f32_16x16x32_bf16 a[236:239], v[24:27], v[120:123], a[236:239]\n\t"
      "v_mul_f32_e32 v156, s66, v156\n\t"
      "v_mul_f32_e32 v157, s66, v157\n\t"
      "v_mul_f32_e32 v158, s66, v158\n\t"
      "v_mul_f32_e32 v159, s66, v159\n\t"
      "v_cvt_pk_bf16_f32 v44, v152, v153\n\t"
      "v_mfma_f32_16x16x32_bf16 a[240:243], v[28:31], v[96:99], a[240:243]\n\t"
      "v_cvt_pk_bf16_f32 v45, v154, v155\n\t"
      "v_cvt_pk_bf16_f32 v46, v156, v157\n\t"
      "v_cvt_pk_bf16_f32 v47, v158, v159\n\t"
      "v_accvgpr_read_b32 v64, a192\n\t"
      "v_accvgpr_read_b32 v65, a193\n\t"
      "v_mfma_f32_16x16x32_bf16 a[244:247], v[28:31], v[104:107], a[244:247]\n\t"
      "v_accvgpr_read_b32 v66, a194\n\t"
      "v_accvgpr_read_b32 v67, a195\n\t"
      "v_accvgpr_read_b32 v68, a208\n\t"
      "v_accvgpr_read_b32 v69, a209\n\t"
      "v_accvgpr_read_b32 v70, a210\n\t"
      "v_mfma_f32_16x16x32_bf16 a[248:251], v[28:31], v[112:115], a[248:251]\n\t"
      "v_accvgpr_read_b32 v71, a211\n\t"
      "v_accvgpr_read_b32 v72, a196\n\t"
      "v_accvgpr_read_b32 v73, a197\n\t"
      "v_accvgpr_read_b32 v74, a198\n\t"
      "v_accvgpr_read_b32 v75, a199\n\t"
      "v_mfma_f32_16x16x32_bf16 a[252:255], v[28:31], v[120:123], a[252:255]\n\t"
      "v_accvgpr_read_b32 v76, a212\n\t"
      "v_accvgpr_read_b32 v77, a213\n\t"
      "v_accvgpr_read_b32 v78, a214\n\t"
      "v_accvgpr_read_b32 v79, a215\n\t"
      "v_accvgpr_read_b32 v80, a200\n\t"
      "v_mfma_f32_16x16x32_bf16 a[224:227], v[56:59], v[100:103], a[224:227]\n\t"
      "v_accvgpr_read_b32 v81, a201\n\t"
      "v_accvgpr_read_b32 v82, a202\n\t"
      "v_accvgpr_read_b32 v83, a203\n\t"
      "v_accvgpr_read_b32 v84, a216\n\t"
      "v_accvgpr_read_b32 v85, a217\n\t"
      "v_mfma_f32_16x16x32_bf16 a[228:231], v[56:59], v[108:111], a[228:231]\n\t"
      "v_accvgpr_read_b32 v86, a218\n\t"
      "v_accvgpr_read_b32 v87, a219\n\t"
      "v_accvgpr_read_b32 v88, a204\n\t"
      "v_accvgpr_read_b32 v89, a205\n\t"
      "v_accvgpr_read_b32 v90, a206\n\t"
      "v_mfma_f32_16x16x32_bf16 a[232:235], v[56:59], v[116:119], a[232:235]\n\t"
      "v_accvgpr_read_b32 v91, a207\n\t"
      "v_accvgpr_read_b32 v92, a220\n\t"
      "v_accvgpr_read_b32 v93, a221\n\t"
      "v_accvgpr_read_b32 v94, a222\n\t"
      "v_accvgpr_read_b32 v95, a223\n\t"
      "v_mfma_f32_16x16x32_bf16 a[236:239], v[56:59], v[124:127], a[236:239]\n\t"
      "v_accvgpr_write_b32 a192, v222\n\t"
      "v_accvgpr_write_b32 a193, v222\n\t"
      "v_accvgpr_write_b32 a194, v222\n\t"
      "v_accvgpr_write_b32 a195, v222\n\t"
      "v_mul_f32_e64 v64, s66, v64\n\t"
      "v_mfma_f32_16x16x32_bf16 a[240:243], v[60:63], v[100:103], a[240:243]\n\t"
      "v_mul_f32_e32 v65, s66, v65\n\t"
      "v_mul_f32_e32 v66, s66, v66\n\t"
      "v_mul_f32_e32 v67, s66, v67\n\t"
      "v_mul_f32_e32 v68, s66, v68\n\t"
      "v_mul_f32_e64 v69, s66, v69\n\t"
      "v_mfma_f32_16x16x32_bf16 a[244:247], v[60:63], v[108:111], a[244:247]\n\t"
      "v_mul_f32_e32 v70, s66, v70\n\t"
      "v_mul_f32_e32 v71, s66, v71\n\t"
      "v_cvt_pk_bf16_f32 v16, v64, v65\n\t"
      "v_cvt_pk_bf16_f32 v17, v66, v67\n\t"
      "v_cvt_pk_bf16_f32 v18, v68, v69\n\t"
      "v_mfma_f32_16x16x32_bf16 a[248:251], v[60:63], v[116:119], a[248:251]\n\t"
      "v_cvt_pk_bf16_f32 v19, v70, v71\n\t"
      "v_mul_f32_e32 v72, s66, v72\n\t"
      "v_mul_f32_e32 v73, s66, v73\n\t"
      "v_mul_f32_e32 v74, s66, v74\n\t"
      "v_mul_f32_e32 v75, s66, v75\n\t"
      "v_mfma_f32_16x16x32_bf16 a[252:255], v[60:63], v[124:127], a[252:255]\n\t"
      "v_mul_f32_e32 v76, s66, v76\n\t"
      "v_mul_f32_e32 v77, s66, v77\n\t"
      "v_mul_f32_e32 v78, s66, v78\n\t"
      "v_mul_f32_e32 v79, s66, v79\n\t"
      "v_cvt_pk_bf16_f32 v20, v72, v73\n\t"
      "v_cvt_pk_bf16_f32 v21, v74, v75\n\t"
      "v_cvt_pk_bf16_f32 v22, v76, v77\n\t"
      "v_cvt_pk_bf16_f32 v23, v78, v79\n\t"
      "v_mul_f32_e32 v80, s66, v80\n\t"
      "v_mul_f32_e32 v81, s66, v81\n\t"
      "v_mul_f32_e32 v82, s66, v82\n\t"
      "v_mul_f32_e32 v83, s66, v83\n\t"
      "v_mul_f32_e32 v84, s66, v84\n\t"
      "v_mul_f32_e32 v85, s66, v85\n\t"
      "v_mul_f32_e32 v86, s66, v86\n\t"
      "v_mul_f32_e32 v87, s66, v87\n\t"
      "v_cvt_pk_bf16_f32 v48, v80, v81\n\t"
      "v_cvt_pk_bf16_f32 v49, v82, v83\n\t"
      "v_cvt_pk_bf16_f32 v50, v84, v85\n\t"
      "v_cvt_pk_bf16_f32 v51, v86, v87\n\t"
      "v_mul_f32_e32 v88, s66, v88\n\t"
      "v_mul_f32_e32 v89, s66, v89\n\t"
      "v_mul_f32_e32 v90, s66, v90\n\t"
      "v_mul_f32_e32 v91, s66, v91\n\t"
      "v_mul_f32_e32 v92, s66, v92\n\t"
      "v_mul_f32_e32 v93, s66, v93\n\t"
      "v_mul_f32_e32 v94, s66, v94\n\t"
      "v_mul_f32_e32 v95, s66, v95\n\t"
      "v_cvt_pk_bf16_f32 v52, v88, v89\n\t"
      "v_cvt_pk_bf16_f32 v53, v90, v91\n\t"
      "v_cvt_pk_bf16_f32 v54, v92, v93\n\t"
      "v_cvt_pk_bf16_f32 v55, v94, v95\n\t"
      "s_and_b32 s68, %6, 7\n\t"
      "s_mul_i32 s68, s68, 28\n\t"
      "s_lshr_b32 s69, %6, 3\n\t"
      "s_add_u32 s68, s68, s69\n\t"
      "s_lshl_b32 s68, s68, 4\n\t"
      "s_cmp_ge_u32 s69, 28\n\t"
      "s_cbranch_scc1 L_DNW\n\t"
      "s_nop 0\n\t"
      "s_mul_i32 s69, s56, 458752\n\t"
      "s_add_u32 s68, s68, s69\n\t"
      "s_nop 0\n\t"
      "s_add_u32 s69, s68, 229376\n\t"
      "s_movk_i32 s70, 3584\n\t"
      "s_nop 0\n\t"
      "v_mul_lo_u32 v218, v241, s70\n\t"
      "s_lshl_b32 s67, s56, 11\n\t"
      "s_mov_b64 s[84:85], %7\n\t"
      "s_and_b32 s85, s85, 0xffff\n\t"
      "s_mov_b32 s86, 1835008\n\t"
      "s_mov_b32 s87, 0x20000\n\t"
      "s_add_u32 m0, s67, 135168\n\t"
      "s_nop 0\n\t"
      "s_nop 0\n\t"
      "buffer_load_dwordx4 v218, s[84:87], s68 offen lds\n\t"
      "s_add_u32 m0, s67, 136192\n\t"
      "s_nop 0\n\t"
      "s_nop 0\n\t"
      "buffer_load_dwordx4 v218, s[84:87], s69 offen lds\n\t"
      "s_mov_b64 s[84:85], %8\n\t"
      "s_nop 0\n\t"
      "s_and_b32 s85, s85, 0xffff\n\t"
      "s_mov_b32 s86, 1835008\n\t"
      "s_mov_b32 s87, 0x20000\n\t"
      "s_add_u32 m0, s67, 143360\n\t"
      "s_nop 0\n\t"
      "s_nop 0\n\t"
      "buffer_load_dwordx4 v218, s[84:87], s68 offen lds\n\t"
      "s_add_u32 m0, s67, 144384\n\t"
      "s_nop 0\n\t"
      "s_nop 0\n\t"
      "buffer_load_dwordx4 v218, s[84:87], s69 offen lds\n\t"
      "s_branch L_GUD\n\t"
      "L_DNW:\n\t"
      "s_mov_b64 s[84:85], %9\n\t"
      "s_and_b32 s85, s85, 0xffff\n\t"
      "s_mov_b32 s86, 1835008\n\t"
      "s_mov_b32 s87, 0x20000\n\t"
      "s_and_b32 s68, %6, 7\n\t"
      "s_lshl_b32 s68, s68, 2\n\t"
      "s_add_u32 s68, s68, s69\n\t"
      "s_sub_u32 s68, s68, 28\n\t"
      "s_lshl_b32 s68, s68, 5\n\t"
      "s_nop 0\n\t"
      "s_mul_i32 s69, s56, 229376\n\t"
      "s_add_u32 s68, s68, s69\n\t"
      "s_nop 0\n\t"
      "s_mul_i32 s67, s56, 7168\n\t"
      "v_lshrrev_b32_e32 v218, 1, v241\n\t"
      "v_lshlrev_b32_e32 v218, 10, v218\n\t"
      "v_and_b32_e64 v219, 1, v241\n\t"
      "v_lshl_add_u32 v218, v219, 4, v218\n\t"
      "s_add_u32 m0, s67, 135168\n\t"
      "s_nop 0\n\t"
      "s_nop 0\n\t"
      "buffer_load_dwordx4 v218, s[84:87], s68 offen lds\n\t"
      "s_add_u32 s68, s68, 0x8000\n\t"
      "s_add_u32 m0, s67, 136192\n\t"
      "s_nop 0\n\t"
      "s_nop 0\n\t"
      "buffer_load_dwordx4 v218, s[84:87], s68 offen lds\n\t"
      "s_add_u32 s68, s68, 0x8000\n\t"
      "s_add_u32 m0, s67, 137216\n\t"
      "s_nop 0\n\t"
      "s_nop 0\n\t"
      "buffer_load_dwordx4 v218, s[84:87], s68 offen lds\n\t"
      "s_add_u32 s68, s68, 0x8000\n\t"
      "s_add_u32 m0, s67, 138240\n\t"
      "s_nop 0\n\t"
      "s_nop 0\n\t"
      "buffer_load_dwordx4 v218, s[84:87], s68 offen lds\n\t"
      "s_add_u32 s68, s68, 0x8000\n\t"
      "s_add_u32 m0, s67, 139264\n\t"
      "s_nop 0\n\t"
      "s_nop 0\n\t"
      "buffer_load_dwordx4 v218, s[84:87], s68 offen lds\n\t"
      "s_add_u32 s68, s68, 0x8000\n\t"
      "s_add_u32 m0, s67, 140288\n\t"
      "s_nop 0\n\t"
      "s_nop 0\n\t"
      "buffer_load_dwordx4 v218, s[84:87], s68 offen lds\n\t"
      "s_add_u32 s68, s68, 0x8000\n\t"
      "s_add_u32 m0, s67, 141312\n\t"
      "s_nop 0\n\t"
      "s_nop 0\n\t"
      "buffer_load_dwordx4 v218, s[84:87], s68 offen lds\n\t"
      "s_add_u32 s68, s68, 0x8000\n\t"
      "L_GUD:\n\t"
      "v_accvgpr_read_b32 v128, a224\n\t"
      "v_accvgpr_read_b32 v129, a225\n\t"
      "v_accvgpr_read_b32 v130, a226\n\t"
      "v_accvgpr_read_b32 v131, a227\n\t"
      "v_accvgpr_read_b32 v132, a240\n\t"
      "v_accvgpr_read_b32 v133, a241\n\t"
      "v_accvgpr_read_b32 v134, a242\n\t"
      "v_accvgpr_read_b32 v135, a243\n\t"
      "v_accvgpr_read_b32 v136, a228\n\t"
      "v_accvgpr_read_b32 v137, a229\n\t"
      "v_accvgpr_read_b32 v138, a230\n\t"
      "v_accvgpr_read_b32 v139, a231\n\t"
      "v_accvgpr_read_b32 v140, a244\n\t"
      "v_accvgpr_read_b32 v141, a245\n\t"
      "v_accvgpr_read_b32 v142, a246\n\t"
      "v_accvgpr_read_b32 v143, a247\n\t"
      "v_accvgpr_read_b32 v144, a232\n\t"
      "v_accvgpr_read_b32 v145, a233\n\t"
      "v_accvgpr_read_b32 v146, a234\n\t"
      "v_accvgpr_read_b32 v147, a235\n\t"
      "v_accvgpr_read_b32 v148, a248\n\t"
      "v_accvgpr_read_b32 v149, a249\n\t"
      "v_accvgpr_read_b32 v150, a250\n\t"
      "v_accvgpr_read_b32 v151, a251\n\t"
      "v_accvgpr_read_b32 v152, a236\n\t"
      "v_accvgpr_read_b32 v153, a237\n\t"
      "v_accvgpr_read_b32 v154, a238\n\t"
      "v_accvgpr_read_b32 v155, a239\n\t"
      "v_accvgpr_read_b32 v156, a252\n\t"
      "v_accvgpr_read_b32 v157, a253\n\t"
      "v_accvgpr_read_b32 v158, a254\n\t"
      "v_accvgpr_read_b32 v159, a255\n\t"
      "v_mul_f32_e32 v128, s66, v128\n\t"
      "v_mul_f32_e32 v129, s66, v129\n\t"
      "v_mul_f32_e32 v130, s66, v130\n\t"
      "v_mul_f32_e32 v131, s66, v131\n\t"
      "v_mul_f32_e32 v132, s66, v132\n\t"
      "v_mul_f32_e32 v133, s66, v133\n\t"
      "v_mul_f32_e32 v134, s66, v134\n\t"
      "v_mul_f32_e32 v135, s66, v135\n\t"
      "v_cvt_pk_bf16_f32 v24, v128, v129\n\t"
      "v_cvt_pk_bf16_f32 v25, v130, v131\n\t"
      "v_cvt_pk_bf16_f32 v26, v132, v133\n\t"
      "v_cvt_pk_bf16_f32 v27, v134, v135\n\t"
      "v_mul_f32_e32 v136, s66, v136\n\t"
      "v_mul_f32_e32 v137, s66, v137\n\t"
      "v_mul_f32_e32 v138, s66, v138\n\t"
      "v_mul_f32_e32 v139, s66, v139\n\t"
      "v_mul_f32_e32 v140, s66, v140\n\t"
      "v_mul_f32_e32 v141, s66, v141\n\t"
      "v_mul_f32_e32 v142, s66, v142\n\t"
      "v_mul_f32_e32 v143, s66, v143\n\t"
      "v_cvt_pk_bf16_f32 v28, v136, v137\n\t"
      "v_cvt_pk_bf16_f32 v29, v138, v139\n\t"
      "v_cvt_pk_bf16_f32 v30, v140, v141\n\t"
      "v_cvt_pk_bf16_f32 v31, v142, v143\n\t"
      "v_mul_f32_e32 v144, s66, v144\n\t"
      "v_mul_f32_e32 v145, s66, v145\n\t"
      "v_mul_f32_e32 v146, s66, v146\n\t"
      "v_mul_f32_e32 v147, s66, v147\n\t"
      "v_mul_f32_e32 v148, s66, v148\n\t"
      "v_mul_f32_e32 v149, s66, v149\n\t"
      "v_mul_f32_e32 v150, s66, v150\n\t"
      "v_mul_f32_e32 v151, s66, v151\n\t"
      "v_cvt_pk_bf16_f32 v56, v144, v145\n\t"
      "v_cvt_pk_bf16_f32 v57, v146, v147\n\t"
      "v_cvt_pk_bf16_f32 v58, v148, v149\n\t"
      "v_cvt_pk_bf16_f32 v59, v150, v151\n\t"
      "v_mul_f32_e32 v152, s66, v152\n\t"
      "v_mul_f32_e32 v153, s66, v153\n\t"
      "v_mul_f32_e32 v154, s66, v154\n\t"
      "v_mul_f32_e32 v155, s66, v155\n\t"
      "v_mul_f32_e32 v156, s66, v156\n\t"
      "v_mul_f32_e32 v157, s66, v157\n\t"
      "v_mul_f32_e32 v158, s66, v158\n\t"
      "v_mul_f32_e32 v159, s66, v159\n\t"
      "v_cvt_pk_bf16_f32 v60, v152, v153\n\t"
      "v_cvt_pk_bf16_f32 v61, v154, v155\n\t"
      "v_cvt_pk_bf16_f32 v62, v156, v157\n\t"
      "v_cvt_pk_bf16_f32 v63, v158, v159\n\t"
      "s_waitcnt lgkmcnt(0)\n\t"
      "s_nop 0\n\t"
      "v_mfma_f32_16x16x32_bf16 v[64:67], a[128:131], v[0:3], 0\n\t"
      "v_mfma_f32_16x16x32_bf16 v[64:67], a[132:135], v[8:11], v[64:67]\n\t"
      "v_mfma_f32_16x16x32_bf16 v[64:67], a[136:139], v[16:19], v[64:67]\n\t"
      "v_mfma_f32_16x16x32_bf16 v[64:67], a[140:143], v[24:27], v[64:67]\n\t"
      "s_nop 0\n\t"
      "s_nop 0\n\t"
      "v_mfma_f32_16x16x32_bf16 v[68:71], a[144:147], v[0:3], 0\n\t"
      "v_mfma_f32_16x16x32_bf16 v[68:71], a[148:151], v[8:11], v[68:71]\n\t"
      "v_mfma_f32_16x16x32_bf16 v[68:71], a[152:155], v[16:19], v[68:71]\n\t"
      "v_mfma_f32_16x16x32_bf16 v[68:71], a[156:159], v[24:27], v[68:71]\n\t"
      "v_mfma_f32_16x16x32_bf16 v[72:75], a[128:131], v[4:7], 0\n\t"
      "s_nop 0\n\t"
      "s_nop 0\n\t"
      "v_mfma_f32_16x16x32_bf16 v[72:75], a[132:135], v[12:15], v[72:75]\n\t"
      "v_mfma_f32_16x16x32_bf16 v[72:75], a[136:139], v[20:23], v[72:75]\n\t"
      "v_mfma_f32_16x16x32_bf16 v[72:75], a[140:143], v[28:31], v[72:75]\n\t"
      "v_mfma_f32_16x16x32_bf16 v[76:79], a[144:147], v[4:7], 0\n\t"
      "v_mfma_f32_16x16x32_bf16 v[76:79], a[148:151], v[12:15], v[76:79]\n\t"
      "s_nop 0\n\t"
      "s_nop 0\n\t"
      "v_mfma_f32_16x16x32_bf16 v[76:79], a[152:155], v[20:23], v[76:79]\n\t"
      "v_mfma_f32_16x16x32_bf16 v[76:79], a[156:159], v[28:31], v[76:79]\n\t"
      "v_mfma_f32_16x16x32_bf16 v[80:83], a[128:131], v[32:35], 0\n\t"
      "v_mfma_f32_16x16x32_bf16 v[80:83], a[132:135], v[40:43], v[80:83]\n\t"
      "v_mfma_f32_16x16x32_bf16 v[80:83], a[136:139], v[48:51], v[80:83]\n\t"
      "v_mfma_f32_16x16x32_bf16 v[80:83], a[140:143], v[56:59], v[80:83]\n\t"
      "s_nop 0\n\t"
      "s_nop 0\n\t"
      "v_mfma_f32_16x16x32_bf16 v[84:87], a[144:147], v[32:35], 0\n\t"
      "v_mfma_f32_16x16x32_bf16 v[84:87], a[148:151], v[40:43], v[84:87]\n\t"
      "v_mfma_f32_16x16x32_bf16 v[84:87], a[152:155], v[48:51], v[84:87]\n\t"
      "v_mfma_f32_16x16x32_bf16 v[84:87], a[156:159], v[56:59], v[84:87]\n\t"
      "v_mfma_f32_16x16x32_bf16 v[88:91], a[128:131], v[36:39], 0\n\t"
      "v_mfma_f32_16x16x32_bf16 v[88:91], a[132:135], v[44:47], v[88:91]\n\t"
      "ds_read_b128 a[128:131], v230 offset:8192\n\t"
      "v_mfma_f32_16x16x32_bf16 v[88:91], a[136:139], v[52:55], v[88:91]\n\t"
      "v_mfma_f32_16x16x32_bf16 v[88:91], a[140:143], v[60:63], v[88:91]\n\t"
      "v_mfma_f32_16x16x32_bf16 v[92:95], a[144:147], v[36:39], 0\n\t"
      "s_waitcnt lgkmcnt(9)\n\t"
      "s_nop 0\n\t"
      "v_mfma_f32_16x16x32_bf16 v[92:95], a[148:151], v[44:47], v[92:95]\n\t"
      "ds_read_b128 a[132:135], v231 offset:8192\n\t"
      "v_mfma_f32_16x16x32_bf16 v[92:95], a[152:155], v[52:55], v[92:95]\n\t"
      "v_mfma_f32_16x16x32_bf16 v[92:95], a[156:159], v[60:63], v[92:95]\n\t"
      "ds_read_b128 a[136:139], v232 offset:8192\n\t"
      "s_nop 0\n\t"
      "s_nop 7\n\t"
      "v_max3_f32 v218, v64, v65, v66\n\t"
      "v_max3_f32 v218, v218, v67, v68\n\t"
      "v_max3_f32 v218, v218, v69, v70\n\t"
      "v_max_f32_e64 v218, v218, v71\n\t"
      "v_max3_f32 v219, v72, v73, v74\n\t"
      "v_max3_f32 v219, v219, v75, v76\n\t"
      "v_max3_f32 v219, v219, v77, v78\n\t"
      "v_max_f32_e64 v219, v219, v79\n\t"
      "v_max3_f32 v220, v80, v81, v82\n\t"
      "v_max3_f32 v220, v220, v83, v84\n\t"
      "v_max3_f32 v220, v220, v85, v86\n\t"
      "v_max_f32_e64 v220, v220, v87\n\t"
      "v_max3_f32 v221, v88, v89, v90\n\t"
      "v_max3_f32 v221, v221, v91, v92\n\t"
      "v_max3_f32 v221, v221, v93, v94\n\t"
      "v_max_f32_e32 v221, v221, v95\n\t"
      "v_mov_b32_e32 v222, v218\n\t"
      "v_mov_b32_e32 v223, v219\n\t"
      "v_mov_b32_e32 v224, v220\n\t"
      "v_mov_b32_e32 v225, v221\n\t"
      "s_nop 1\n\t"
      "v_permlane16_swap_b32_e32 v218, v222\n\t"
      "v_permlane16_swap_b32_e32 v219, v223\n\t"
      "v_permlane16_swap_b32_e32 v220, v224\n\t"
      "v_permlane16_swap_b32_e32 v221, v225\n\t"
      "s_nop 0\n\t"
      "v_max_f32_e32 v218, v218, v222\n\t"
      "v_max_f32_e32 v219, v219, v223\n\t"
      "v_max_f32_e32 v220, v220, v224\n\t"
      "v_max_f32_e32 v221, v221, v225\n\t"
      "v_mov_b32_e32 v222, v218\n\t"
      "v_mov_b32_e32 v223, v219\n\t"
      "v_mov_b32_e32 v224, v220\n\t"
      "v_mov_b32_e32 v225, v221\n\t"
      "s_nop 1\n\t"
      "v_permlane32_swap_b32_e32 v218, v222\n\t"
      "v_permlane32_swap_b32_e32 v219, v223\n\t"
      "v_permlane32_swap_b32_e32 v220, v224\n\t"
      "v_permlane32_swap_b32_e32 v221, v225\n\t"
      "s_nop 0\n\t"
      "v_max_f32_e32 v218, v218, v222\n\t"
      "v_max_f32_e32 v219, v219, v223\n\t"
      "v_max_f32_e32 v220, v220, v224\n\t"
      "v_max_f32_e32 v221, v221, v225\n\t"
      "v_sub_f32_e32 v64, v64, v218\n\t"
      "v_sub_f32_e32 v65, v65, v218\n\t"
      "v_sub_f32_e32 v66, v66, v218\n\t"
      "v_sub_f32_e32 v67, v67, v218\n\t"
      "v_sub_f32_e32 v68, v68, v218\n\t"
      "v_sub_f32_e32 v69, v69, v218\n\t"
      "v_sub_f32_e32 v70, v70, v218\n\t"
      "v_sub_f32_e32 v71, v71, v218\n\t"
      "v_sub_f32_e32 v128, 0, v218\n\t"
      "v_sub_f32_e32 v129, 0, v218\n\t"
      "v_sub_f32_e32 v130, 0, v218\n\t"
      "v_sub_f32_e32 v131, 0, v218\n\t"
      "v_mov_b32_e32 v192, 0\n\t"
      "v_mov_b32_e32 v193, 0\n\t"
      "v_mov_b32_e32 v194, 0\n\t"
      "v_mov_b32_e32 v195, 0\n\t"
      "v_sub_f32_e32 v72, v72, v219\n\t"
      "v_sub_f32_e32 v73, v73, v219\n\t"
      "v_sub_f32_e32 v74, v74, v219\n\t"
      "v_sub_f32_e32 v75, v75, v219\n\t"
      "v_sub_f32_e32 v76, v76, v219\n\t"
      "v_sub_f32_e32 v77, v77, v219\n\t"
      "v_sub_f32_e32 v78, v78, v219\n\t"
      "v_sub_f32_e32 v79, v79, v219\n\t"
      "v_sub_f32_e32 v132, 0, v219\n\t"
      "v_sub_f32_e32 v133, 0, v219\n\t"
      "v_sub_f32_e32 v134, 0, v219\n\t"
      "v_sub_f32_e32 v135, 0, v219\n\t"
      "v_mov_b32_e32 v196, 0\n\t"
      "v_mov_b32_e32 v197, 0\n\t"
      "v_mov_b32_e32 v198, 0\n\t"
      "v_mov_b32_e32 v199, 0\n\t"
      "v_sub_f32_e32 v80, v80, v220\n\t"
      "v_sub_f32_e32 v81, v81, v220\n\t"
      "v_sub_f32_e32 v82, v82, v220\n\t"
      "v_sub_f32_e32 v83, v83, v220\n\t"
      "v_sub_f32_e32 v84, v84, v220\n\t"
      "v_sub_f32_e32 v85, v85, v220\n\t"
      "v_sub_f32_e32 v86, v86, v220\n\t"
      "v_sub_f32_e32 v87, v87, v220\n\t"
      "v_sub_f32_e32 v136, 0, v220\n\t"
      "v_sub_f32_e32 v137, 0, v220\n\t"
      "v_sub_f32_e32 v138, 0, v220\n\t"
      "v_sub_f32_e32 v139, 0, v220\n\t"
      "v_mov_b32_e32 v200, 0\n\t"
      "v_mov_b32_e32 v201, 0\n\t"
      "v_mov_b32_e32 v202, 0\n\t"
      "v_mov_b32_e32 v203, 0\n\t"
      "v_sub_f32_e32 v88, v88, v221\n\t"
      "v_sub_f32_e32 v89, v89, v221\n\t"
      "v_sub_f32_e32 v90, v90, v221\n\t"
      "v_sub_f32_e32 v91, v91, v221\n\t"
      "v_sub_f32_e32 v92, v92, v221\n\t"
      "v_sub_f32_e32 v93, v93, v221\n\t"
      "v_sub_f32_e32 v94, v94, v221\n\t"
      "v_sub_f32_e32 v95, v95, v221\n\t"
      "v_sub_f32_e32 v140, 0, v221\n\t"
      "v_sub_f32_e32 v141, 0, v221\n\t"
      "v_sub_f32_e32 v142, 0, v221\n\t"
      "v_sub_f32_e32 v143, 0, v221\n\t"
      "v_mov_b32_e32 v204, 0\n\t"
      "v_mov_b32_e32 v205, 0\n\t"
      "v_mov_b32_e32 v206, 0\n\t"
      "v_mov_b32_e32 v207, 0\n\t"
      "v_exp_f32_e32 v208, v64\n\t"
      "v_exp_f32_e32 v209, v65\n\t"
      "s_nop 0\n\t"
      "v_cvt_pk_bf16_f32 v144, v208, v209\n\t"
      "v_exp_f32_e32 v210, v66\n\t"
      "v_exp_f32_e64 v211, v67\n\t"
      "s_nop 0\n\t"
      "v_cvt_pk_bf16_f32 v145, v210, v211\n\t"
      "v_exp_f32_e32 v212, v68\n\t"
      "v_exp_f32_e64 v213, v69\n\t"
      "s_nop 0\n\t"
      "v_cvt_pk_bf16_f32 v146, v212, v213\n\t"
      "v_exp_f32_e32 v214, v70\n\t"
      "v_exp_f32_e64 v215, v71\n\t"
      "s_nop 0\n\t"
      "v_cvt_pk_bf16_f32 v147, v214, v215\n\t"
      "s_nop 0\n\t"
      "s_nop 0\n\t"
      "ds_read_b128 a[140:143], v233 offset:8192\n\t"
      "s_nop 0\n\t"
      "v_exp_f32_e64 v208, v72\n\t"
      "s_nop 0\n\t"
      "ds_read_b128 a[144:147], v230 offset:12288\n\t"
      "s_nop 0\n\t"
      "v_exp_f32_e64 v209, v73\n\t"
      "s_nop 0\n\t"
      "ds_read_b128 a[148:151], v231 offset:12288\n\t"
      "v_cvt_pk_bf16_f32 v148, v208, v209\n\t"
      "s_nop 0\n\t"
      "v_exp_f32_e64 v210, v74\n\t"
      "s_nop 0\n\t"
      "ds_read_b128 a[152:155], v232 offset:12288\n\t"
      "ds_read_b64_tr_b16 a[160:161], v234 offset:0\n\t"
      "s_nop 0\n\t"
      "v_exp_f32_e64 v211, v75\n\t"
      "s_nop 0\n\t"
      "ds_read_b128 a[156:159], v233 offset:12288\n\t"
      "v_cvt_pk_bf16_f32 v149, v210, v211\n\t"
      "s_nop 0\n\t"
      "v_exp_f32_e64 v212, v76\n\t"
      "s_nop 0\n\t"
      "ds_read_b64_tr_b16 a[162:163], v234 offset:4096\n\t"
      "s_nop 0\n\t"
      "v_exp_f32_e64 v213, v77\n\t"
      "s_nop 0\n\t"
      "ds_read_b64_tr_b16 a[164:165], v234 offset:256\n\t"
      "v_cvt_pk_bf16_f32 v150, v212, v213\n\t"
      "s_nop 0\n\t"
      "v_exp_f32_e64 v214, v78\n\t"
      "s_nop 0\n\t"
      "ds_read_b64_tr_b16 a[166:167], v234 offset:4352\n\t"
      "s_nop 0\n\t"
      "v_exp_f32_e64 v215, v79\n\t"
      "s_nop 0\n\t"
      "ds_read_b64_tr_b16 a[168:169], v234 offset:512\n\t"
      "v_cvt_pk_bf16_f32 v151, v214, v215\n\t"
      "s_nop 0\n\t"
      "s_nop 0\n\t"
      "ds_read_b64_tr_b16 a[170:171], v234 offset:4608\n\t"
      "s_nop 0\n\t"
      "v_exp_f32_e64 v208, v80\n\t"
      "s_nop 0\n\t"
      "ds_read_b64_tr_b16 a[172:173], v234 offset:768\n\t"
      "s_nop 0\n\t"
      "v_exp_f32_e64 v209, v81\n\t"
      "s_nop 0\n\t"
      "ds_read_b64_tr_b16 a[174:175], v234 offset:4864\n\t"
      "v_cvt_pk_bf16_f32 v152, v208, v209\n\t"
      "s_nop 0\n\t"
      "v_exp_f32_e64 v210, v82\n\t"
      "s_nop 0\n\t"
      "ds_read_b64_tr_b16 a[176:177], v234 offset:1024\n\t"
      "s_nop 0\n\t"
      "v_exp_f32_e64 v211, v83\n\t"
      "s_nop 0\n\t"
      "ds_read_b64_tr_b16 a[178:179], v234 offset:5120\n\t"
      "v_cvt_pk_bf16_f32 v153, v210, v211\n\t"
      "s_nop 0\n\t"
      "v_exp_f32_e64 v212, v84\n\t"
      "s_nop 0\n\t"
      "ds_read_b64_tr_b16 a[180:181], v234 offset:1280\n\t"
      "s_nop 0\n\t"
      "v_exp_f32_e64 v213, v85\n\t"
      "s_nop 0\n\t"
      "ds_read_b64_tr_b16 a[182:183], v234 offset:5376\n\t"
      "v_cvt_pk_bf16_f32 v154, v212, v213\n\t"
      "s_nop 0\n\t"
      "s_waitcnt lgkmcnt(11)\n\t"
      "v_exp_f32_e32 v214, v86\n\t"
      "s_nop 0\n\t"
      "ds_read_b64_tr_b16 a[184:185], v234 offset:1536\n\t"
      "s_branch L_H1\n\t"
      "L_RARE0:\n\t"
      "s_mov_b32 s64, 0\n\t"
      "s_branch L_RARER1\n\t"
      "L_RARE1:\n\t"
      "s_mov_b32 s64, 1\n\t"
      "s_branch L_RARER0\n\t"
      "L_RARE2:\n\t"
      "s_mov_b32 s64, 2\n\t"
      "s_branch L_RARER1\n\t"
      "L_RARE3:\n\t"
      "s_mov_b32 s64, 3\n\t"
      "s_branch L_RARER0\n\t"
      "L_RARE4:\n\t"
      "s_mov_b32 s64, 4\n\t"
      "s_branch L_RARER1\n\t"
      "L_RARE5:\n\t"
      "s_mov_b32 s64, 5\n\t"
      "s_branch L_RARER0\n\t"
      "L_RARE6:\n\t"
      "s_mov_b32 s64, 6\n\t"
      "s_branch L_RARER1\n\t"
      "L_RARE7:\n\t"
      "s_mov_b32 s64, 7\n\t"
      "s_branch L_RARER0\n\t"
      "L_RARE8:\n\t"
      "s_mov_b32 s64, 8\n\t"
      "s_branch L_RARER1\n\t"
      "L_RARER0:\n\t"
      "s_nop 15\n\t"
      "v_log_f32_e32 v222, v192\n\t"
      "v_log_f32_e32 v223, v196\n\t"
      "v_log_f32_e32 v224, v200\n\t"
      "v_log_f32_e32 v225, v204\n\t"
      "v_max3_f32 v218, v64, v65, v66\n\t"
      "v_max3_f32 v218, v218, v67, v68\n\t"
      "v_max3_f32 v218, v218, v69, v70\n\t"
      "v_max3_f32 v218, v218, v71, v222\n\t"
      "v_max_f32_e32 v218, 0, v218\n\t"
      "v_max3_f32 v219, v72, v73, v74\n\t"
      "v_max3_f32 v219, v219, v75, v76\n\t"
      "v_max3_f32 v219, v219, v77, v78\n\t"
      "v_max3_f32 v219, v219, v79, v223\n\t"
      "v_max_f32_e32 v219, 0, v219\n\t"
      "v_max3_f32 v220, v80, v81, v82\n\t"
      "v_max3_f32 v220, v220, v83, v84\n\t"
      "v_max3_f32 v220, v220, v85, v86\n\t"
      "v_max3_f32 v220, v220, v87, v224\n\t"
      "v_max_f32_e32 v220, 0, v220\n\t"
      "v_max3_f32 v221, v88, v89, v90\n\t"
      "v_max3_f32 v221, v221, v91, v92\n\t"
      "v_max3_f32 v221, v221, v93, v94\n\t"
      "v_max3_f32 v221, v221, v95, v225\n\t"
      "v_max_f32_e32 v221, 0, v221\n\t"
      "v_mov_b32_e32 v222, v218\n\t"
      "v_mov_b32_e32 v223, v219\n\t"
      "v_mov_b32_e32 v224, v220\n\t"
      "v_mov_b32_e32 v225, v221\n\t"
      "s_nop 1\n\t"
      "v_permlane16_swap_b32_e32 v218, v222\n\t"
      "v_permlane16_swap_b32_e32 v219, v223\n\t"
      "v_permlane16_swap_b32_e32 v220, v224\n\t"
      "v_permlane16_swap_b32_e32 v221, v225\n\t"
      "s_nop 0\n\t"
      "v_max_f32_e32 v218, v218, v222\n\t"
      "v_max_f32_e32 v219, v219, v223\n\t"
      "v_max_f32_e32 v220, v220, v224\n\t"
      "v_max_f32_e32 v221, v221, v225\n\t"
      "v_mov_b32_e32 v222, v218\n\t"
      "v_mov_b32_e32 v223, v219\n\t"
      "v_mov_b32_e32 v224, v220\n\t"
      "v_mov_b32_e32 v225, v221\n\t"
      "s_nop 1\n\t"
      "v_permlane32_swap_b32_e32 v218, v222\n\t"
      "v_permlane32_swap_b32_e32 v219, v223\n\t"
      "v_permlane32_swap_b32_e32 v220, v224\n\t"
      "v_permlane32_swap_b32_e32 v221, v225\n\t"
      "s_nop 0\n\t"
      "v_max_f32_e32 v218, v218, v222\n\t"
      "v_max_f32_e32 v219, v219, v223\n\t"
      "v_max_f32_e32 v220, v220, v224\n\t"
      "v_max_f32_e32 v221, v221, v225\n\t"
      "v_exp_f32_e64 v226, -v218\n\t"
      "v_exp_f32_e64 v227, -v219\n\t"
      "v_exp_f32_e64 v228, -v220\n\t"
      "v_exp_f32_e64 v229, -v221\n\t"
      "s_nop 0\n\t"
      "v_mul_f32_e32 v192, v192, v226\n\t"
      "v_sub_f32_e32 v128, v128, v218\n\t"
      "v_sub_f32_e32 v129, v129, v218\n\t"
      "v_sub_f32_e32 v130, v130, v218\n\t"
      "v_sub_f32_e32 v131, v131, v218\n\t"
      "v_sub_f32_e32 v64, v64, v218\n\t"
      "v_sub_f32_e32 v96, v96, v218\n\t"
      "v_sub_f32_e32 v65, v65, v218\n\t"
      "v_sub_f32_e32 v97, v97, v218\n\t"
      "v_sub_f32_e32 v66, v66, v218\n\t"
      "v_sub_f32_e32 v98, v98, v218\n\t"
      "v_sub_f32_e32 v67, v67, v218\n\t"
      "v_sub_f32_e32 v99, v99, v218\n\t"
      "v_sub_f32_e32 v68, v68, v218\n\t"
      "v_sub_f32_e32 v100, v100, v218\n\t"
      "v_sub_f32_e32 v69, v69, v218\n\t"
      "v_sub_f32_e32 v101, v101, v218\n\t"
      "v_sub_f32_e32 v70, v70, v218\n\t"
      "v_sub_f32_e32 v102, v102, v218\n\t"
      "v_sub_f32_e32 v71, v71, v218\n\t"
      "v_sub_f32_e32 v103, v103, v218\n\t"
      "v_accvgpr_read_b32 v222, a0\n\t"
      "v_mul_f32_e32 v222, v222, v226\n\t"
      "v_accvgpr_write_b32 a0, v222\n\t"
      "v_accvgpr_read_b32 v223, a1\n\t"
      "v_mul_f32_e32 v223, v223, v226\n\t"
      "v_accvgpr_write_b32 a1, v223\n\t"
      "v_accvgpr_read_b32 v224, a2\n\t"
      "v_mul_f32_e32 v224, v224, v226\n\t"
      "v_accvgpr_write_b32 a2, v224\n\t"
      "v_accvgpr_read_b32 v225, a3\n\t"
      "v_mul_f32_e32 v225, v225, v226\n\t"
      "v_accvgpr_write_b32 a3, v225\n\t"
      "v_accvgpr_read_b32 v222, a16\n\t"
      "v_mul_f32_e32 v222, v222, v226\n\t"
      "v_accvgpr_write_b32 a16, v222\n\t"
      "v_accvgpr_read_b32 v223, a17\n\t"
      "v_mul_f32_e32 v223, v223, v226\n\t"
      "v_accvgpr_write_b32 a17, v223\n\t"
      "v_accvgpr_read_b32 v224, a18\n\t"
      "v_mul_f32_e32 v224, v224, v226\n\t"
      "v_accvgpr_write_b32 a18, v224\n\t"
      "v_accvgpr_read_b32 v225, a19\n\t"
      "v_mul_f32_e32 v225, v225, v226\n\t"
      "v_accvgpr_write_b32 a19, v225\n\t"
      "v_accvgpr_read_b32 v222, a32\n\t"
      "v_mul_f32_e32 v222, v222, v226\n\t"
      "v_accvgpr_write_b32 a32, v222\n\t"
      "v_accvgpr_read_b32 v223, a33\n\t"
      "v_mul_f32_e32 v223, v223, v226\n\t"
      "v_accvgpr_write_b32 a33, v223\n\t"
      "v_accvgpr_read_b32 v224, a34\n\t"
      "v_mul_f32_e32 v224, v224, v226\n\t"
      "v_accvgpr_write_b32 a34, v224\n\t"
      "v_accvgpr_read_b32 v225, a35\n\t"
      "v_mul_f32_e32 v225, v225, v226\n\t"
      "v_accvgpr_write_b32 a35, v225\n\t"
      "v_accvgpr_read_b32 v222, a48\n\t"
      "v_mul_f32_e32 v222, v222, v226\n\t"
      "v_accvgpr_write_b32 a48, v222\n\t"
      "v_accvgpr_read_b32 v223, a49\n\t"
      "v_mul_f32_e32 v223, v223, v226\n\t"
      "v_accvgpr_write_b32 a49, v223\n\t"
      "v_accvgpr_read_b32 v224, a50\n\t"
      "v_mul_f32_e32 v224, v224, v226\n\t"
      "v_accvgpr_write_b32 a50, v224\n\t"
      "v_accvgpr_read_b32 v225, a51\n\t"
      "v_mul_f32_e32 v225, v225, v226\n\t"
      "v_accvgpr_write_b32 a51, v225\n\t"
      "v_accvgpr_read_b32 v222, a64\n\t"
      "v_mul_f32_e32 v222, v222, v226\n\t"
      "v_accvgpr_write_b32 a64, v222\n\t"
      "v_accvgpr_read_b32 v223, a65\n\t"
      "v_mul_f32_e32 v223, v223, v226\n\t"
      "v_accvgpr_write_b32 a65, v223\n\t"
      "v_accvgpr_read_b32 v224, a66\n\t"
      "v_mul_f32_e32 v224, v224, v226\n\t"
      "v_accvgpr_write_b32 a66, v224\n\t"
      "v_accvgpr_read_b32 v225, a67\n\t"
      "v_mul_f32_e32 v225, v225, v226\n\t"
      "v_accvgpr_write_b32 a67, v225\n\t"
      "v_accvgpr_read_b32 v222, a80\n\t"
      "v_mul_f32_e32 v222, v222, v226\n\t"
      "v_accvgpr_write_b32 a80, v222\n\t"
      "v_accvgpr_read_b32 v223, a81\n\t"
      "v_mul_f32_e32 v223, v223, v226\n\t"
      "v_accvgpr_write_b32 a81, v223\n\t"
      "v_accvgpr_read_b32 v224, a82\n\t"
      "v_mul_f32_e32 v224, v224, v226\n\t"
      "v_accvgpr_write_b32 a82, v224\n\t"
      "v_accvgpr_read_b32 v225, a83\n\t"
      "v_mul_f32_e32 v225, v225, v226\n\t"
      "v_accvgpr_write_b32 a83, v225\n\t"
      "v_accvgpr_read_b32 v222, a96\n\t"
      "v_mul_f32_e32 v222, v222, v226\n\t"
      "v_accvgpr_write_b32 a96, v222\n\t"
      "v_accvgpr_read_b32 v223, a97\n\t"
      "v_mul_f32_e32 v223, v223, v226\n\t"
      "v_accvgpr_write_b32 a97, v223\n\t"
      "v_accvgpr_read_b32 v224, a98\n\t"
      "v_mul_f32_e32 v224, v224, v226\n\t"
      "v_accvgpr_write_b32 a98, v224\n\t"
      "v_accvgpr_read_b32 v225, a99\n\t"
      "v_mul_f32_e32 v225, v225, v226\n\t"
      "v_accvgpr_write_b32 a99, v225\n\t"
      "v_accvgpr_read_b32 v222, a112\n\t"
      "v_mul_f32_e32 v222, v222, v226\n\t"
      "v_accvgpr_write_b32 a112, v222\n\t"
      "v_accvgpr_read_b32 v223, a113\n\t"
      "v_mul_f32_e32 v223, v223, v226\n\t"
      "v_accvgpr_write_b32 a113, v223\n\t"
      "v_accvgpr_read_b32 v224, a114\n\t"
      "v_mul_f32_e32 v224, v224, v226\n\t"
      "v_accvgpr_write_b32 a114, v224\n\t"
      "v_accvgpr_read_b32 v225, a115\n\t"
      "v_mul_f32_e32 v225, v225, v226\n\t"
      "v_accvgpr_write_b32 a115, v225\n\t"
      "v_mul_f32_e32 v196, v196, v227\n\t"
      "v_sub_f32_e32 v132, v132, v219\n\t"
      "v_sub_f32_e32 v133, v133, v219\n\t"
      "v_sub_f32_e32 v134, v134, v219\n\t"
      "v_sub_f32_e32 v135, v135, v219\n\t"
      "v_sub_f32_e32 v72, v72, v219\n\t"
      "v_sub_f32_e32 v104, v104, v219\n\t"
      "v_sub_f32_e32 v73, v73, v219\n\t"
      "v_sub_f32_e32 v105, v105, v219\n\t"
      "v_sub_f32_e32 v74, v74, v219\n\t"
      "v_sub_f32_e32 v106, v106, v219\n\t"
      "v_sub_f32_e32 v75, v75, v219\n\t"
      "v_sub_f32_e32 v107, v107, v219\n\t"
      "v_sub_f32_e32 v76, v76, v219\n\t"
      "v_sub_f32_e32 v108, v108, v219\n\t"
      "v_sub_f32_e32 v77, v77, v219\n\t"
      "v_sub_f32_e32 v109, v109, v219\n\t"
      "v_sub_f32_e32 v78, v78, v219\n\t"
      "v_sub_f32_e32 v110, v110, v219\n\t"
      "v_sub_f32_e32 v79, v79, v219\n\t"
      "v_sub_f32_e32 v111, v111, v219\n\t"
      "v_accvgpr_read_b32 v222, a4\n\t"
      "v_mul_f32_e32 v222, v222, v227\n\t"
      "v_accvgpr_write_b32 a4, v222\n\t"
      "v_accvgpr_read_b32 v223, a5\n\t"
      "v_mul_f32_e32 v223, v223, v227\n\t"
      "v_accvgpr_write_b32 a5, v223\n\t"
      "v_accvgpr_read_b32 v224, a6\n\t"
      "v_mul_f32_e32 v224, v224, v227\n\t"
      "v_accvgpr_write_b32 a6, v224\n\t"
      "v_accvgpr_read_b32 v225, a7\n\t"
      "v_mul_f32_e32 v225, v225, v227\n\t"
      "v_accvgpr_write_b32 a7, v225\n\t"
      "v_accvgpr_read_b32 v222, a20\n\t"
      "v_mul_f32_e32 v222, v222, v227\n\t"
      "v_accvgpr_write_b32 a20, v222\n\t"
      "v_accvgpr_read_b32 v223, a21\n\t"
      "v_mul_f32_e32 v223, v223, v227\n\t"
      "v_accvgpr_write_b32 a21, v223\n\t"
      "v_accvgpr_read_b32 v224, a22\n\t"
      "v_mul_f32_e32 v224, v224, v227\n\t"
      "v_accvgpr_write_b32 a22, v224\n\t"
      "v_accvgpr_read_b32 v225, a23\n\t"
      "v_mul_f32_e32 v225, v225, v227\n\t"
      "v_accvgpr_write_b32 a23, v225\n\t"
      "v_accvgpr_read_b32 v222, a36\n\t"
      "v_mul_f32_e32 v222, v222, v227\n\t"
      "v_accvgpr_write_b32 a36, v222\n\t"
      "v_accvgpr_read_b32 v223, a37\n\t"
      "v_mul_f32_e32 v223, v223, v227\n\t"
      "v_accvgpr_write_b32 a37, v223\n\t"
      "v_accvgpr_read_b32 v224, a38\n\t"
      "v_mul_f32_e32 v224, v224, v227\n\t"
      "v_accvgpr_write_b32 a38, v224\n\t"
      "v_accvgpr_read_b32 v225, a39\n\t"
      "v_mul_f32_e32 v225, v225, v227\n\t"
      "v_accvgpr_write_b32 a39, v225\n\t"
      "v_accvgpr_read_b32 v222, a52\n\t"
      "v_mul_f32_e32 v222, v222, v227\n\t"
      "v_accvgpr_write_b32 a52, v222\n\t"
      "v_accvgpr_read_b32 v223, a53\n\t"
      "v_mul_f32_e32 v223, v223, v227\n\t"
      "v_accvgpr_write_b32 a53, v223\n\t"
      "v_accvgpr_read_b32 v224, a54\n\t"
      "v_mul_f32_e32 v224, v224, v227\n\t"
      "v_accvgpr_write_b32 a54, v224\n\t"
      "v_accvgpr_read_b32 v225, a55\n\t"
      "v_mul_f32_e32 v225, v225, v227\n\t"
      "v_accvgpr_write_b32 a55, v225\n\t"
      "v_accvgpr_read_b32 v222, a68\n\t"
      "v_mul_f32_e32 v222, v222, v227\n\t"
      "v_accvgpr_write_b32 a68, v222\n\t"
      "v_accvgpr_read_b32 v223, a69\n\t"
      "v_mul_f32_e32 v223, v223, v227\n\t"
      "v_accvgpr_write_b32 a69, v223\n\t"
      "v_accvgpr_read_b32 v224, a70\n\t"
      "v_mul_f32_e32 v224, v224, v227\n\t"
      "v_accvgpr_write_b32 a70, v224\n\t"
      "v_accvgpr_read_b32 v225, a71\n\t"
      "v_mul_f32_e32 v225, v225, v227\n\t"
      "v_accvgpr_write_b32 a71, v225\n\t"
      "v_accvgpr_read_b32 v222, a84\n\t"
      "v_mul_f32_e32 v222, v222, v227\n\t"
      "v_accvgpr_write_b32 a84, v222\n\t"
      "v_accvgpr_read_b32 v223, a85\n\t"
      "v_mul_f32_e32 v223, v223, v227\n\t"
      "v_accvgpr_write_b32 a85, v223\n\t"
      "v_accvgpr_read_b32 v224, a86\n\t"
      "v_mul_f32_e32 v224, v224, v227\n\t"
      "v_accvgpr_write_b32 a86, v224\n\t"
      "v_accvgpr_read_b32 v225, a87\n\t"
      "v_mul_f32_e32 v225, v225, v227\n\t"
      "v_accvgpr_write_b32 a87, v225\n\t"
      "v_accvgpr_read_b32 v222, a100\n\t"
      "v_mul_f32_e32 v222, v222, v227\n\t"
      "v_accvgpr_write_b32 a100, v222\n\t"
      "v_accvgpr_read_b32 v223, a101\n\t"
      "v_mul_f32_e32 v223, v223, v227\n\t"
      "v_accvgpr_write_b32 a101, v223\n\t"
      "v_accvgpr_read_b32 v224, a102\n\t"
      "v_mul_f32_e32 v224, v224, v227\n\t"
      "v_accvgpr_write_b32 a102, v224\n\t"
      "v_accvgpr_read_b32 v225, a103\n\t"
      "v_mul_f32_e32 v225, v225, v227\n\t"
      "v_accvgpr_write_b32 a103, v225\n\t"
      "v_accvgpr_read_b32 v222, a116\n\t"
      "v_mul_f32_e32 v222, v222, v227\n\t"
      "v_accvgpr_write_b32 a116, v222\n\t"
      "v_accvgpr_read_b32 v223, a117\n\t"
      "v_mul_f32_e32 v223, v223, v227\n\t"
      "v_accvgpr_write_b32 a117, v223\n\t"
      "v_accvgpr_read_b32 v224, a118\n\t"
      "v_mul_f32_e32 v224, v224, v227\n\t"
      "v_accvgpr_write_b32 a118, v224\n\t"
      "v_accvgpr_read_b32 v225, a119\n\t"
      "v_mul_f32_e32 v225, v225, v227\n\t"
      "v_accvgpr_write_b32 a119, v225\n\t"
      "v_mul_f32_e32 v200, v200, v228\n\t"
      "v_sub_f32_e32 v136, v136, v220\n\t"
      "v_sub_f32_e32 v137, v137, v220\n\t"
      "v_sub_f32_e32 v138, v138, v220\n\t"
      "v_sub_f32_e32 v139, v139, v220\n\t"
      "v_sub_f32_e32 v80, v80, v220\n\t"
      "v_sub_f32_e32 v112, v112, v220\n\t"
      "v_sub_f32_e32 v81, v81, v220\n\t"
      "v_sub_f32_e32 v113, v113, v220\n\t"
      "v_sub_f32_e32 v82, v82, v220\n\t"
      "v_sub_f32_e32 v114, v114, v220\n\t"
      "v_sub_f32_e32 v83, v83, v220\n\t"
      "v_sub_f32_e32 v115, v115, v220\n\t"
      "v_sub_f32_e32 v84, v84, v220\n\t"
      "v_sub_f32_e32 v116, v116, v220\n\t"
      "v_sub_f32_e32 v85, v85, v220\n\t"
      "v_sub_f32_e32 v117, v117, v220\n\t"
      "v_sub_f32_e32 v86, v86, v220\n\t"
      "v_sub_f32_e32 v118, v118, v220\n\t"
      "v_sub_f32_e32 v87, v87, v220\n\t"
      "v_sub_f32_e32 v119, v119, v220\n\t"
      "v_accvgpr_read_b32 v222, a8\n\t"
      "v_mul_f32_e32 v222, v222, v228\n\t"
      "v_accvgpr_write_b32 a8, v222\n\t"
      "v_accvgpr_read_b32 v223, a9\n\t"
      "v_mul_f32_e32 v223, v223, v228\n\t"
      "v_accvgpr_write_b32 a9, v223\n\t"
      "v_accvgpr_read_b32 v224, a10\n\t"
      "v_mul_f32_e32 v224, v224, v228\n\t"
      "v_accvgpr_write_b32 a10, v224\n\t"
      "v_accvgpr_read_b32 v225, a11\n\t"
      "v_mul_f32_e32 v225, v225, v228\n\t"
      "v_accvgpr_write_b32 a11, v225\n\t"
      "v_accvgpr_read_b32 v222, a24\n\t"
      "v_mul_f32_e32 v222, v222, v228\n\t"
      "v_accvgpr_write_b32 a24, v222\n\t"
      "v_accvgpr_read_b32 v223, a25\n\t"
      "v_mul_f32_e32 v223, v223, v228\n\t"
      "v_accvgpr_write_b32 a25, v223\n\t"
      "v_accvgpr_read_b32 v224, a26\n\t"
      "v_mul_f32_e32 v224, v224, v228\n\t"
      "v_accvgpr_write_b32 a26, v224\n\t"
      "v_accvgpr_read_b32 v225, a27\n\t"
      "v_mul_f32_e32 v225, v225, v228\n\t"
      "v_accvgpr_write_b32 a27, v225\n\t"
      "v_accvgpr_read_b32 v222, a40\n\t"
      "v_mul_f32_e32 v222, v222, v228\n\t"
      "v_accvgpr_write_b32 a40, v222\n\t"
      "v_accvgpr_read_b32 v223, a41\n\t"
      "v_mul_f32_e32 v223, v223, v228\n\t"
      "v_accvgpr_write_b32 a41, v223\n\t"
      "v_accvgpr_read_b32 v224, a42\n\t"
      "v_mul_f32_e32 v224, v224, v228\n\t"
      "v_accvgpr_write_b32 a42, v224\n\t"
      "v_accvgpr_read_b32 v225, a43\n\t"
      "v_mul_f32_e32 v225, v225, v228\n\t"
      "v_accvgpr_write_b32 a43, v225\n\t"
      "v_accvgpr_read_b32 v222, a56\n\t"
      "v_mul_f32_e32 v222, v222, v228\n\t"
      "v_accvgpr_write_b32 a56, v222\n\t"
      "v_accvgpr_read_b32 v223, a57\n\t"
      "v_mul_f32_e32 v223, v223, v228\n\t"
      "v_accvgpr_write_b32 a57, v223\n\t"
      "v_accvgpr_read_b32 v224, a58\n\t"
      "v_mul_f32_e32 v224, v224, v228\n\t"
      "v_accvgpr_write_b32 a58, v224\n\t"
      "v_accvgpr_read_b32 v225, a59\n\t"
      "v_mul_f32_e32 v225, v225, v228\n\t"
      "v_accvgpr_write_b32 a59, v225\n\t"
      "v_accvgpr_read_b32 v222, a72\n\t"
      "v_mul_f32_e32 v222, v222, v228\n\t"
      "v_accvgpr_write_b32 a72, v222\n\t"
      "v_accvgpr_read_b32 v223, a73\n\t"
      "v_mul_f32_e32 v223, v223, v228\n\t"
      "v_accvgpr_write_b32 a73, v223\n\t"
      "v_accvgpr_read_b32 v224, a74\n\t"
      "v_mul_f32_e32 v224, v224, v228\n\t"
      "v_accvgpr_write_b32 a74, v224\n\t"
      "v_accvgpr_read_b32 v225, a75\n\t"
      "v_mul_f32_e32 v225, v225, v228\n\t"
      "v_accvgpr_write_b32 a75, v225\n\t"
      "v_accvgpr_read_b32 v222, a88\n\t"
      "v_mul_f32_e32 v222, v222, v228\n\t"
      "v_accvgpr_write_b32 a88, v222\n\t"
      "v_accvgpr_read_b32 v223, a89\n\t"
      "v_mul_f32_e32 v223, v223, v228\n\t"
      "v_accvgpr_write_b32 a89, v223\n\t"
      "v_accvgpr_read_b32 v224, a90\n\t"
      "v_mul_f32_e32 v224, v224, v228\n\t"
      "v_accvgpr_write_b32 a90, v224\n\t"
      "v_accvgpr_read_b32 v225, a91\n\t"
      "v_mul_f32_e32 v225, v225, v228\n\t"
      "v_accvgpr_write_b32 a91, v225\n\t"
      "v_accvgpr_read_b32 v222, a104\n\t"
      "v_mul_f32_e32 v222, v222, v228\n\t"
      "v_accvgpr_write_b32 a104, v222\n\t"
      "v_accvgpr_read_b32 v223, a105\n\t"
      "v_mul_f32_e32 v223, v223, v228\n\t"
      "v_accvgpr_write_b32 a105, v223\n\t"
      "v_accvgpr_read_b32 v224, a106\n\t"
      "v_mul_f32_e32 v224, v224, v228\n\t"
      "v_accvgpr_write_b32 a106, v224\n\t"
      "v_accvgpr_read_b32 v225, a107\n\t"
      "v_mul_f32_e32 v225, v225, v228\n\t"
      "v_accvgpr_write_b32 a107, v225\n\t"
      "v_accvgpr_read_b32 v222, a120\n\t"
      "v_mul_f32_e32 v222, v222, v228\n\t"
      "v_accvgpr_write_b32 a120, v222\n\t"
      "v_accvgpr_read_b32 v223, a121\n\t"
      "v_mul_f32_e32 v223, v223, v228\n\t"
      "v_accvgpr_write_b32 a121, v223\n\t"
      "v_accvgpr_read_b32 v224, a122\n\t"
      "v_mul_f32_e32 v224, v224, v228\n\t"
      "v_accvgpr_write_b32 a122, v224\n\t"
      "v_accvgpr_read_b32 v225, a123\n\t"
      "v_mul_f32_e32 v225, v225, v228\n\t"
      "v_accvgpr_write_b32 a123, v225\n\t"
      "v_mul_f32_e32 v204, v204, v229\n\t"
      "v_sub_f32_e32 v140, v140, v221\n\t"
      "v_sub_f32_e32 v141, v141, v221\n\t"
      "v_sub_f32_e32 v142, v142, v221\n\t"
      "v_sub_f32_e32 v143, v143, v221\n\t"
      "v_sub_f32_e32 v88, v88, v221\n\t"
      "v_sub_f32_e32 v120, v120, v221\n\t"
      "v_sub_f32_e32 v89, v89, v221\n\t"
      "v_sub_f32_e32 v121, v121, v221\n\t"
      "v_sub_f32_e32 v90, v90, v221\n\t"
      "v_sub_f32_e32 v122, v122, v221\n\t"
      "v_sub_f32_e32 v91, v91, v221\n\t"
      "v_sub_f32_e32 v123, v123, v221\n\t"
      "v_sub_f32_e32 v92, v92, v221\n\t"
      "v_sub_f32_e32 v124, v124, v221\n\t"
      "v_sub_f32_e32 v93, v93, v221\n\t"
      "v_sub_f32_e32 v125, v125, v221\n\t"
      "v_sub_f32_e32 v94, v94, v221\n\t"
      "v_sub_f32_e32 v126, v126, v221\n\t"
      "v_sub_f32_e32 v95, v95, v221\n\t"
      "v_sub_f32_e32 v127, v127, v221\n\t"
      "v_accvgpr_read_b32 v222, a12\n\t"
      "v_mul_f32_e32 v222, v222, v229\n\t"
      "v_accvgpr_write_b32 a12, v222\n\t"
      "v_accvgpr_read_b32 v223, a13\n\t"
      "v_mul_f32_e32 v223, v223, v229\n\t"
      "v_accvgpr_write_b32 a13, v223\n\t"
      "v_accvgpr_read_b32 v224, a14\n\t"
      "v_mul_f32_e32 v224, v224, v229\n\t"
      "v_accvgpr_write_b32 a14, v224\n\t"
      "v_accvgpr_read_b32 v225, a15\n\t"
      "v_mul_f32_e32 v225, v225, v229\n\t"
      "v_accvgpr_write_b32 a15, v225\n\t"
      "v_accvgpr_read_b32 v222, a28\n\t"
      "v_mul_f32_e32 v222, v222, v229\n\t"
      "v_accvgpr_write_b32 a28, v222\n\t"
      "v_accvgpr_read_b32 v223, a29\n\t"
      "v_mul_f32_e32 v223, v223, v229\n\t"
      "v_accvgpr_write_b32 a29, v223\n\t"
      "v_accvgpr_read_b32 v224, a30\n\t"
      "v_mul_f32_e32 v224, v224, v229\n\t"
      "v_accvgpr_write_b32 a30, v224\n\t"
      "v_accvgpr_read_b32 v225, a31\n\t"
      "v_mul_f32_e32 v225, v225, v229\n\t"
      "v_accvgpr_write_b32 a31, v225\n\t"
      "v_accvgpr_read_b32 v222, a44\n\t"
      "v_mul_f32_e32 v222, v222, v229\n\t"
      "v_accvgpr_write_b32 a44, v222\n\t"
      "v_accvgpr_read_b32 v223, a45\n\t"
      "v_mul_f32_e32 v223, v223, v229\n\t"
      "v_accvgpr_write_b32 a45, v223\n\t"
      "v_accvgpr_read_b32 v224, a46\n\t"
      "v_mul_f32_e32 v224, v224, v229\n\t"
      "v_accvgpr_write_b32 a46, v224\n\t"
      "v_accvgpr_read_b32 v225, a47\n\t"
      "v_mul_f32_e32 v225, v225, v229\n\t"
      "v_accvgpr_write_b32 a47, v225\n\t"
      "v_accvgpr_read_b32 v222, a60\n\t"
      "v_mul_f32_e32 v222, v222, v229\n\t"
      "v_accvgpr_write_b32 a60, v222\n\t"
      "v_accvgpr_read_b32 v223, a61\n\t"
      "v_mul_f32_e32 v223, v223, v229\n\t"
      "v_accvgpr_write_b32 a61, v223\n\t"
      "v_accvgpr_read_b32 v224, a62\n\t"
      "v_mul_f32_e32 v224, v224, v229\n\t"
      "v_accvgpr_write_b32 a62, v224\n\t"
      "v_accvgpr_read_b32 v225, a63\n\t"
      "v_mul_f32_e32 v225, v225, v229\n\t"
      "v_accvgpr_write_b32 a63, v225\n\t"
      "v_accvgpr_read_b32 v222, a76\n\t"
      "v_mul_f32_e32 v222, v222, v229\n\t"
      "v_accvgpr_write_b32 a76, v222\n\t"
      "v_accvgpr_read_b32 v223, a77\n\t"
      "v_mul_f32_e32 v223, v223, v229\n\t"
      "v_accvgpr_write_b32 a77, v223\n\t"
      "v_accvgpr_read_b32 v224, a78\n\t"
      "v_mul_f32_e32 v224, v224, v229\n\t"
      "v_accvgpr_write_b32 a78, v224\n\t"
      "v_accvgpr_read_b32 v225, a79\n\t"
      "v_mul_f32_e32 v225, v225, v229\n\t"
      "v_accvgpr_write_b32 a79, v225\n\t"
      "v_accvgpr_read_b32 v222, a92\n\t"
      "v_mul_f32_e32 v222, v222, v229\n\t"
      "v_accvgpr_write_b32 a92, v222\n\t"
      "v_accvgpr_read_b32 v223, a93\n\t"
      "v_mul_f32_e32 v223, v223, v229\n\t"
      "v_accvgpr_write_b32 a93, v223\n\t"
      "v_accvgpr_read_b32 v224, a94\n\t"
      "v_mul_f32_e32 v224, v224, v229\n\t"
      "v_accvgpr_write_b32 a94, v224\n\t"
      "v_accvgpr_read_b32 v225, a95\n\t"
      "v_mul_f32_e32 v225, v225, v229\n\t"
      "v_accvgpr_write_b32 a95, v225\n\t"
      "v_accvgpr_read_b32 v222, a108\n\t"
      "v_mul_f32_e32 v222, v222, v229\n\t"
      "v_accvgpr_write_b32 a108, v222\n\t"
      "v_accvgpr_read_b32 v223, a109\n\t"
      "v_mul_f32_e32 v223, v223, v229\n\t"
      "v_accvgpr_write_b32 a109, v223\n\t"
      "v_accvgpr_read_b32 v224, a110\n\t"
      "v_mul_f32_e32 v224, v224, v229\n\t"
      "v_accvgpr_write_b32 a110, v224\n\t"
      "v_accvgpr_read_b32 v225, a111\n\t"
      "v_mul_f32_e32 v225, v225, v229\n\t"
      "v_accvgpr_write_b32 a111, v225\n\t"
      "v_accvgpr_read_b32 v222, a124\n\t"
      "v_mul_f32_e32 v222, v222, v229\n\t"
      "v_accvgpr_write_b32 a124, v222\n\t"
      "v_accvgpr_read_b32 v223, a125\n\t"
      "v_mul_f32_e32 v223, v223, v229\n\t"
      "v_accvgpr_write_b32 a125, v223\n\t"
      "v_accvgpr_read_b32 v224, a126\n\t"
      "v_mul_f32_e32 v224, v224, v229\n\t"
      "v_accvgpr_write_b32 a126, v224\n\t"
      "v_accvgpr_read_b32 v225, a127\n\t"
      "v_mul_f32_e32 v225, v225, v229\n\t"
      "v_accvgpr_write_b32 a127, v225\n\t"
      "v_exp_f32_e32 v208, v64\n\t"
      "v_exp_f32_e32 v209, v65\n\t"
      "s_nop 0\n\t"
      "v_cvt_pk_bf16_f32 v144, v208, v209\n\t"
      "v_exp_f32_e32 v210, v66\n\t"
      "v_exp_f32_e32 v211, v67\n\t"
      "s_nop 0\n\t"
      "v_cvt_pk_bf16_f32 v145, v210, v211\n\t"
      "v_exp_f32_e32 v212, v68\n\t"
      "v_exp_f32_e32 v213, v69\n\t"
      "s_nop 0\n\t"
      "v_cvt_pk_bf16_f32 v146, v212, v213\n\t"
      "v_exp_f32_e32 v214, v70\n\t"
      "v_exp_f32_e32 v215, v71\n\t"
      "s_nop 0\n\t"
      "v_cvt_pk_bf16_f32 v147, v214, v215\n\t"
      "v_exp_f32_e32 v208, v72\n\t"
      "v_exp_f32_e32 v209, v73\n\t"
      "s_nop 0\n\t"
      "v_cvt_pk_bf16_f32 v148, v208, v209\n\t"
      "v_exp_f32_e32 v210, v74\n\t"
      "v_exp_f32_e32 v211, v75\n\t"
      "s_nop 0\n\t"
      "v_cvt_pk_bf16_f32 v149, v210, v211\n\t"
      "v_exp_f32_e32 v212, v76\n\t"
      "v_exp_f32_e32 v213, v77\n\t"
      "s_nop 0\n\t"
      "v_cvt_pk_bf16_f32 v150, v212, v213\n\t"
      "v_exp_f32_e32 v214, v78\n\t"
      "v_exp_f32_e32 v215, v79\n\t"
      "s_nop 0\n\t"
      "v_cvt_pk_bf16_f32 v151, v214, v215\n\t"
      "v_exp_f32_e32 v208, v80\n\t"
      "v_exp_f32_e32 v209, v81\n\t"
      "s_nop 0\n\t"
      "v_cvt_pk_bf16_f32 v152, v208, v209\n\t"
      "v_exp_f32_e32 v210, v82\n\t"
      "v_exp_f32_e32 v211, v83\n\t"
      "s_nop 0\n\t"
      "v_cvt_pk_bf16_f32 v153, v210, v211\n\t"
      "v_exp_f32_e32 v212, v84\n\t"
      "v_exp_f32_e32 v213, v85\n\t"
      "s_nop 0\n\t"
      "v_cvt_pk_bf16_f32 v154, v212, v213\n\t"
      "v_exp_f32_e32 v214, v86\n\t"
      "v_exp_f32_e32 v215, v87\n\t"
      "s_nop 0\n\t"
      "v_cvt_pk_bf16_f32 v155, v214, v215\n\t"
      "v_exp_f32_e32 v208, v88\n\t"
      "v_exp_f32_e32 v209, v89\n\t"
      "s_nop 0\n\t"
      "v_cvt_pk_bf16_f32 v156, v208, v209\n\t"
      "v_exp_f32_e32 v210, v90\n\t"
      "v_exp_f32_e32 v211, v91\n\t"
      "s_nop 0\n\t"
      "v_cvt_pk_bf16_f32 v157, v210, v211\n\t"
      "v_exp_f32_e32 v212, v92\n\t"
      "v_exp_f32_e32 v213, v93\n\t"
      "s_nop 0\n\t"
      "v_cvt_pk_bf16_f32 v158, v212, v213\n\t"
      "v_exp_f32_e32 v214, v94\n\t"
      "v_exp_f32_e32 v215, v95\n\t"
      "s_nop 0\n\t"
      "v_cvt_pk_bf16_f32 v159, v214, v215\n\t"
      "s_nop 1\n\t"
      "v_mfma_f32_16x16x32_bf16 v[176:179], a[192:195], v[144:147], v[192:195]\n\t"
      "v_mfma_f32_16x16x32_bf16 v[180:183], a[192:195], v[148:151], v[196:199]\n\t"
      "v_mfma_f32_16x16x32_bf16 v[184:187], a[192:195], v[152:155], v[200:203]\n\t"
      "v_mfma_f32_16x16x32_bf16 v[188:191], a[192:195], v[156:159], v[204:207]\n\t"
      "v_exp_f32_e32 v208, v96\n\t"
      "v_exp_f32_e32 v209, v97\n\t"
      "s_nop 0\n\t"
      "v_cvt_pk_bf16_f32 v160, v208, v209\n\t"
      "v_exp_f32_e32 v210, v98\n\t"
      "v_exp_f32_e32 v211, v99\n\t"
      "s_nop 0\n\t"
      "v_cvt_pk_bf16_f32 v161, v210, v211\n\t"
      "v_exp_f32_e32 v212, v100\n\t"
      "v_exp_f32_e32 v213, v101\n\t"
      "s_nop 0\n\t"
      "v_cvt_pk_bf16_f32 v162, v212, v213\n\t"
      "v_exp_f32_e32 v214, v102\n\t"
      "s_nop 7\n\t"
      "s_cmp_eq_u32 s64, 1\n\t"
      "s_cbranch_scc1 L_BACK1\n\t"
      "s_cmp_eq_u32 s64, 3\n\t"
      "s_cbranch_scc1 L_BACK3\n\t"
      "s_cmp_eq_u32 s64, 5\n\t"
      "s_cbranch_scc1 L_BACK5\n\t"
      "s_cmp_eq_u32 s64, 7\n\t"
      "s_cbranch_scc1 L_BACK7\n\t"
      "s_endpgm\n\t"
      "L_RARER1:\n\t"
      "s_nop 15\n\t"
      "v_log_f32_e32 v222, v176\n\t"
      "v_log_f32_e32 v223, v180\n\t"
      "v_log_f32_e32 v224, v184\n\t"
      "v_log_f32_e32 v225, v188\n\t"
      "v_max3_f32 v218, v96, v97, v98\n\t"
      "v_max3_f32 v218, v218, v99, v100\n\t"
      "v_max3_f32 v218, v218, v101, v102\n\t"
      "v_max3_f32 v218, v218, v103, v222\n\t"
      "v_max_f32_e32 v218, 0, v218\n\t"
      "v_max3_f32 v219, v104, v105, v106\n\t"
      "v_max3_f32 v219, v219, v107, v108\n\t"
      "v_max3_f32 v219, v219, v109, v110\n\t"
      "v_max3_f32 v219, v219, v111, v223\n\t"
      "v_max_f32_e32 v219, 0, v219\n\t"
      "v_max3_f32 v220, v112, v113, v114\n\t"
      "v_max3_f32 v220, v220, v115, v116\n\t"
      "v_max3_f32 v220, v220, v117, v118\n\t"
      "v_max3_f32 v220, v220, v119, v224\n\t"
      "v_max_f32_e32 v220, 0, v220\n\t"
      "v_max3_f32 v221, v120, v121, v122\n\t"
      "v_max3_f32 v221, v221, v123, v124\n\t"
      "v_max3_f32 v221, v221, v125, v126\n\t"
      "v_max3_f32 v221, v221, v127, v225\n\t"
      "v_max_f32_e32 v221, 0, v221\n\t"
      "v_mov_b32_e32 v222, v218\n\t"
      "v_mov_b32_e32 v223, v219\n\t"
      "v_mov_b32_e32 v224, v220\n\t"
      "v_mov_b32_e32 v225, v221\n\t"
      "s_nop 1\n\t"
      "v_permlane16_swap_b32_e32 v218, v222\n\t"
      "v_permlane16_swap_b32_e32 v219, v223\n\t"
      "v_permlane16_swap_b32_e32 v220, v224\n\t"
      "v_permlane16_swap_b32_e32 v221, v225\n\t"
      "s_nop 0\n\t"
      "v_max_f32_e32 v218, v218, v222\n\t"
      "v_max_f32_e32 v219, v219, v223\n\t"
      "v_max_f32_e32 v220, v220, v224\n\t"
      "v_max_f32_e32 v221, v221, v225\n\t"
      "v_mov_b32_e32 v222, v218\n\t"
      "v_mov_b32_e32 v223, v219\n\t"
      "v_mov_b32_e32 v224, v220\n\t"
      "v_mov_b32_e32 v225, v221\n\t"
      "s_nop 1\n\t"
      "v_permlane32_swap_b32_e32 v218, v222\n\t"
      "v_permlane32_swap_b32_e32 v219, v223\n\t"
      "v_permlane32_swap_b32_e32 v220, v224\n\t"
      "v_permlane32_swap_b32_e32 v221, v225\n\t"
      "s_nop 0\n\t"
      "v_max_f32_e32 v218, v218, v222\n\t"
      "v_max_f32_e32 v219, v219, v223\n\t"
      "v_max_f32_e32 v220, v220, v224\n\t"
      "v_max_f32_e32 v221, v221, v225\n\t"
      "v_exp_f32_e64 v226, -v218\n\t"
      "v_exp_f32_e64 v227, -v219\n\t"
      "v_exp_f32_e64 v228, -v220\n\t"
      "v_exp_f32_e64 v229, -v221\n\t"
      "s_nop 0\n\t"
      "v_mul_f32_e32 v176, v176, v226\n\t"
      "v_sub_f32_e32 v128, v128, v218\n\t"
      "v_sub_f32_e32 v129, v129, v218\n\t"
      "v_sub_f32_e32 v130, v130, v218\n\t"
      "v_sub_f32_e32 v131, v131, v218\n\t"
      "v_sub_f32_e32 v96, v96, v218\n\t"
      "v_sub_f32_e32 v64, v64, v218\n\t"
      "v_sub_f32_e32 v97, v97, v218\n\t"
      "v_sub_f32_e32 v65, v65, v218\n\t"
      "v_sub_f32_e32 v98, v98, v218\n\t"
      "v_sub_f32_e32 v66, v66, v218\n\t"
      "v_sub_f32_e32 v99, v99, v218\n\t"
      "v_sub_f32_e32 v67, v67, v218\n\t"
      "v_sub_f32_e32 v100, v100, v218\n\t"
      "v_sub_f32_e32 v68, v68, v218\n\t"
      "v_sub_f32_e32 v101, v101, v218\n\t"
      "v_sub_f32_e32 v69, v69, v218\n\t"
      "v_sub_f32_e32 v102, v102, v218\n\t"
      "v_sub_f32_e32 v70, v70, v218\n\t"
      "v_sub_f32_e32 v103, v103, v218\n\t"
      "v_sub_f32_e32 v71, v71, v218\n\t"
      "v_accvgpr_read_b32 v222, a0\n\t"
      "v_mul_f32_e32 v222, v222, v226\n\t"
      "v_accvgpr_write_b32 a0, v222\n\t"
      "v_accvgpr_read_b32 v223, a1\n\t"
      "v_mul_f32_e32 v223, v223, v226\n\t"
      "v_accvgpr_write_b32 a1, v223\n\t"
      "v_accvgpr_read_b32 v224, a2\n\t"
      "v_mul_f32_e32 v224, v224, v226\n\t"
      "v_accvgpr_write_b32 a2, v224\n\t"
      "v_accvgpr_read_b32 v225, a3\n\t"
      "v_mul_f32_e32 v225, v225, v226\n\t"
      "v_accvgpr_write_b32 a3, v225\n\t"
      "v_accvgpr_read_b32 v222, a16\n\t"
      "v_mul_f32_e32 v222, v222, v226\n\t"
      "v_accvgpr_write_b32 a16, v222\n\t"
      "v_accvgpr_read_b32 v223, a17\n\t"
      "v_mul_f32_e32 v223, v223, v226\n\t"
      "v_accvgpr_write_b32 a17, v223\n\t"
      "v_accvgpr_read_b32 v224, a18\n\t"
      "v_mul_f32_e32 v224, v224, v226\n\t"
      "v_accvgpr_write_b32 a18, v224\n\t"
      "v_accvgpr_read_b32 v225, a19\n\t"
      "v_mul_f32_e32 v225, v225, v226\n\t"
      "v_accvgpr_write_b32 a19, v225\n\t"
      "v_accvgpr_read_b32 v222, a32\n\t"
      "v_mul_f32_e32 v222, v222, v226\n\t"
      "v_accvgpr_write_b32 a32, v222\n\t"
      "v_accvgpr_read_b32 v223, a33\n\t"
      "v_mul_f32_e32 v223, v223, v226\n\t"
      "v_accvgpr_write_b32 a33, v223\n\t"
      "v_accvgpr_read_b32 v224, a34\n\t"
      "v_mul_f32_e32 v224, v224, v226\n\t"
      "v_accvgpr_write_b32 a34, v224\n\t"
      "v_accvgpr_read_b32 v225, a35\n\t"
      "v_mul_f32_e32 v225, v225, v226\n\t"
      "v_accvgpr_write_b32 a35, v225\n\t"
      "v_accvgpr_read_b32 v222, a48\n\t"
      "v_mul_f32_e32 v222, v222, v226\n\t"
      "v_accvgpr_write_b32 a48, v222\n\t"
      "v_accvgpr_read_b32 v223, a49\n\t"
      "v_mul_f32_e32 v223, v223, v226\n\t"
      "v_accvgpr_write_b32 a49, v223\n\t"
      "v_accvgpr_read_b32 v224, a50\n\t"
      "v_mul_f32_e32 v224, v224, v226\n\t"
      "v_accvgpr_write_b32 a50, v224\n\t"
      "v_accvgpr_read_b32 v225, a51\n\t"
      "v_mul_f32_e32 v225, v225, v226\n\t"
      "v_accvgpr_write_b32 a51, v225\n\t"
      "v_accvgpr_read_b32 v222, a64\n\t"
      "v_mul_f32_e32 v222, v222, v226\n\t"
      "v_accvgpr_write_b32 a64, v222\n\t"
      "v_accvgpr_read_b32 v223, a65\n\t"
      "v_mul_f32_e32 v223, v223, v226\n\t"
      "v_accvgpr_write_b32 a65, v223\n\t"
      "v_accvgpr_read_b32 v224, a66\n\t"
      "v_mul_f32_e32 v224, v224, v226\n\t"
      "v_accvgpr_write_b32 a66, v224\n\t"
      "v_accvgpr_read_b32 v225, a67\n\t"
      "v_mul_f32_e32 v225, v225, v226\n\t"
      "v_accvgpr_write_b32 a67, v225\n\t"
      "v_accvgpr_read_b32 v222, a80\n\t"
      "v_mul_f32_e32 v222, v222, v226\n\t"
      "v_accvgpr_write_b32 a80, v222\n\t"
      "v_accvgpr_read_b32 v223, a81\n\t"
      "v_mul_f32_e32 v223, v223, v226\n\t"
      "v_accvgpr_write_b32 a81, v223\n\t"
      "v_accvgpr_read_b32 v224, a82\n\t"
      "v_mul_f32_e32 v224, v224, v226\n\t"
      "v_accvgpr_write_b32 a82, v224\n\t"
      "v_accvgpr_read_b32 v225, a83\n\t"
      "v_mul_f32_e32 v225, v225, v226\n\t"
      "v_accvgpr_write_b32 a83, v225\n\t"
      "v_accvgpr_read_b32 v222, a96\n\t"
      "v_mul_f32_e32 v222, v222, v226\n\t"
      "v_accvgpr_write_b32 a96, v222\n\t"
      "v_accvgpr_read_b32 v223, a97\n\t"
      "v_mul_f32_e32 v223, v223, v226\n\t"
      "v_accvgpr_write_b32 a97, v223\n\t"
      "v_accvgpr_read_b32 v224, a98\n\t"
      "v_mul_f32_e32 v224, v224, v226\n\t"
      "v_accvgpr_write_b32 a98, v224\n\t"
      "v_accvgpr_read_b32 v225, a99\n\t"
      "v_mul_f32_e32 v225, v225, v226\n\t"
      "v_accvgpr_write_b32 a99, v225\n\t"
      "v_accvgpr_read_b32 v222, a112\n\t"
      "v_mul_f32_e32 v222, v222, v226\n\t"
      "v_accvgpr_write_b32 a112, v222\n\t"
      "v_accvgpr_read_b32 v223, a113\n\t"
      "v_mul_f32_e32 v223, v223, v226\n\t"
      "v_accvgpr_write_b32 a113, v223\n\t"
      "v_accvgpr_read_b32 v224, a114\n\t"
      "v_mul_f32_e32 v224, v224, v226\n\t"
      "v_accvgpr_write_b32 a114, v224\n\t"
      "v_accvgpr_read_b32 v225, a115\n\t"
      "v_mul_f32_e32 v225, v225, v226\n\t"
      "v_accvgpr_write_b32 a115, v225\n\t"
      "v_mul_f32_e32 v180, v180, v227\n\t"
      "v_sub_f32_e32 v132, v132, v219\n\t"
      "v_sub_f32_e32 v133, v133, v219\n\t"
      "v_sub_f32_e32 v134, v134, v219\n\t"
      "v_sub_f32_e32 v135, v135, v219\n\t"
      "v_sub_f32_e32 v104, v104, v219\n\t"
      "v_sub_f32_e32 v72, v72, v219\n\t"
      "v_sub_f32_e32 v105, v105, v219\n\t"
      "v_sub_f32_e32 v73, v73, v219\n\t"
      "v_sub_f32_e32 v106, v106, v219\n\t"
      "v_sub_f32_e32 v74, v74, v219\n\t"
      "v_sub_f32_e32 v107, v107, v219\n\t"
      "v_sub_f32_e32 v75, v75, v219\n\t"
      "v_sub_f32_e32 v108, v108, v219\n\t"
      "v_sub_f32_e32 v76, v76, v219\n\t"
      "v_sub_f32_e32 v109, v109, v219\n\t"
      "v_sub_f32_e32 v77, v77, v219\n\t"
      "v_sub_f32_e32 v110, v110, v219\n\t"
      "v_sub_f32_e32 v78, v78, v219\n\t"
      "v_sub_f32_e32 v111, v111, v219\n\t"
      "v_sub_f32_e32 v79, v79, v219\n\t"
      "v_accvgpr_read_b32 v222, a4\n\t"
      "v_mul_f32_e32 v222, v222, v227\n\t"
      "v_accvgpr_write_b32 a4, v222\n\t"
      "v_accvgpr_read_b32 v223, a5\n\t"
      "v_mul_f32_e32 v223, v223, v227\n\t"
      "v_accvgpr_write_b32 a5, v223\n\t"
      "v_accvgpr_read_b32 v224, a6\n\t"
      "v_mul_f32_e32 v224, v224, v227\n\t"
      "v_accvgpr_write_b32 a6, v224\n\t"
      "v_accvgpr_read_b32 v225, a7\n\t"
      "v_mul_f32_e32 v225, v225, v227\n\t"
      "v_accvgpr_write_b32 a7, v225\n\t"
      "v_accvgpr_read_b32 v222, a20\n\t"
      "v_mul_f32_e32 v222, v222, v227\n\t"
      "v_accvgpr_write_b32 a20, v222\n\t"
      "v_accvgpr_read_b32 v223, a21\n\t"
      "v_mul_f32_e32 v223, v223, v227\n\t"
      "v_accvgpr_write_b32 a21, v223\n\t"
      "v_accvgpr_read_b32 v224, a22\n\t"
      "v_mul_f32_e32 v224, v224, v227\n\t"
      "v_accvgpr_write_b32 a22, v224\n\t"
      "v_accvgpr_read_b32 v225, a23\n\t"
      "v_mul_f32_e32 v225, v225, v227\n\t"
      "v_accvgpr_write_b32 a23, v225\n\t"
      "v_accvgpr_read_b32 v222, a36\n\t"
      "v_mul_f32_e32 v222, v222, v227\n\t"
      "v_accvgpr_write_b32 a36, v222\n\t"
      "v_accvgpr_read_b32 v223, a37\n\t"
      "v_mul_f32_e32 v223, v223, v227\n\t"
      "v_accvgpr_write_b32 a37, v223\n\t"
      "v_accvgpr_read_b32 v224, a38\n\t"
      "v_mul_f32_e32 v224, v224, v227\n\t"
      "v_accvgpr_write_b32 a38, v224\n\t"
      "v_accvgpr_read_b32 v225, a39\n\t"
      "v_mul_f32_e32 v225, v225, v227\n\t"
      "v_accvgpr_write_b32 a39, v225\n\t"
      "v_accvgpr_read_b32 v222, a52\n\t"
      "v_mul_f32_e32 v222, v222, v227\n\t"
      "v_accvgpr_write_b32 a52, v222\n\t"
      "v_accvgpr_read_b32 v223, a53\n\t"
      "v_mul_f32_e32 v223, v223, v227\n\t"
      "v_accvgpr_write_b32 a53, v223\n\t"
      "v_accvgpr_read_b32 v224, a54\n\t"
      "v_mul_f32_e32 v224, v224, v227\n\t"
      "v_accvgpr_write_b32 a54, v224\n\t"
      "v_accvgpr_read_b32 v225, a55\n\t"
      "v_mul_f32_e32 v225, v225, v227\n\t"
      "v_accvgpr_write_b32 a55, v225\n\t"
      "v_accvgpr_read_b32 v222, a68\n\t"
      "v_mul_f32_e32 v222, v222, v227\n\t"
      "v_accvgpr_write_b32 a68, v222\n\t"
      "v_accvgpr_read_b32 v223, a69\n\t"
      "v_mul_f32_e32 v223, v223, v227\n\t"
      "v_accvgpr_write_b32 a69, v223\n\t"
      "v_accvgpr_read_b32 v224, a70\n\t"
      "v_mul_f32_e32 v224, v224, v227\n\t"
      "v_accvgpr_write_b32 a70, v224\n\t"
      "v_accvgpr_read_b32 v225, a71\n\t"
      "v_mul_f32_e32 v225, v225, v227\n\t"
      "v_accvgpr_write_b32 a71, v225\n\t"
      "v_accvgpr_read_b32 v222, a84\n\t"
      "v_mul_f32_e32 v222, v222, v227\n\t"
      "v_accvgpr_write_b32 a84, v222\n\t"
      "v_accvgpr_read_b32 v223, a85\n\t"
      "v_mul_f32_e32 v223, v223, v227\n\t"
      "v_accvgpr_write_b32 a85, v223\n\t"
      "v_accvgpr_read_b32 v224, a86\n\t"
      "v_mul_f32_e32 v224, v224, v227\n\t"
      "v_accvgpr_write_b32 a86, v224\n\t"
      "v_accvgpr_read_b32 v225, a87\n\t"
      "v_mul_f32_e32 v225, v225, v227\n\t"
      "v_accvgpr_write_b32 a87, v225\n\t"
      "v_accvgpr_read_b32 v222, a100\n\t"
      "v_mul_f32_e32 v222, v222, v227\n\t"
      "v_accvgpr_write_b32 a100, v222\n\t"
      "v_accvgpr_read_b32 v223, a101\n\t"
      "v_mul_f32_e32 v223, v223, v227\n\t"
      "v_accvgpr_write_b32 a101, v223\n\t"
      "v_accvgpr_read_b32 v224, a102\n\t"
      "v_mul_f32_e32 v224, v224, v227\n\t"
      "v_accvgpr_write_b32 a102, v224\n\t"
      "v_accvgpr_read_b32 v225, a103\n\t"
      "v_mul_f32_e32 v225, v225, v227\n\t"
      "v_accvgpr_write_b32 a103, v225\n\t"
      "v_accvgpr_read_b32 v222, a116\n\t"
      "v_mul_f32_e32 v222, v222, v227\n\t"
      "v_accvgpr_write_b32 a116, v222\n\t"
      "v_accvgpr_read_b32 v223, a117\n\t"
      "v_mul_f32_e32 v223, v223, v227\n\t"
      "v_accvgpr_write_b32 a117, v223\n\t"
      "v_accvgpr_read_b32 v224, a118\n\t"
      "v_mul_f32_e32 v224, v224, v227\n\t"
      "v_accvgpr_write_b32 a118, v224\n\t"
      "v_accvgpr_read_b32 v225, a119\n\t"
      "v_mul_f32_e32 v225, v225, v227\n\t"
      "v_accvgpr_write_b32 a119, v225\n\t"
      "v_mul_f32_e32 v184, v184, v228\n\t"
      "v_sub_f32_e32 v136, v136, v220\n\t"
      "v_sub_f32_e32 v137, v137, v220\n\t"
      "v_sub_f32_e32 v138, v138, v220\n\t"
      "v_sub_f32_e32 v139, v139, v220\n\t"
      "v_sub_f32_e32 v112, v112, v220\n\t"
      "v_sub_f32_e32 v80, v80, v220\n\t"
      "v_sub_f32_e32 v113, v113, v220\n\t"
      "v_sub_f32_e32 v81, v81, v220\n\t"
      "v_sub_f32_e32 v114, v114, v220\n\t"
      "v_sub_f32_e32 v82, v82, v220\n\t"
      "v_sub_f32_e32 v115, v115, v220\n\t"
      "v_sub_f32_e32 v83, v83, v220\n\t"
      "v_sub_f32_e32 v116, v116, v220\n\t"
      "v_sub_f32_e32 v84, v84, v220\n\t"
      "v_sub_f32_e32 v117, v117, v220\n\t"
      "v_sub_f32_e32 v85, v85, v220\n\t"
      "v_sub_f32_e32 v118, v118, v220\n\t"
      "v_sub_f32_e32 v86, v86, v220\n\t"
      "v_sub_f32_e32 v119, v119, v220\n\t"
      "v_sub_f32_e32 v87, v87, v220\n\t"
      "v_accvgpr_read_b32 v222, a8\n\t"
      "v_mul_f32_e32 v222, v222, v228\n\t"
      "v_accvgpr_write_b32 a8, v222\n\t"
      "v_accvgpr_read_b32 v223, a9\n\t"
      "v_mul_f32_e32 v223, v223, v228\n\t"
      "v_accvgpr_write_b32 a9, v223\n\t"
      "v_accvgpr_read_b32 v224, a10\n\t"
      "v_mul_f32_e32 v224, v224, v228\n\t"
      "v_accvgpr_write_b32 a10, v224\n\t"
      "v_accvgpr_read_b32 v225, a11\n\t"
      "v_mul_f32_e32 v225, v225, v228\n\t"
      "v_accvgpr_write_b32 a11, v225\n\t"
      "v_accvgpr_read_b32 v222, a24\n\t"
      "v_mul_f32_e32 v222, v222, v228\n\t"
      "v_accvgpr_write_b32 a24, v222\n\t"
      "v_accvgpr_read_b32 v223, a25\n\t"
      "v_mul_f32_e32 v223, v223, v228\n\t"
      "v_accvgpr_write_b32 a25, v223\n\t"
      "v_accvgpr_read_b32 v224, a26\n\t"
      "v_mul_f32_e32 v224, v224, v228\n\t"
      "v_accvgpr_write_b32 a26, v224\n\t"
      "v_accvgpr_read_b32 v225, a27\n\t"
      "v_mul_f32_e32 v225, v225, v228\n\t"
      "v_accvgpr_write_b32 a27, v225\n\t"
      "v_accvgpr_read_b32 v222, a40\n\t"
      "v_mul_f32_e32 v222, v222, v228\n\t"
      "v_accvgpr_write_b32 a40, v222\n\t"
      "v_accvgpr_read_b32 v223, a41\n\t"
      "v_mul_f32_e32 v223, v223, v228\n\t"
      "v_accvgpr_write_b32 a41, v223\n\t"
      "v_accvgpr_read_b32 v224, a42\n\t"
      "v_mul_f32_e32 v224, v224, v228\n\t"
      "v_accvgpr_write_b32 a42, v224\n\t"
      "v_accvgpr_read_b32 v225, a43\n\t"
      "v_mul_f32_e32 v225, v225, v228\n\t"
      "v_accvgpr_write_b32 a43, v225\n\t"
      "v_accvgpr_read_b32 v222, a56\n\t"
      "v_mul_f32_e32 v222, v222, v228\n\t"
      "v_accvgpr_write_b32 a56, v222\n\t"
      "v_accvgpr_read_b32 v223, a57\n\t"
      "v_mul_f32_e32 v223, v223, v228\n\t"
      "v_accvgpr_write_b32 a57, v223\n\t"
      "v_accvgpr_read_b32 v224, a58\n\t"
      "v_mul_f32_e32 v224, v224, v228\n\t"
      "v_accvgpr_write_b32 a58, v224\n\t"
      "v_accvgpr_read_b32 v225, a59\n\t"
      "v_mul_f32_e32 v225, v225, v228\n\t"
      "v_accvgpr_write_b32 a59, v225\n\t"
      "v_accvgpr_read_b32 v222, a72\n\t"
      "v_mul_f32_e32 v222, v222, v228\n\t"
      "v_accvgpr_write_b32 a72, v222\n\t"
      "v_accvgpr_read_b32 v223, a73\n\t"
      "v_mul_f32_e32 v223, v223, v228\n\t"
      "v_accvgpr_write_b32 a73, v223\n\t"
      "v_accvgpr_read_b32 v224, a74\n\t"
      "v_mul_f32_e32 v224, v224, v228\n\t"
      "v_accvgpr_write_b32 a74, v224\n\t"
      "v_accvgpr_read_b32 v225, a75\n\t"
      "v_mul_f32_e32 v225, v225, v228\n\t"
      "v_accvgpr_write_b32 a75, v225\n\t"
      "v_accvgpr_read_b32 v222, a88\n\t"
      "v_mul_f32_e32 v222, v222, v228\n\t"
      "v_accvgpr_write_b32 a88, v222\n\t"
      "v_accvgpr_read_b32 v223, a89\n\t"
      "v_mul_f32_e32 v223, v223, v228\n\t"
      "v_accvgpr_write_b32 a89, v223\n\t"
      "v_accvgpr_read_b32 v224, a90\n\t"
      "v_mul_f32_e32 v224, v224, v228\n\t"
      "v_accvgpr_write_b32 a90, v224\n\t"
      "v_accvgpr_read_b32 v225, a91\n\t"
      "v_mul_f32_e32 v225, v225, v228\n\t"
      "v_accvgpr_write_b32 a91, v225\n\t"
      "v_accvgpr_read_b32 v222, a104\n\t"
      "v_mul_f32_e32 v222, v222, v228\n\t"
      "v_accvgpr_write_b32 a104, v222\n\t"
      "v_accvgpr_read_b32 v223, a105\n\t"
      "v_mul_f32_e32 v223, v223, v228\n\t"
      "v_accvgpr_write_b32 a105, v223\n\t"
      "v_accvgpr_read_b32 v224, a106\n\t"
      "v_mul_f32_e32 v224, v224, v228\n\t"
      "v_accvgpr_write_b32 a106, v224\n\t"
      "v_accvgpr_read_b32 v225, a107\n\t"
      "v_mul_f32_e32 v225, v225, v228\n\t"
      "v_accvgpr_write_b32 a107, v225\n\t"
      "v_accvgpr_read_b32 v222, a120\n\t"
      "v_mul_f32_e32 v222, v222, v228\n\t"
      "v_accvgpr_write_b32 a120, v222\n\t"
      "v_accvgpr_read_b32 v223, a121\n\t"
      "v_mul_f32_e32 v223, v223, v228\n\t"
      "v_accvgpr_write_b32 a121, v223\n\t"
      "v_accvgpr_read_b32 v224, a122\n\t"
      "v_mul_f32_e32 v224, v224, v228\n\t"
      "v_accvgpr_write_b32 a122, v224\n\t"
      "v_accvgpr_read_b32 v225, a123\n\t"
      "v_mul_f32_e32 v225, v225, v228\n\t"
      "v_accvgpr_write_b32 a123, v225\n\t"
      "v_mul_f32_e32 v188, v188, v229\n\t"
      "v_sub_f32_e32 v140, v140, v221\n\t"
      "v_sub_f32_e32 v141, v141, v221\n\t"
      "v_sub_f32_e32 v142, v142, v221\n\t"
      "v_sub_f32_e32 v143, v143, v221\n\t"
      "v_sub_f32_e32 v120, v120, v221\n\t"
      "v_sub_f32_e32 v88, v88, v221\n\t"
      "v_sub_f32_e32 v121, v121, v221\n\t"
      "v_sub_f32_e32 v89, v89, v221\n\t"
      "v_sub_f32_e32 v122, v122, v221\n\t"
      "v_sub_f32_e32 v90, v90, v221\n\t"
      "v_sub_f32_e32 v123, v123, v221\n\t"
      "v_sub_f32_e32 v91, v91, v221\n\t"
      "v_sub_f32_e32 v124, v124, v221\n\t"
      "v_sub_f32_e32 v92, v92, v221\n\t"
      "v_sub_f32_e32 v125, v125, v221\n\t"
      "v_sub_f32_e32 v93, v93, v221\n\t"
      "v_sub_f32_e32 v126, v126, v221\n\t"
      "v_sub_f32_e32 v94, v94, v221\n\t"
      "v_sub_f32_e32 v127, v127, v221\n\t"
      "v_sub_f32_e32 v95, v95, v221\n\t"
      "v_accvgpr_read_b32 v222, a12\n\t"
      "v_mul_f32_e32 v222, v222, v229\n\t"
      "v_accvgpr_write_b32 a12, v222\n\t"
      "v_accvgpr_read_b32 v223, a13\n\t"
      "v_mul_f32_e32 v223, v223, v229\n\t"
      "v_accvgpr_write_b32 a13, v223\n\t"
      "v_accvgpr_read_b32 v224, a14\n\t"
      "v_mul_f32_e32 v224, v224, v229\n\t"
      "v_accvgpr_write_b32 a14, v224\n\t"
      "v_accvgpr_read_b32 v225, a15\n\t"
      "v_mul_f32_e32 v225, v225, v229\n\t"
      "v_accvgpr_write_b32 a15, v225\n\t"
      "v_accvgpr_read_b32 v222, a28\n\t"
      "v_mul_f32_e32 v222, v222, v229\n\t"
      "v_accvgpr_write_b32 a28, v222\n\t"
      "v_accvgpr_read_b32 v223, a29\n\t"
      "v_mul_f32_e32 v223, v223, v229\n\t"
      "v_accvgpr_write_b32 a29, v223\n\t"
      "v_accvgpr_read_b32 v224, a30\n\t"
      "v_mul_f32_e32 v224, v224, v229\n\t"
      "v_accvgpr_write_b32 a30, v224\n\t"
      "v_accvgpr_read_b32 v225, a31\n\t"
      "v_mul_f32_e32 v225, v225, v229\n\t"
      "v_accvgpr_write_b32 a31, v225\n\t"
      "v_accvgpr_read_b32 v222, a44\n\t"
      "v_mul_f32_e32 v222, v222, v229\n\t"
      "v_accvgpr_write_b32 a44, v222\n\t"
      "v_accvgpr_read_b32 v223, a45\n\t"
      "v_mul_f32_e32 v223, v223, v229\n\t"
      "v_accvgpr_write_b32 a45, v223\n\t"
      "v_accvgpr_read_b32 v224, a46\n\t"
      "v_mul_f32_e32 v224, v224, v229\n\t"
      "v_accvgpr_write_b32 a46, v224\n\t"
      "v_accvgpr_read_b32 v225, a47\n\t"
      "v_mul_f32_e32 v225, v225, v229\n\t"
      "v_accvgpr_write_b32 a47, v225\n\t"
      "v_accvgpr_read_b32 v222, a60\n\t"
      "v_mul_f32_e32 v222, v222, v229\n\t"
      "v_accvgpr_write_b32 a60, v222\n\t"
      "v_accvgpr_read_b32 v223, a61\n\t"
      "v_mul_f32_e32 v223, v223, v229\n\t"
      "v_accvgpr_write_b32 a61, v223\n\t"
      "v_accvgpr_read_b32 v224, a62\n\t"
      "v_mul_f32_e32 v224, v224, v229\n\t"
      "v_accvgpr_write_b32 a62, v224\n\t"
      "v_accvgpr_read_b32 v225, a63\n\t"
      "v_mul_f32_e32 v225, v225, v229\n\t"
      "v_accvgpr_write_b32 a63, v225\n\t"
      "v_accvgpr_read_b32 v222, a76\n\t"
      "v_mul_f32_e32 v222, v222, v229\n\t"
      "v_accvgpr_write_b32 a76, v222\n\t"
      "v_accvgpr_read_b32 v223, a77\n\t"
      "v_mul_f32_e32 v223, v223, v229\n\t"
      "v_accvgpr_write_b32 a77, v223\n\t"
      "v_accvgpr_read_b32 v224, a78\n\t"
      "v_mul_f32_e32 v224, v224, v229\n\t"
      "v_accvgpr_write_b32 a78, v224\n\t"
      "v_accvgpr_read_b32 v225, a79\n\t"
      "v_mul_f32_e32 v225, v225, v229\n\t"
      "v_accvgpr_write_b32 a79, v225\n\t"
      "v_accvgpr_read_b32 v222, a92\n\t"
      "v_mul_f32_e32 v222, v222, v229\n\t"
      "v_accvgpr_write_b32 a92, v222\n\t"
      "v_accvgpr_read_b32 v223, a93\n\t"
      "v_mul_f32_e32 v223, v223, v229\n\t"
      "v_accvgpr_write_b32 a93, v223\n\t"
      "v_accvgpr_read_b32 v224, a94\n\t"
      "v_mul_f32_e32 v224, v224, v229\n\t"
      "v_accvgpr_write_b32 a94, v224\n\t"
      "v_accvgpr_read_b32 v225, a95\n\t"
      "v_mul_f32_e32 v225, v225, v229\n\t"
      "v_accvgpr_write_b32 a95, v225\n\t"
      "v_accvgpr_read_b32 v222, a108\n\t"
      "v_mul_f32_e32 v222, v222, v229\n\t"
      "v_accvgpr_write_b32 a108, v222\n\t"
      "v_accvgpr_read_b32 v223, a109\n\t"
      "v_mul_f32_e32 v223, v223, v229\n\t"
      "v_accvgpr_write_b32 a109, v223\n\t"
      "v_accvgpr_read_b32 v224, a110\n\t"
      "v_mul_f32_e32 v224, v224, v229\n\t"
      "v_accvgpr_write_b32 a110, v224\n\t"
      "v_accvgpr_read_b32 v225, a111\n\t"
      "v_mul_f32_e32 v225, v225, v229\n\t"
      "v_accvgpr_write_b32 a111, v225\n\t"
      "v_accvgpr_read_b32 v222, a124\n\t"
      "v_mul_f32_e32 v222, v222, v229\n\t"
      "v_accvgpr_write_b32 a124, v222\n\t"
      "v_accvgpr_read_b32 v223, a125\n\t"
      "v_mul_f32_e32 v223, v223, v229\n\t"
      "v_accvgpr_write_b32 a125, v223\n\t"
      "v_accvgpr_read_b32 v224, a126\n\t"
      "v_mul_f32_e32 v224, v224, v229\n\t"
      "v_accvgpr_write_b32 a126, v224\n\t"
      "v_accvgpr_read_b32 v225, a127\n\t"
      "v_mul_f32_e32 v225, v225, v229\n\t"
      "v_accvgpr_write_b32 a127, v225\n\t"
      "v_exp_f32_e32 v208, v96\n\t"
      "v_exp_f32_e32 v209, v97\n\t"
      "s_nop 0\n\t"
      "v_cvt_pk_bf16_f32 v160, v208, v209\n\t"
      "v_exp_f32_e32 v210, v98\n\t"
      "v_exp_f32_e32 v211, v99\n\t"
      "s_nop 0\n\t"
      "v_cvt_pk_bf16_f32 v161, v210, v211\n\t"
      "v_exp_f32_e32 v212, v100\n\t"
      "v_exp_f32_e32 v213, v101\n\t"
      "s_nop 0\n\t"
      "v_cvt_pk_bf16_f32 v162, v212, v213\n\t"
      "v_exp_f32_e32 v214, v102\n\t"
      "v_exp_f32_e32 v215, v103\n\t"
      "s_nop 0\n\t"
      "v_cvt_pk_bf16_f32 v163, v214, v215\n\t"
      "v_exp_f32_e32 v208, v104\n\t"
      "v_exp_f32_e32 v209, v105\n\t"
      "s_nop 0\n\t"
      "v_cvt_pk_bf16_f32 v164, v208, v209\n\t"
      "v_exp_f32_e32 v210, v106\n\t"
      "v_exp_f32_e32 v211, v107\n\t"
      "s_nop 0\n\t"
      "v_cvt_pk_bf16_f32 v165, v210, v211\n\t"
      "v_exp_f32_e32 v212, v108\n\t"
      "v_exp_f32_e32 v213, v109\n\t"
      "s_nop 0\n\t"
      "v_cvt_pk_bf16_f32 v166, v212, v213\n\t"
      "v_exp_f32_e32 v214, v110\n\t"
      "v_exp_f32_e32 v215, v111\n\t"
      "s_nop 0\n\t"
      "v_cvt_pk_bf16_f32 v167, v214, v215\n\t"
      "v_exp_f32_e32 v208, v112\n\t"
      "v_exp_f32_e32 v209, v113\n\t"
      "s_nop 0\n\t"
      "v_cvt_pk_bf16_f32 v168, v208, v209\n\t"
      "v_exp_f32_e32 v210, v114\n\t"
      "v_exp_f32_e32 v211, v115\n\t"
      "s_nop 0\n\t"
      "v_cvt_pk_bf16_f32 v169, v210, v211\n\t"
      "v_exp_f32_e32 v212, v116\n\t"
      "v_exp_f32_e32 v213, v117\n\t"
      "s_nop 0\n\t"
      "v_cvt_pk_bf16_f32 v170, v212, v213\n\t"
      "v_exp_f32_e32 v214, v118\n\t"
      "v_exp_f32_e32 v215, v119\n\t"
      "s_nop 0\n\t"
      "v_cvt_pk_bf16_f32 v171, v214, v215\n\t"
      "v_exp_f32_e32 v208, v120\n\t"
      "v_exp_f32_e32 v209, v121\n\t"
      "s_nop 0\n\t"
      "v_cvt_pk_bf16_f32 v172, v208, v209\n\t"
      "v_exp_f32_e32 v210, v122\n\t"
      "v_exp_f32_e32 v211, v123\n\t"
      "s_nop 0\n\t"
      "v_cvt_pk_bf16_f32 v173, v210, v211\n\t"
      "v_exp_f32_e32 v212, v124\n\t"
      "v_exp_f32_e32 v213, v125\n\t"
      "s_nop 0\n\t"
      "v_cvt_pk_bf16_f32 v174, v212, v213\n\t"
      "v_exp_f32_e32 v214, v126\n\t"
      "v_exp_f32_e32 v215, v127\n\t"
      "s_nop 0\n\t"
      "v_cvt_pk_bf16_f32 v175, v214, v215\n\t"
      "s_nop 1\n\t"
      "v_mfma_f32_16x16x32_bf16 v[192:195], a[192:195], v[160:163], v[176:179]\n\t"
      "v_mfma_f32_16x16x32_bf16 v[196:199], a[192:195], v[164:167], v[180:183]\n\t"
      "v_mfma_f32_16x16x32_bf16 v[200:203], a[192:195], v[168:171], v[184:187]\n\t"
      "v_mfma_f32_16x16x32_bf16 v[204:207], a[192:195], v[172:175], v[188:191]\n\t"
      "v_exp_f32_e32 v208, v64\n\t"
      "v_exp_f32_e32 v209, v65\n\t"
      "s_nop 0\n\t"
      "v_cvt_pk_bf16_f32 v144, v208, v209\n\t"
      "v_exp_f32_e32 v210, v66\n\t"
      "v_exp_f32_e32 v211, v67\n\t"
      "s_nop 0\n\t"
      "v_cvt_pk_bf16_f32 v145, v210, v211\n\t"
      "v_exp_f32_e32 v212, v68\n\t"
      "v_exp_f32_e32 v213, v69\n\t"
      "s_nop 0\n\t"
      "v_cvt_pk_bf16_f32 v146, v212, v213\n\t"
      "v_exp_f32_e32 v214, v70\n\t"
      "s_nop 7\n\t"
      "s_cmp_eq_u32 s64, 0\n\t"
      "s_cbranch_scc1 L_BACK0\n\t"
      "s_cmp_eq_u32 s64, 2\n\t"
      "s_cbranch_scc1 L_BACK2\n\t"
      "s_cmp_eq_u32 s64, 4\n\t"
      "s_cbranch_scc1 L_BACK4\n\t"
      "s_cmp_eq_u32 s64, 6\n\t"
      "s_cbranch_scc1 L_BACK6\n\t"
      "s_cmp_eq_u32 s64, 8\n\t"
      "s_cbranch_scc1 L_BACK8\n\t"
      "s_endpgm\n\t"
      ".p2align 6\n\t"
      "L_H0:\n\t"
      "v_mfma_f32_16x16x32_bf16 v[64:67], a[128:131], v[0:3], v[128:131]\n\t"
      "v_exp_f32_e64 v215, v119\n\t"
      "v_mfma_f32_16x16x32_bf16 v[64:67], a[132:135], v[8:11], v[64:67]\n\t"
      "ds_read_b64_tr_b16 a[186:187], v234 offset:62976\n\t"
      "v_cvt_pk_bf16_f32 v171, v214, v215\n\t"
      "v_mfma_f32_16x16x32_bf16 v[64:67], a[136:139], v[16:19], v[64:67]\n\t"
      "ds_read_b64_tr_b16 a[188:189], v234 offset:59136\n\t"
      "v_mfma_f32_16x16x32_bf16 v[64:67], a[140:143], v[24:27], v[64:67]\n\t"
      "v_exp_f32_e64 v208, v120\n\t"
      "v_mfma_f32_16x16x32_bf16 v[192:195], a[192:195], v[160:163], v[176:179]\n\t"
      "ds_read_b64_tr_b16 a[190:191], v234 offset:63232\n\t"
      "v_mfma_f32_16x16x32_bf16 v[68:71], a[144:147], v[0:3], v[128:131]\n\t"
      "s_add_u32 m0, s59, 49152\n\t"
      "v_exp_f32_e64 v209, v121\n\t"
      "v_mfma_f32_16x16x32_bf16 v[68:71], a[148:151], v[8:11], v[68:71]\n\t"
      "v_cvt_pk_bf16_f32 v172, v208, v209\n\t"
      "v_mfma_f32_16x16x32_bf16 v[68:71], a[152:155], v[16:19], v[68:71]\n\t"
      "v_exp_f32_e64 v210, v122\n\t"
      "v_mfma_f32_16x16x32_bf16 v[68:71], a[156:159], v[24:27], v[68:71]\n\t"
      "buffer_load_dwordx4 v235, s[76:79], s61 offen lds\n\t"
      "v_mfma_f32_16x16x32_bf16 v[72:75], a[128:131], v[4:7], v[132:135]\n\t"
      "v_exp_f32_e64 v211, v123\n\t"
      "v_mfma_f32_16x16x32_bf16 v[196:199], a[192:195], v[164:167], v[180:183]\n\t"
      "v_cvt_pk_bf16_f32 v173, v210, v211\n\t"
      "v_mfma_f32_16x16x32_bf16 v[72:75], a[132:135], v[12:15], v[72:75]\n\t"
      "v_exp_f32_e64 v212, v124\n\t"
      "v_mfma_f32_16x16x32_bf16 v[72:75], a[136:139], v[20:23], v[72:75]\n\t"
      "buffer_load_dwordx4 v236, s[76:79], s61 offen offset:1024 lds\n\t"
      "v_mfma_f32_16x16x32_bf16 v[72:75], a[140:143], v[28:31], v[72:75]\n\t"
      "v_exp_f32_e64 v213, v125\n\t"
      "v_mfma_f32_16x16x32_bf16 v[76:79], a[144:147], v[4:7], v[132:135]\n\t"
      "v_cvt_pk_bf16_f32 v174, v212, v213\n\t"
      "v_mfma_f32_16x16x32_bf16 v[76:79], a[148:151], v[12:15], v[76:79]\n\t"
      "v_exp_f32_e64 v214, v126\n\t"
      "v_mfma_f32_16x16x32_bf16 v[200:203], a[192:195], v[168:171], v[184:187]\n\t"
      "v_mfma_f32_16x16x32_bf16 v[76:79], a[152:155], v[20:23], v[76:79]\n\t"
      "v_exp_f32_e64 v215, v127\n\t"
      "v_mfma_f32_16x16x32_bf16 v[76:79], a[156:159], v[28:31], v[76:79]\n\t"
      "v_cvt_pk_bf16_f32 v175, v214, v215\n\t"
      "v_mfma_f32_16x16x32_bf16 v[80:83], a[128:131], v[32:35], v[136:139]\n\t"
      "buffer_load_dwordx4 v237, s[76:79], s61 offen offset:2048 lds\n\t"
      "v_mfma_f32_16x16x32_bf16 v[80:83], a[132:135], v[40:43], v[80:83]\n\t"
      "v_exp_f32_e64 v208, v64\n\t"
      "v_mfma_f32_16x16x32_bf16 v[80:83], a[136:139], v[48:51], v[80:83]\n\t"
      "v_mfma_f32_16x16x32_bf16 v[80:83], a[140:143], v[56:59], v[80:83]\n\t"
      "v_exp_f32_e64 v209, v65\n\t"
      "v_mfma_f32_16x16x32_bf16 v[204:207], a[192:195], v[172:175], v[188:191]\n\t"
      "v_cvt_pk_bf16_f32 v144, v208, v209\n\t"
      "v_mfma_f32_16x16x32_bf16 v[84:87], a[144:147], v[32:35], v[136:139]\n\t"
      "v_exp_f32_e64 v210, v66\n\t"
      "v_mfma_f32_16x16x32_bf16 v[84:87], a[148:151], v[40:43], v[84:87]\n\t"
      "buffer_load_dwordx4 v238, s[76:79], s61 offen offset:3072 lds\n\t"
      "v_mfma_f32_16x16x32_bf16 v[84:87], a[152:155], v[48:51], v[84:87]\n\t"
      "s_add_u32 s61, s61, 0x4000\n\t"
      "v_exp_f32_e64 v211, v67\n\t"
      "v_mfma_f32_16x16x32_bf16 v[84:87], a[156:159], v[56:59], v[84:87]\n\t"
      "v_max3_f32 v216, v192, v196, v200\n\t"
      "v_cvt_pk_bf16_f32 v145, v210, v211\n\t"
      "v_mfma_f32_16x16x32_bf16 v[88:91], a[128:131], v[36:39], v[140:143]\n\t"
      "v_exp_f32_e64 v212, v68\n\t"
      "v_mfma_f32_16x16x32_bf16 v[88:91], a[132:135], v[44:47], v[88:91]\n\t"
      "ds_read_b128 a[128:131], v230 offset:8192\n\t"
      "v_max_f32_e64 v216, v216, v204\n\t"
      "v_mfma_f32_16x16x32_bf16 v[88:91], a[136:139], v[52:55], v[88:91]\n\t"
      "v_exp_f32_e64 v213, v69\n\t"
      "v_mfma_f32_16x16x32_bf16 v[88:91], a[140:143], v[60:63], v[88:91]\n\t"
      "v_cmp_nge_f32_e64 vcc, s65, v216\n\t"
      "v_cvt_pk_bf16_f32 v146, v212, v213\n\t"
      "v_mfma_f32_16x16x32_bf16 v[92:95], a[144:147], v[36:39], v[140:143]\n\t"
      "s_waitcnt lgkmcnt(1)\n\t"
      "v_exp_f32_e32 v214, v70\n\t"
      "v_mfma_f32_16x16x32_bf16 v[92:95], a[148:151], v[44:47], v[92:95]\n\t"
      "ds_read_b128 a[132:135], v231 offset:8192\n\t"
      "v_mfma_f32_16x16x32_bf16 v[92:95], a[152:155], v[52:55], v[92:95]\n\t"
      "s_cbranch_vccnz L_RARE0\n\t"
      "L_BACK0:\n\t"
      "v_exp_f32_e32 v215, v71\n\t"
      "v_mfma_f32_16x16x32_bf16 v[92:95], a[156:159], v[60:63], v[92:95]\n\t"
      "ds_read_b128 a[136:139], v232 offset:8192\n\t"
      "v_cvt_pk_bf16_f32 v147, v214, v215\n\t"
      "v_mfma_f32_16x16x32_bf16 a[0:3], a[160:163], v[160:163], a[0:3]\n\t"
      "ds_read_b128 a[140:143], v233 offset:8192\n\t"
      "v_mfma_f32_16x16x32_bf16 a[4:7], a[160:163], v[164:167], a[4:7]\n\t"
      "v_exp_f32_e64 v208, v72\n\t"
      "v_mfma_f32_16x16x32_bf16 a[8:11], a[160:163], v[168:171], a[8:11]\n\t"
      "ds_read_b128 a[144:147], v230 offset:12288\n\t"
      "v_mfma_f32_16x16x32_bf16 a[12:15], a[160:163], v[172:175], a[12:15]\n\t"
      "v_exp_f32_e64 v209, v73\n\t"
      "v_mfma_f32_16x16x32_bf16 a[16:19], a[164:167], v[160:163], a[16:19]\n\t"
      "ds_read_b128 a[148:151], v231 offset:12288\n\t"
      "v_cvt_pk_bf16_f32 v148, v208, v209\n\t"
      "v_mfma_f32_16x16x32_bf16 a[20:23], a[164:167], v[164:167], a[20:23]\n\t"
      "v_exp_f32_e64 v210, v74\n\t"
      "v_mfma_f32_16x16x32_bf16 a[24:27], a[164:167], v[168:171], a[24:27]\n\t"
      "ds_read_b128 a[152:155], v232 offset:12288\n\t"
      "ds_read_b64_tr_b16 a[160:161], v234 offset:0\n\t"
      "v_mfma_f32_16x16x32_bf16 a[28:31], a[164:167], v[172:175], a[28:31]\n\t"
      "v_exp_f32_e64 v211, v75\n\t"
      "v_mfma_f32_16x16x32_bf16 a[32:35], a[168:171], v[160:163], a[32:35]\n\t"
      "ds_read_b128 a[156:159], v233 offset:12288\n\t"
      "v_cvt_pk_bf16_f32 v149, v210, v211\n\t"
      "v_mfma_f32_16x16x32_bf16 a[36:39], a[168:171], v[164:167], a[36:39]\n\t"
      "v_exp_f32_e64 v212, v76\n\t"
      "v_mfma_f32_16x16x32_bf16 a[40:43], a[168:171], v[168:171], a[40:43]\n\t"
      "ds_read_b64_tr_b16 a[162:163], v234 offset:4096\n\t"
      "v_mfma_f32_16x16x32_bf16 a[44:47], a[168:171], v[172:175], a[44:47]\n\t"
      "v_exp_f32_e64 v213, v77\n\t"
      "v_mfma_f32_16x16x32_bf16 a[48:51], a[172:175], v[160:163], a[48:51]\n\t"
      "ds_read_b64_tr_b16 a[164:165], v234 offset:256\n\t"
      "v_cvt_pk_bf16_f32 v150, v212, v213\n\t"
      "v_mfma_f32_16x16x32_bf16 a[52:55], a[172:175], v[164:167], a[52:55]\n\t"
      "v_exp_f32_e64 v214, v78\n\t"
      "v_mfma_f32_16x16x32_bf16 a[56:59], a[172:175], v[168:171], a[56:59]\n\t"
      "ds_read_b64_tr_b16 a[166:167], v234 offset:4352\n\t"
      "v_mfma_f32_16x16x32_bf16 a[60:63], a[172:175], v[172:175], a[60:63]\n\t"
      "v_exp_f32_e64 v215, v79\n\t"
      "v_mfma_f32_16x16x32_bf16 a[64:67], a[176:179], v[160:163], a[64:67]\n\t"
      "ds_read_b64_tr_b16 a[168:169], v234 offset:512\n\t"
      "v_cvt_pk_bf16_f32 v151, v214, v215\n\t"
      "v_mfma_f32_16x16x32_bf16 a[68:71], a[176:179], v[164:167], a[68:71]\n\t"
      "ds_read_b64_tr_b16 a[170:171], v234 offset:4608\n\t"
      "v_mfma_f32_16x16x32_bf16 a[72:75], a[176:179], v[168:171], a[72:75]\n\t"
      "v_exp_f32_e64 v208, v80\n\t"
      "v_mfma_f32_16x16x32_bf16 a[76:79], a[176:179], v[172:175], a[76:79]\n\t"
      "ds_read_b64_tr_b16 a[172:173], v234 offset:768\n\t"
      "v_mfma_f32_16x16x32_bf16 a[80:83], a[180:183], v[160:163], a[80:83]\n\t"
      "v_exp_f32_e64 v209, v81\n\t"
      "v_mfma_f32_16x16x32_bf16 a[84:87], a[180:183], v[164:167], a[84:87]\n\t"
      "ds_read_b64_tr_b16 a[174:175], v234 offset:4864\n\t"
      "v_cvt_pk_bf16_f32 v152, v208, v209\n\t"
      "v_mfma_f32_16x16x32_bf16 a[88:91], a[180:183], v[168:171], a[88:91]\n\t"
      "v_exp_f32_e64 v210, v82\n\t"
      "v_mfma_f32_16x16x32_bf16 a[92:95], a[180:183], v[172:175], a[92:95]\n\t"
      "ds_read_b64_tr_b16 a[176:177], v234 offset:1024\n\t"
      "v_mfma_f32_16x16x32_bf16 a[96:99], a[184:187], v[160:163], a[96:99]\n\t"
      "v_exp_f32_e64 v211, v83\n\t"
      "v_mfma_f32_16x16x32_bf16 a[100:103], a[184:187], v[164:167], a[100:103]\n\t"
      "ds_read_b64_tr_b16 a[178:179], v234 offset:5120\n\t"
      "v_cvt_pk_bf16_f32 v153, v210, v211\n\t"
      "v_mfma_f32_16x16x32_bf16 a[104:107], a[184:187], v[168:171], a[104:107]\n\t"
      "v_exp_f32_e64 v212, v84\n\t"
      "v_mfma_f32_16x16x32_bf16 a[108:111], a[184:187], v[172:175], a[108:111]\n\t"
      "ds_read_b64_tr_b16 a[180:181], v234 offset:1280\n\t"
      "v_mfma_f32_16x16x32_bf16 a[112:115], a[188:191], v[160:163], a[112:115]\n\t"
      "v_exp_f32_e64 v213, v85\n\t"
      "v_mfma_f32_16x16x32_bf16 a[116:119], a[188:191], v[164:167], a[116:119]\n\t"
      "ds_read_b64_tr_b16 a[182:183], v234 offset:5376\n\t"
      "v_cvt_pk_bf16_f32 v154, v212, v213\n\t"
      "v_mfma_f32_16x16x32_bf16 a[120:123], a[188:191], v[168:171], a[120:123]\n\t"
      "s_waitcnt lgkmcnt(11)\n\t"
      "v_exp_f32_e32 v214, v86\n\t"
      "v_mfma_f32_16x16x32_bf16 a[124:127], a[188:191], v[172:175], a[124:127]\n\t"
      "ds_read_b64_tr_b16 a[184:185], v234 offset:1536\n\t"
      "L_H1:\n\t"
      "v_mfma_f32_16x16x32_bf16 v[96:99], a[128:131], v[0:3], v[128:131]\n\t"
      "s_waitcnt vmcnt(12)\n\t"
      "v_exp_f32_e32 v215, v87\n\t"
      "v_mfma_f32_16x16x32_bf16 v[96:99], a[132:135], v[8:11], v[96:99]\n\t"
      "ds_read_b64_tr_b16 a[186:187], v234 offset:5632\n\t"
      "v_cvt_pk_bf16_f32 v155, v214, v215\n\t"
      "v_mfma_f32_16x16x32_bf16 v[96:99], a[136:139], v[16:19], v[96:99]\n\t"
      "ds_read_b64_tr_b16 a[188:189], v234 offset:1792\n\t"
      "v_mfma_f32_16x16x32_bf16 v[96:99], a[140:143], v[24:27], v[96:99]\n\t"
      "s_barrier\n\t"
      "v_exp_f32_e32 v208, v88\n\t"
      "v_mfma_f32_16x16x32_bf16 v[176:179], a[192:195], v[144:147], v[192:195]\n\t"
      "ds_read_b64_tr_b16 a[190:191], v234 offset:5888\n\t"
      "v_mfma_f32_16x16x32_bf16 v[100:103], a[144:147], v[0:3], v[128:131]\n\t"
      "s_add_u32 m0, s60, 49152\n\t"
      "v_exp_f32_e64 v209, v89\n\t"
      "v_mfma_f32_16x16x32_bf16 v[100:103], a[148:151], v[8:11], v[100:103]\n\t"
      "v_cvt_pk_bf16_f32 v156, v208, v209\n\t"
      "v_mfma_f32_16x16x32_bf16 v[100:103], a[152:155], v[16:19], v[100:103]\n\t"
      "v_exp_f32_e64 v210, v90\n\t"
      "v_mfma_f32_16x16x32_bf16 v[100:103], a[156:159], v[24:27], v[100:103]\n\t"
      "buffer_load_dwordx4 v239, s[36:39], s62 offen lds\n\t"
      "v_mfma_f32_16x16x32_bf16 v[104:107], a[128:131], v[4:7], v[132:135]\n\t"
      "v_exp_f32_e64 v211, v91\n\t"
      "v_mfma_f32_16x16x32_bf16 v[180:183], a[192:195], v[148:151], v[196:199]\n\t"
      "v_cvt_pk_bf16_f32 v157, v210, v211\n\t"
      "v_mfma_f32_16x16x32_bf16 v[104:107], a[132:135], v[12:15], v[104:107]\n\t"
      "v_exp_f32_e64 v212, v92\n\t"
      "v_mfma_f32_16x16x32_bf16 v[104:107], a[136:139], v[20:23], v[104:107]\n\t"
      "buffer_load_dwordx4 v240, s[36:39], s62 offen offset:1024 lds\n\t"
      "v_mfma_f32_16x16x32_bf16 v[104:107], a[140:143], v[28:31], v[104:107]\n\t"
      "v_exp_f32_e64 v213, v93\n\t"
      "v_mfma_f32_16x16x32_bf16 v[108:111], a[144:147], v[4:7], v[132:135]\n\t"
      "v_cvt_pk_bf16_f32 v158, v212, v213\n\t"
      "v_mfma_f32_16x16x32_bf16 v[108:111], a[148:151], v[12:15], v[108:111]\n\t"
      "v_exp_f32_e64 v214, v94\n\t"
      "v_mfma_f32_16x16x32_bf16 v[184:187], a[192:195], v[152:155], v[200:203]\n\t"
      "v_mfma_f32_16x16x32_bf16 v[108:111], a[152:155], v[20:23], v[108:111]\n\t"
      "v_exp_f32_e64 v215, v95\n\t"
      "v_mfma_f32_16x16x32_bf16 v[108:111], a[156:159], v[28:31], v[108:111]\n\t"
      "v_cvt_pk_bf16_f32 v159, v214, v215\n\t"
      "v_mfma_f32_16x16x32_bf16 v[112:115], a[128:131], v[32:35], v[136:139]\n\t"
      "buffer_load_dwordx4 v239, s[36:39], s62 offen offset:2048 lds\n\t"
      "v_mfma_f32_16x16x32_bf16 v[112:115], a[132:135], v[40:43], v[112:115]\n\t"
      "v_exp_f32_e64 v208, v96\n\t"
      "v_mfma_f32_16x16x32_bf16 v[112:115], a[136:139], v[48:51], v[112:115]\n\t"
      "v_mfma_f32_16x16x32_bf16 v[112:115], a[140:143], v[56:59], v[112:115]\n\t"
      "v_exp_f32_e64 v209, v97\n\t"
      "v_mfma_f32_16x16x32_bf16 v[188:191], a[192:195], v[156:159], v[204:207]\n\t"
      "v_cvt_pk_bf16_f32 v160, v208, v209\n\t"
      "v_mfma_f32_16x16x32_bf16 v[116:119], a[144:147], v[32:35], v[136:139]\n\t"
      "v_exp_f32_e64 v210, v98\n\t"
      "v_mfma_f32_16x16x32_bf16 v[116:119], a[148:151], v[40:43], v[116:119]\n\t"
      "buffer_load_dwordx4 v240, s[36:39], s62 offen offset:3072 lds\n\t"
      "v_mfma_f32_16x16x32_bf16 v[116:119], a[152:155], v[48:51], v[116:119]\n\t"
      "s_add_u32 s62, s62, 0x4000\n\t"
      "v_exp_f32_e64 v211, v99\n\t"
      "v_mfma_f32_16x16x32_bf16 v[116:119], a[156:159], v[56:59], v[116:119]\n\t"
      "v_max3_f32 v216, v176, v180, v184\n\t"
      "v_cvt_pk_bf16_f32 v161, v210, v211\n\t"
      "v_mfma_f32_16x16x32_bf16 v[120:123], a[128:131], v[36:39], v[140:143]\n\t"
      "v_exp_f32_e64 v212, v100\n\t"
      "v_mfma_f32_16x16x32_bf16 v[120:123], a[132:135], v[44:47], v[120:123]\n\t"
      "ds_read_b128 a[128:131], v230 offset:16384\n\t"
      "v_max_f32_e64 v216, v216, v188\n\t"
      "v_mfma_f32_16x16x32_bf16 v[120:123], a[136:139], v[52:55], v[120:123]\n\t"
      "v_exp_f32_e64 v213, v101\n\t"
      "v_mfma_f32_16x16x32_bf16 v[120:123], a[140:143], v[60:63], v[120:123]\n\t"
      "v_cmp_nge_f32_e64 vcc, s65, v216\n\t"
      "v_cvt_pk_bf16_f32 v162, v212, v213\n\t"
      "v_mfma_f32_16x16x32_bf16 v[124:127], a[144:147], v[36:39], v[140:143]\n\t"
      "s_waitcnt lgkmcnt(1)\n\t"
      "v_exp_f32_e32 v214, v102\n\t"
      "v_mfma_f32_16x16x32_bf16 v[124:127], a[148:151], v[44:47], v[124:127]\n\t"
      "ds_read_b128 a[132:135], v231 offset:16384\n\t"
      "v_mfma_f32_16x16x32_bf16 v[124:127], a[152:155], v[52:55], v[124:127]\n\t"
      "s_cbranch_vccnz L_RARE1\n\t"
      "L_BACK1:\n\t"
      "v_exp_f32_e32 v215, v103\n\t"
      "v_mfma_f32_16x16x32_bf16 v[124:127], a[156:159], v[60:63], v[124:127]\n\t"
      "ds_read_b128 a[136:139], v232 offset:16384\n\t"
      "v_cvt_pk_bf16_f32 v163, v214, v215\n\t"
      "v_mfma_f32_16x16x32_bf16 a[0:3], a[160:163], v[144:147], a[0:3]\n\t"
      "ds_read_b128 a[140:143], v233 offset:16384\n\t"
      "v_mfma_f32_16x16x32_bf16 a[4:7], a[160:163], v[148:151], a[4:7]\n\t"
      "v_exp_f32_e64 v208, v104\n\t"
      "v_mfma_f32_16x16x32_bf16 a[8:11], a[160:163], v[152:155], a[8:11]\n\t"
      "ds_read_b128 a[144:147], v230 offset:20480\n\t"
      "v_mfma_f32_16x16x32_bf16 a[12:15], a[160:163], v[156:159], a[12:15]\n\t"
      "v_exp_f32_e64 v209, v105\n\t"
      "v_mfma_f32_16x16x32_bf16 a[16:19], a[164:167], v[144:147], a[16:19]\n\t"
      "ds_read_b128 a[148:151], v231 offset:20480\n\t"
      "v_cvt_pk_bf16_f32 v164, v208, v209\n\t"
      "v_mfma_f32_16x16x32_bf16 a[20:23], a[164:167], v[148:151], a[20:23]\n\t"
      "v_exp_f32_e64 v210, v106\n\t"
      "v_mfma_f32_16x16x32_bf16 a[24:27], a[164:167], v[152:155], a[24:27]\n\t"
      "ds_read_b128 a[152:155], v232 offset:20480\n\t"
      "ds_read_b64_tr_b16 a[160:161], v234 offset:8192\n\t"
      "v_mfma_f32_16x16x32_bf16 a[28:31], a[164:167], v[156:159], a[28:31]\n\t"
      "v_exp_f32_e64 v211, v107\n\t"
      "v_mfma_f32_16x16x32_bf16 a[32:35], a[168:171], v[144:147], a[32:35]\n\t"
      "ds_read_b128 a[156:159], v233 offset:20480\n\t"
      "v_cvt_pk_bf16_f32 v165, v210, v211\n\t"
      "v_mfma_f32_16x16x32_bf16 a[36:39], a[168:171], v[148:151], a[36:39]\n\t"
      "v_exp_f32_e64 v212, v108\n\t"
      "v_mfma_f32_16x16x32_bf16 a[40:43], a[168:171], v[152:155], a[40:43]\n\t"
      "ds_read_b64_tr_b16 a[162:163], v234 offset:12288\n\t"
      "v_mfma_f32_16x16x32_bf16 a[44:47], a[168:171], v[156:159], a[44:47]\n\t"
      "v_exp_f32_e64 v213, v109\n\t"
      "v_mfma_f32_16x16x32_bf16 a[48:51], a[172:175], v[144:147], a[48:51]\n\t"
      "ds_read_b64_tr_b16 a[164:165], v234 offset:8448\n\t"
      "v_cvt_pk_bf16_f32 v166, v212, v213\n\t"
      "v_mfma_f32_16x16x32_bf16 a[52:55], a[172:175], v[148:151], a[52:55]\n\t"
      "v_exp_f32_e64 v214, v110\n\t"
      "v_mfma_f32_16x16x32_bf16 a[56:59], a[172:175], v[152:155], a[56:59]\n\t"
      "ds_read_b64_tr_b16 a[166:167], v234 offset:12544\n\t"
      "v_mfma_f32_16x16x32_bf16 a[60:63], a[172:175], v[156:159], a[60:63]\n\t"
      "v_exp_f32_e64 v215, v111\n\t"
      "v_mfma_f32_16x16x32_bf16 a[64:67], a[176:179], v[144:147], a[64:67]\n\t"
      "ds_read_b64_tr_b16 a[168:169], v234 offset:8704\n\t"
      "v_cvt_pk_bf16_f32 v167, v214, v215\n\t"
      "v_mfma_f32_16x16x32_bf16 a[68:71], a[176:179], v[148:151], a[68:71]\n\t"
      "ds_read_b64_tr_b16 a[170:171], v234 offset:12800\n\t"
      "v_mfma_f32_16x16x32_bf16 a[72:75], a[176:179], v[152:155], a[72:75]\n\t"
      "v_exp_f32_e64 v208, v112\n\t"
      "v_mfma_f32_16x16x32_bf16 a[76:79], a[176:179], v[156:159], a[76:79]\n\t"
      "ds_read_b64_tr_b16 a[172:173], v234 offset:8960\n\t"
      "v_mfma_f32_16x16x32_bf16 a[80:83], a[180:183], v[144:147], a[80:83]\n\t"
      "v_exp_f32_e64 v209, v113\n\t"
      "v_mfma_f32_16x16x32_bf16 a[84:87], a[180:183], v[148:151], a[84:87]\n\t"
      "ds_read_b64_tr_b16 a[174:175], v234 offset:13056\n\t"
      "v_cvt_pk_bf16_f32 v168, v208, v209\n\t"
      "v_mfma_f32_16x16x32_bf16 a[88:91], a[180:183], v[152:155], a[88:91]\n\t"
      "v_exp_f32_e64 v210, v114\n\t"
      "v_mfma_f32_16x16x32_bf16 a[92:95], a[180:183], v[156:159], a[92:95]\n\t"
      "ds_read_b64_tr_b16 a[176:177], v234 offset:9216\n\t"
      "v_mfma_f32_16x16x32_bf16 a[96:99], a[184:187], v[144:147], a[96:99]\n\t"
      "v_exp_f32_e64 v211, v115\n\t"
      "v_mfma_f32_16x16x32_bf16 a[100:103], a[184:187], v[148:151], a[100:103]\n\t"
      "ds_read_b64_tr_b16 a[178:179], v234 offset:13312\n\t"
      "v_cvt_pk_bf16_f32 v169, v210, v211\n\t"
      "v_mfma_f32_16x16x32_bf16 a[104:107], a[184:187], v[152:155], a[104:107]\n\t"
      "v_exp_f32_e64 v212, v116\n\t"
      "v_mfma_f32_16x16x32_bf16 a[108:111], a[184:187], v[156:159], a[108:111]\n\t"
      "ds_read_b64_tr_b16 a[180:181], v234 offset:9472\n\t"
      "v_mfma_f32_16x16x32_bf16 a[112:115], a[188:191], v[144:147], a[112:115]\n\t"
      "v_exp_f32_e64 v213, v117\n\t"
      "v_mfma_f32_16x16x32_bf16 a[116:119], a[188:191], v[148:151], a[116:119]\n\t"
      "ds_read_b64_tr_b16 a[182:183], v234 offset:13568\n\t"
      "v_cvt_pk_bf16_f32 v170, v212, v213\n\t"
      "v_mfma_f32_16x16x32_bf16 a[120:123], a[188:191], v[152:155], a[120:123]\n\t"
      "s_waitcnt lgkmcnt(11)\n\t"
      "v_exp_f32_e32 v214, v118\n\t"
      "v_mfma_f32_16x16x32_bf16 a[124:127], a[188:191], v[156:159], a[124:127]\n\t"
      "ds_read_b64_tr_b16 a[184:185], v234 offset:9728\n\t"
      "L_H2:\n\t"
      "v_mfma_f32_16x16x32_bf16 v[64:67], a[128:131], v[0:3], v[128:131]\n\t"
      "v_exp_f32_e64 v215, v119\n\t"
      "v_mfma_f32_16x16x32_bf16 v[64:67], a[132:135], v[8:11], v[64:67]\n\t"
      "ds_read_b64_tr_b16 a[186:187], v234 offset:13824\n\t"
      "v_cvt_pk_bf16_f32 v171, v214, v215\n\t"
      "v_mfma_f32_16x16x32_bf16 v[64:67], a[136:139], v[16:19], v[64:67]\n\t"
      "ds_read_b64_tr_b16 a[188:189], v234 offset:9984\n\t"
      "v_mfma_f32_16x16x32_bf16 v[64:67], a[140:143], v[24:27], v[64:67]\n\t"
      "v_exp_f32_e64 v208, v120\n\t"
      "v_mfma_f32_16x16x32_bf16 v[192:195], a[192:195], v[160:163], v[176:179]\n\t"
      "ds_read_b64_tr_b16 a[190:191], v234 offset:14080\n\t"
      "v_mfma_f32_16x16x32_bf16 v[68:71], a[144:147], v[0:3], v[128:131]\n\t"
      "s_add_u32 m0, s59, 0\n\t"
      "v_exp_f32_e32 v209, v121\n\t"
      "v_mfma_f32_16x16x32_bf16 v[68:71], a[148:151], v[8:11], v[68:71]\n\t"
      "v_cvt_pk_bf16_f32 v172, v208, v209\n\t"
      "v_mfma_f32_16x16x32_bf16 v[68:71], a[152:155], v[16:19], v[68:71]\n\t"
      "v_exp_f32_e64 v210, v122\n\t"
      "v_mfma_f32_16x16x32_bf16 v[68:71], a[156:159], v[24:27], v[68:71]\n\t"
      "buffer_load_dwordx4 v235, s[76:79], s61 offen lds\n\t"
      "v_mfma_f32_16x16x32_bf16 v[72:75], a[128:131], v[4:7], v[132:135]\n\t"
      "v_exp_f32_e64 v211, v123\n\t"
      "v_mfma_f32_16x16x32_bf16 v[196:199], a[192:195], v[164:167], v[180:183]\n\t"
      "v_cvt_pk_bf16_f32 v173, v210, v211\n\t"
      "v_mfma_f32_16x16x32_bf16 v[72:75], a[132:135], v[12:15], v[72:75]\n\t"
      "v_exp_f32_e64 v212, v124\n\t"
      "v_mfma_f32_16x16x32_bf16 v[72:75], a[136:139], v[20:23], v[72:75]\n\t"
      "buffer_load_dwordx4 v236, s[76:79], s61 offen offset:1024 lds\n\t"
      "v_mfma_f32_16x16x32_bf16 v[72:75], a[140:143], v[28:31], v[72:75]\n\t"
      "v_exp_f32_e64 v213, v125\n\t"
      "v_mfma_f32_16x16x32_bf16 v[76:79], a[144:147], v[4:7], v[132:135]\n\t"
      "v_cvt_pk_bf16_f32 v174, v212, v213\n\t"
      "v_mfma_f32_16x16x32_bf16 v[76:79], a[148:151], v[12:15], v[76:79]\n\t"
      "v_exp_f32_e64 v214, v126\n\t"
      "v_mfma_f32_16x16x32_bf16 v[200:203], a[192:195], v[168:171], v[184:187]\n\t"
      "v_mfma_f32_16x16x32_bf16 v[76:79], a[152:155], v[20:23], v[76:79]\n\t"
      "v_exp_f32_e64 v215, v127\n\t"
      "v_mfma_f32_16x16x32_bf16 v[76:79], a[156:159], v[28:31], v[76:79]\n\t"
      "v_cvt_pk_bf16_f32 v175, v214, v215\n\t"
      "v_mfma_f32_16x16x32_bf16 v[80:83], a[128:131], v[32:35], v[136:139]\n\t"
      "buffer_load_dwordx4 v237, s[76:79], s61 offen offset:2048 lds\n\t"
      "v_mfma_f32_16x16x32_bf16 v[80:83], a[132:135], v[40:43], v[80:83]\n\t"
      "v_exp_f32_e64 v208, v64\n\t"
      "v_mfma_f32_16x16x32_bf16 v[80:83], a[136:139], v[48:51], v[80:83]\n\t"
      "v_mfma_f32_16x16x32_bf16 v[80:83], a[140:143], v[56:59], v[80:83]\n\t"
      "v_exp_f32_e64 v209, v65\n\t"
      "v_mfma_f32_16x16x32_bf16 v[204:207], a[192:195], v[172:175], v[188:191]\n\t"
      "v_cvt_pk_bf16_f32 v144, v208, v209\n\t"
      "v_mfma_f32_16x16x32_bf16 v[84:87], a[144:147], v[32:35], v[136:139]\n\t"
      "v_exp_f32_e64 v210, v66\n\t"
      "v_mfma_f32_16x16x32_bf16 v[84:87], a[148:151], v[40:43], v[84:87]\n\t"
      "buffer_load_dwordx4 v238, s[76:79], s61 offen offset:3072 lds\n\t"
      "v_mfma_f32_16x16x32_bf16 v[84:87], a[152:155], v[48:51], v[84:87]\n\t"
      "s_add_u32 s61, s61, 0x4000\n\t"
      "v_exp_f32_e64 v211, v67\n\t"
      "v_mfma_f32_16x16x32_bf16 v[84:87], a[156:159], v[56:59], v[84:87]\n\t"
      "v_max3_f32 v216, v192, v196, v200\n\t"
      "v_cvt_pk_bf16_f32 v145, v210, v211\n\t"
      "v_mfma_f32_16x16x32_bf16 v[88:91], a[128:131], v[36:39], v[140:143]\n\t"
      "v_exp_f32_e64 v212, v68\n\t"
      "v_mfma_f32_16x16x32_bf16 v[88:91], a[132:135], v[44:47], v[88:91]\n\t"
      "ds_read_b128 a[128:131], v230 offset:24576\n\t"
      "v_max_f32_e64 v216, v216, v204\n\t"
      "v_mfma_f32_16x16x32_bf16 v[88:91], a[136:139], v[52:55], v[88:91]\n\t"
      "v_exp_f32_e64 v213, v69\n\t"
      "v_mfma_f32_16x16x32_bf16 v[88:91], a[140:143], v[60:63], v[88:91]\n\t"
      "v_cmp_nge_f32_e64 vcc, s65, v216\n\t"
      "v_cvt_pk_bf16_f32 v146, v212, v213\n\t"
      "v_mfma_f32_16x16x32_bf16 v[92:95], a[144:147], v[36:39], v[140:143]\n\t"
      "s_waitcnt lgkmcnt(1)\n\t"
      "v_exp_f32_e32 v214, v70\n\t"
      "v_mfma_f32_16x16x32_bf16 v[92:95], a[148:151], v[44:47], v[92:95]\n\t"
      "ds_read_b128 a[132:135], v231 offset:24576\n\t"
      "v_mfma_f32_16x16x32_bf16 v[92:95], a[152:155], v[52:55], v[92:95]\n\t"
      "s_cbranch_vccnz L_RARE2\n\t"
      "L_BACK2:\n\t"
      "v_exp_f32_e32 v215, v71\n\t"
      "v_mfma_f32_16x16x32_bf16 v[92:95], a[156:159], v[60:63], v[92:95]\n\t"
      "ds_read_b128 a[136:139], v232 offset:24576\n\t"
      "v_cvt_pk_bf16_f32 v147, v214, v215\n\t"
      "v_mfma_f32_16x16x32_bf16 a[0:3], a[160:163], v[160:163], a[0:3]\n\t"
      "ds_read_b128 a[140:143], v233 offset:24576\n\t"
      "v_mfma_f32_16x16x32_bf16 a[4:7], a[160:163], v[164:167], a[4:7]\n\t"
      "v_exp_f32_e64 v208, v72\n\t"
      "v_mfma_f32_16x16x32_bf16 a[8:11], a[160:163], v[168:171], a[8:11]\n\t"
      "ds_read_b128 a[144:147], v230 offset:28672\n\t"
      "v_mfma_f32_16x16x32_bf16 a[12:15], a[160:163], v[172:175], a[12:15]\n\t"
      "v_exp_f32_e64 v209, v73\n\t"
      "v_mfma_f32_16x16x32_bf16 a[16:19], a[164:167], v[160:163], a[16:19]\n\t"
      "ds_read_b128 a[148:151], v231 offset:28672\n\t"
      "v_cvt_pk_bf16_f32 v148, v208, v209\n\t"
      "v_mfma_f32_16x16x32_bf16 a[20:23], a[164:167], v[164:167], a[20:23]\n\t"
      "v_exp_f32_e64 v210, v74\n\t"
      "v_mfma_f32_16x16x32_bf16 a[24:27], a[164:167], v[168:171], a[24:27]\n\t"
      "ds_read_b128 a[152:155], v232 offset:28672\n\t"
      "ds_read_b64_tr_b16 a[160:161], v234 offset:16384\n\t"
      "v_mfma_f32_16x16x32_bf16 a[28:31], a[164:167], v[172:175], a[28:31]\n\t"
      "v_exp_f32_e64 v211, v75\n\t"
      "v_mfma_f32_16x16x32_bf16 a[32:35], a[168:171], v[160:163], a[32:35]\n\t"
      "ds_read_b128 a[156:159], v233 offset:28672\n\t"
      "v_cvt_pk_bf16_f32 v149, v210, v211\n\t"
      "v_mfma_f32_16x16x32_bf16 a[36:39], a[168:171], v[164:167], a[36:39]\n\t"
      "v_exp_f32_e64 v212, v76\n\t"
      "v_mfma_f32_16x16x32_bf16 a[40:43], a[168:171], v[168:171], a[40:43]\n\t"
      "ds_read_b64_tr_b16 a[162:163], v234 offset:20480\n\t"
      "v_mfma_f32_16x16x32_bf16 a[44:47], a[168:171], v[172:175], a[44:47]\n\t"
      "v_exp_f32_e64 v213, v77\n\t"
      "v_mfma_f32_16x16x32_bf16 a[48:51], a[172:175], v[160:163], a[48:51]\n\t"
      "ds_read_b64_tr_b16 a[164:165], v234 offset:16640\n\t"
      "v_cvt_pk_bf16_f32 v150, v212, v213\n\t"
      "v_mfma_f32_16x16x32_bf16 a[52:55], a[172:175], v[164:167], a[52:55]\n\t"
      "v_exp_f32_e64 v214, v78\n\t"
      "v_mfma_f32_16x16x32_bf16 a[56:59], a[172:175], v[168:171], a[56:59]\n\t"
      "ds_read_b64_tr_b16 a[166:167], v234 offset:20736\n\t"
      "v_mfma_f32_16x16x32_bf16 a[60:63], a[172:175], v[172:175], a[60:63]\n\t"
      "v_exp_f32_e64 v215, v79\n\t"
      "v_mfma_f32_16x16x32_bf16 a[64:67], a[176:179], v[160:163], a[64:67]\n\t"
      "ds_read_b64_tr_b16 a[168:169], v234 offset:16896\n\t"
      "v_cvt_pk_bf16_f32 v151, v214, v215\n\t"
      "v_mfma_f32_16x16x32_bf16 a[68:71], a[176:179], v[164:167], a[68:71]\n\t"
      "ds_read_b64_tr_b16 a[170:171], v234 offset:20992\n\t"
      "v_mfma_f32_16x16x32_bf16 a[72:75], a[176:179], v[168:171], a[72:75]\n\t"
      "v_exp_f32_e64 v208, v80\n\t"
      "v_mfma_f32_16x16x32_bf16 a[76:79], a[176:179], v[172:175], a[76:79]\n\t"
      "ds_read_b64_tr_b16 a[172:173], v234 offset:17152\n\t"
      "v_mfma_f32_16x16x32_bf16 a[80:83], a[180:183], v[160:163], a[80:83]\n\t"
      "v_exp_f32_e64 v209, v81\n\t"
      "v_mfma_f32_16x16x32_bf16 a[84:87], a[180:183], v[164:167], a[84:87]\n\t"
      "ds_read_b64_tr_b16 a[174:175], v234 offset:21248\n\t"
      "v_cvt_pk_bf16_f32 v152, v208, v209\n\t"
      "v_mfma_f32_16x16x32_bf16 a[88:91], a[180:183], v[168:171], a[88:91]\n\t"
      "v_exp_f32_e64 v210, v82\n\t"
      "v_mfma_f32_16x16x32_bf16 a[92:95], a[180:183], v[172:175], a[92:95]\n\t"
      "ds_read_b64_tr_b16 a[176:177], v234 offset:17408\n\t"
      "v_mfma_f32_16x16x32_bf16 a[96:99], a[184:187], v[160:163], a[96:99]\n\t"
      "v_exp_f32_e64 v211, v83\n\t"
      "v_mfma_f32_16x16x32_bf16 a[100:103], a[184:187], v[164:167], a[100:103]\n\t"
      "ds_read_b64_tr_b16 a[178:179], v234 offset:21504\n\t"
      "v_cvt_pk_bf16_f32 v153, v210, v211\n\t"
      "v_mfma_f32_16x16x32_bf16 a[104:107], a[184:187], v[168:171], a[104:107]\n\t"
      "v_exp_f32_e64 v212, v84\n\t"
      "v_mfma_f32_16x16x32_bf16 a[108:111], a[184:187], v[172:175], a[108:111]\n\t"
      "ds_read_b64_tr_b16 a[180:181], v234 offset:17664\n\t"
      "v_mfma_f32_16x16x32_bf16 a[112:115], a[188:191], v[160:163], a[112:115]\n\t"
      "v_exp_f32_e64 v213, v85\n\t"
      "v_mfma_f32_16x16x32_bf16 a[116:119], a[188:191], v[164:167], a[116:119]\n\t"
      "ds_read_b64_tr_b16 a[182:183], v234 offset:21760\n\t"
      "v_cvt_pk_bf16_f32 v154, v212, v213\n\t"
      "v_mfma_f32_16x16x32_bf16 a[120:123], a[188:191], v[168:171], a[120:123]\n\t"
      "s_waitcnt lgkmcnt(11)\n\t"
      "v_exp_f32_e32 v214, v86\n\t"
      "v_mfma_f32_16x16x32_bf16 a[124:127], a[188:191], v[172:175], a[124:127]\n\t"
      "ds_read_b64_tr_b16 a[184:185], v234 offset:17920\n\t"
      "L_H3:\n\t"
      "v_mfma_f32_16x16x32_bf16 v[96:99], a[128:131], v[0:3], v[128:131]\n\t"
      "s_waitcnt vmcnt(12)\n\t"
      "v_exp_f32_e32 v215, v87\n\t"
      "v_mfma_f32_16x16x32_bf16 v[96:99], a[132:135], v[8:11], v[96:99]\n\t"
      "ds_read_b64_tr_b16 a[186:187], v234 offset:22016\n\t"
      "v_cvt_pk_bf16_f32 v155, v214, v215\n\t"
      "v_mfma_f32_16x16x32_bf16 v[96:99], a[136:139], v[16:19], v[96:99]\n\t"
      "ds_read_b64_tr_b16 a[188:189], v234 offset:18176\n\t"
      "v_mfma_f32_16x16x32_bf16 v[96:99], a[140:143], v[24:27], v[96:99]\n\t"
      "s_barrier\n\t"
      "v_exp_f32_e32 v208, v88\n\t"
      "v_mfma_f32_16x16x32_bf16 v[176:179], a[192:195], v[144:147], v[192:195]\n\t"
      "ds_read_b64_tr_b16 a[190:191], v234 offset:22272\n\t"
      "v_mfma_f32_16x16x32_bf16 v[100:103], a[144:147], v[0:3], v[128:131]\n\t"
      "s_add_u32 m0, s60, 0\n\t"
      "v_exp_f32_e32 v209, v89\n\t"
      "v_mfma_f32_16x16x32_bf16 v[100:103], a[148:151], v[8:11], v[100:103]\n\t"
      "v_cvt_pk_bf16_f32 v156, v208, v209\n\t"
      "v_mfma_f32_16x16x32_bf16 v[100:103], a[152:155], v[16:19], v[100:103]\n\t"
      "v_exp_f32_e64 v210, v90\n\t"
      "v_mfma_f32_16x16x32_bf16 v[100:103], a[156:159], v[24:27], v[100:103]\n\t"
      "buffer_load_dwordx4 v239, s[36:39], s62 offen lds\n\t"
      "v_mfma_f32_16x16x32_bf16 v[104:107], a[128:131], v[4:7], v[132:135]\n\t"
      "v_exp_f32_e64 v211, v91\n\t"
      "v_mfma_f32_16x16x32_bf16 v[180:183], a[192:195], v[148:151], v[196:199]\n\t"
      "v_cvt_pk_bf16_f32 v157, v210, v211\n\t"
      "v_mfma_f32_16x16x32_bf16 v[104:107], a[132:135], v[12:15], v[104:107]\n\t"
      "v_exp_f32_e64 v212, v92\n\t"
      "v_mfma_f32_16x16x32_bf16 v[104:107], a[136:139], v[20:23], v[104:107]\n\t"
      "buffer_load_dwordx4 v240, s[36:39], s62 offen offset:1024 lds\n\t"
      "v_mfma_f32_16x16x32_bf16 v[104:107], a[140:143], v[28:31], v[104:107]\n\t"
      "v_exp_f32_e64 v213, v93\n\t"
      "v_mfma_f32_16x16x32_bf16 v[108:111], a[144:147], v[4:7], v[132:135]\n\t"
      "v_cvt_pk_bf16_f32 v158, v212, v213\n\t"
      "v_mfma_f32_16x16x32_bf16 v[108:111], a[148:151], v[12:15], v[108:111]\n\t"
      "v_exp_f32_e64 v214, v94\n\t"
      "v_mfma_f32_16x16x32_bf16 v[184:187], a[192:195], v[152:155], v[200:203]\n\t"
      "v_mfma_f32_16x16x32_bf16 v[108:111], a[152:155], v[20:23], v[108:111]\n\t"
      "v_exp_f32_e64 v215, v95\n\t"
      "v_mfma_f32_16x16x32_bf16 v[108:111], a[156:159], v[28:31], v[108:111]\n\t"
      "v_cvt_pk_bf16_f32 v159, v214, v215\n\t"
      "v_mfma_f32_16x16x32_bf16 v[112:115], a[128:131], v[32:35], v[136:139]\n\t"
      "buffer_load_dwordx4 v239, s[36:39], s62 offen offset:2048 lds\n\t"
      "v_mfma_f32_16x16x32_bf16 v[112:115], a[132:135], v[40:43], v[112:115]\n\t"
      "v_exp_f32_e64 v208, v96\n\t"
      "v_mfma_f32_16x16x32_bf16 v[112:115], a[136:139], v[48:51], v[112:115]\n\t"
      "v_mfma_f32_16x16x32_bf16 v[112:115], a[140:143], v[56:59], v[112:115]\n\t"
      "v_exp_f32_e64 v209, v97\n\t"
      "v_mfma_f32_16x16x32_bf16 v[188:191], a[192:195], v[156:159], v[204:207]\n\t"
      "v_cvt_pk_bf16_f32 v160, v208, v209\n\t"
      "v_mfma_f32_16x16x32_bf16 v[116:119], a[144:147], v[32:35], v[136:139]\n\t"
      "v_exp_f32_e64 v210, v98\n\t"
      "v_mfma_f32_16x16x32_bf16 v[116:119], a[148:151], v[40:43], v[116:119]\n\t"
      "buffer_load_dwordx4 v240, s[36:39], s62 offen offset:3072 lds\n\t"
      "v_mfma_f32_16x16x32_bf16 v[116:119], a[152:155], v[48:51], v[116:119]\n\t"
      "s_add_u32 s62, s62, 0x4000\n\t"
      "v_exp_f32_e64 v211, v99\n\t"
      "v_mfma_f32_16x16x32_bf16 v[116:119], a[156:159], v[56:59], v[116:119]\n\t"
      "v_max3_f32 v216, v176, v180, v184\n\t"
      "v_cvt_pk_bf16_f32 v161, v210, v211\n\t"
      "v_mfma_f32_16x16x32_bf16 v[120:123], a[128:131], v[36:39], v[140:143]\n\t"
      "v_exp_f32_e64 v212, v100\n\t"
      "v_mfma_f32_16x16x32_bf16 v[120:123], a[132:135], v[44:47], v[120:123]\n\t"
      "ds_read_b128 a[128:131], v230 offset:32768\n\t"
      "v_max_f32_e64 v216, v216, v188\n\t"
      "v_mfma_f32_16x16x32_bf16 v[120:123], a[136:139], v[52:55], v[120:123]\n\t"
      "v_exp_f32_e64 v213, v101\n\t"
      "v_mfma_f32_16x16x32_bf16 v[120:123], a[140:143], v[60:63], v[120:123]\n\t"
      "v_cmp_nge_f32_e64 vcc, s65, v216\n\t"
      "v_cvt_pk_bf16_f32 v162, v212, v213\n\t"
      "v_mfma_f32_16x16x32_bf16 v[124:127], a[144:147], v[36:39], v[140:143]\n\t"
      "s_waitcnt lgkmcnt(1)\n\t"
      "v_exp_f32_e32 v214, v102\n\t"
      "v_mfma_f32_16x16x32_bf16 v[124:127], a[148:151], v[44:47], v[124:127]\n\t"
      "ds_read_b128 a[132:135], v231 offset:32768\n\t"
      "v_mfma_f32_16x16x32_bf16 v[124:127], a[152:155], v[52:55], v[124:127]\n\t"
      "s_cbranch_vccnz L_RARE3\n\t"
      "L_BACK3:\n\t"
      "v_exp_f32_e32 v215, v103\n\t"
      "v_mfma_f32_16x16x32_bf16 v[124:127], a[156:159], v[60:63], v[124:127]\n\t"
      "ds_read_b128 a[136:139], v232 offset:32768\n\t"
      "v_cvt_pk_bf16_f32 v163, v214, v215\n\t"
      "v_mfma_f32_16x16x32_bf16 a[0:3], a[160:163], v[144:147], a[0:3]\n\t"
      "ds_read_b128 a[140:143], v233 offset:32768\n\t"
      "v_mfma_f32_16x16x32_bf16 a[4:7], a[160:163], v[148:151], a[4:7]\n\t"
      "v_exp_f32_e64 v208, v104\n\t"
      "v_mfma_f32_16x16x32_bf16 a[8:11], a[160:163], v[152:155], a[8:11]\n\t"
      "ds_read_b128 a[144:147], v230 offset:36864\n\t"
      "v_mfma_f32_16x16x32_bf16 a[12:15], a[160:163], v[156:159], a[12:15]\n\t"
      "v_exp_f32_e64 v209, v105\n\t"
      "v_mfma_f32_16x16x32_bf16 a[16:19], a[164:167], v[144:147], a[16:19]\n\t"
      "ds_read_b128 a[148:151], v231 offset:36864\n\t"
      "v_cvt_pk_bf16_f32 v164, v208, v209\n\t"
      "v_mfma_f32_16x16x32_bf16 a[20:23], a[164:167], v[148:151], a[20:23]\n\t"
      "v_exp_f32_e64 v210, v106\n\t"
      "v_mfma_f32_16x16x32_bf16 a[24:27], a[164:167], v[152:155], a[24:27]\n\t"
      "ds_read_b128 a[152:155], v232 offset:36864\n\t"
      "ds_read_b64_tr_b16 a[160:161], v234 offset:24576\n\t"
      "v_mfma_f32_16x16x32_bf16 a[28:31], a[164:167], v[156:159], a[28:31]\n\t"
      "v_exp_f32_e64 v211, v107\n\t"
      "v_mfma_f32_16x16x32_bf16 a[32:35], a[168:171], v[144:147], a[32:35]\n\t"
      "ds_read_b128 a[156:159], v233 offset:36864\n\t"
      "v_cvt_pk_bf16_f32 v165, v210, v211\n\t"
      "v_mfma_f32_16x16x32_bf16 a[36:39], a[168:171], v[148:151], a[36:39]\n\t"
      "v_exp_f32_e64 v212, v108\n\t"
      "v_mfma_f32_16x16x32_bf16 a[40:43], a[168:171], v[152:155], a[40:43]\n\t"
      "ds_read_b64_tr_b16 a[162:163], v234 offset:28672\n\t"
      "v_mfma_f32_16x16x32_bf16 a[44:47], a[168:171], v[156:159], a[44:47]\n\t"
      "v_exp_f32_e64 v213, v109\n\t"
      "v_mfma_f32_16x16x32_bf16 a[48:51], a[172:175], v[144:147], a[48:51]\n\t"
      "ds_read_b64_tr_b16 a[164:165], v234 offset:24832\n\t"
      "v_cvt_pk_bf16_f32 v166, v212, v213\n\t"
      "v_mfma_f32_16x16x32_bf16 a[52:55], a[172:175], v[148:151], a[52:55]\n\t"
      "v_exp_f32_e64 v214, v110\n\t"
      "v_mfma_f32_16x16x32_bf16 a[56:59], a[172:175], v[152:155], a[56:59]\n\t"
      "ds_read_b64_tr_b16 a[166:167], v234 offset:28928\n\t"
      "v_mfma_f32_16x16x32_bf16 a[60:63], a[172:175], v[156:159], a[60:63]\n\t"
      "v_exp_f32_e64 v215, v111\n\t"
      "v_mfma_f32_16x16x32_bf16 a[64:67], a[176:179], v[144:147], a[64:67]\n\t"
      "ds_read_b64_tr_b16 a[168:169], v234 offset:25088\n\t"
      "v_cvt_pk_bf16_f32 v167, v214, v215\n\t"
      "v_mfma_f32_16x16x32_bf16 a[68:71], a[176:179], v[148:151], a[68:71]\n\t"
      "ds_read_b64_tr_b16 a[170:171], v234 offset:29184\n\t"
      "v_mfma_f32_16x16x32_bf16 a[72:75], a[176:179], v[152:155], a[72:75]\n\t"
      "v_exp_f32_e64 v208, v112\n\t"
      "v_mfma_f32_16x16x32_bf16 a[76:79], a[176:179], v[156:159], a[76:79]\n\t"
      "ds_read_b64_tr_b16 a[172:173], v234 offset:25344\n\t"
      "v_mfma_f32_16x16x32_bf16 a[80:83], a[180:183], v[144:147], a[80:83]\n\t"
      "v_exp_f32_e64 v209, v113\n\t"
      "v_mfma_f32_16x16x32_bf16 a[84:87], a[180:183], v[148:151], a[84:87]\n\t"
      "ds_read_b64_tr_b16 a[174:175], v234 offset:29440\n\t"
      "v_cvt_pk_bf16_f32 v168, v208, v209\n\t"
      "v_mfma_f32_16x16x32_bf16 a[88:91], a[180:183], v[152:155], a[88:91]\n\t"
      "v_exp_f32_e64 v210, v114\n\t"
      "v_mfma_f32_16x16x32_bf16 a[92:95], a[180:183], v[156:159], a[92:95]\n\t"
      "ds_read_b64_tr_b16 a[176:177], v234 offset:25600\n\t"
      "v_mfma_f32_16x16x32_bf16 a[96:99], a[184:187], v[144:147], a[96:99]\n\t"
      "v_exp_f32_e64 v211, v115\n\t"
      "v_mfma_f32_16x16x32_bf16 a[100:103], a[184:187], v[148:151], a[100:103]\n\t"
      "ds_read_b64_tr_b16 a[178:179], v234 offset:29696\n\t"
      "v_cvt_pk_bf16_f32 v169, v210, v211\n\t"
      "v_mfma_f32_16x16x32_bf16 a[104:107], a[184:187], v[152:155], a[104:107]\n\t"
      "v_exp_f32_e64 v212, v116\n\t"
      "v_mfma_f32_16x16x32_bf16 a[108:111], a[184:187], v[156:159], a[108:111]\n\t"
      "ds_read_b64_tr_b16 a[180:181], v234 offset:25856\n\t"
      "v_mfma_f32_16x16x32_bf16 a[112:115], a[188:191], v[144:147], a[112:115]\n\t"
      "v_exp_f32_e64 v213, v117\n\t"
      "v_mfma_f32_16x16x32_bf16 a[116:119], a[188:191], v[148:151], a[116:119]\n\t"
      "ds_read_b64_tr_b16 a[182:183], v234 offset:29952\n\t"
      "v_cvt_pk_bf16_f32 v170, v212, v213\n\t"
      "v_mfma_f32_16x16x32_bf16 a[120:123], a[188:191], v[152:155], a[120:123]\n\t"
      "s_waitcnt lgkmcnt(11)\n\t"
      "v_exp_f32_e32 v214, v118\n\t"
      "v_mfma_f32_16x16x32_bf16 a[124:127], a[188:191], v[156:159], a[124:127]\n\t"
      "ds_read_b64_tr_b16 a[184:185], v234 offset:26112\n\t"
      "L_H4:\n\t"
      "v_mfma_f32_16x16x32_bf16 v[64:67], a[128:131], v[0:3], v[128:131]\n\t"
      "v_exp_f32_e64 v215, v119\n\t"
      "v_mfma_f32_16x16x32_bf16 v[64:67], a[132:135], v[8:11], v[64:67]\n\t"
      "ds_read_b64_tr_b16 a[186:187], v234 offset:30208\n\t"
      "v_cvt_pk_bf16_f32 v171, v214, v215\n\t"
      "v_mfma_f32_16x16x32_bf16 v[64:67], a[136:139], v[16:19], v[64:67]\n\t"
      "ds_read_b64_tr_b16 a[188:189], v234 offset:26368\n\t"
      "v_mfma_f32_16x16x32_bf16 v[64:67], a[140:143], v[24:27], v[64:67]\n\t"
      "v_exp_f32_e64 v208, v120\n\t"
      "v_mfma_f32_16x16x32_bf16 v[192:195], a[192:195], v[160:163], v[176:179]\n\t"
      "ds_read_b64_tr_b16 a[190:191], v234 offset:30464\n\t"
      "v_mfma_f32_16x16x32_bf16 v[68:71], a[144:147], v[0:3], v[128:131]\n\t"
      "s_add_u32 m0, s59, 16384\n\t"
      "v_exp_f32_e64 v209, v121\n\t"
      "v_mfma_f32_16x16x32_bf16 v[68:71], a[148:151], v[8:11], v[68:71]\n\t"
      "v_cvt_pk_bf16_f32 v172, v208, v209\n\t"
      "v_mfma_f32_16x16x32_bf16 v[68:71], a[152:155], v[16:19], v[68:71]\n\t"
      "v_exp_f32_e64 v210, v122\n\t"
      "v_mfma_f32_16x16x32_bf16 v[68:71], a[156:159], v[24:27], v[68:71]\n\t"
      "buffer_load_dwordx4 v235, s[76:79], s61 offen lds\n\t"
      "v_mfma_f32_16x16x32_bf16 v[72:75], a[128:131], v[4:7], v[132:135]\n\t"
      "v_exp_f32_e64 v211, v123\n\t"
      "v_mfma_f32_16x16x32_bf16 v[196:199], a[192:195], v[164:167], v[180:183]\n\t"
      "v_cvt_pk_bf16_f32 v173, v210, v211\n\t"
      "v_mfma_f32_16x16x32_bf16 v[72:75], a[132:135], v[12:15], v[72:75]\n\t"
      "v_exp_f32_e64 v212, v124\n\t"
      "v_mfma_f32_16x16x32_bf16 v[72:75], a[136:139], v[20:23], v[72:75]\n\t"
      "buffer_load_dwordx4 v236, s[76:79], s61 offen offset:1024 lds\n\t"
      "v_mfma_f32_16x16x32_bf16 v[72:75], a[140:143], v[28:31], v[72:75]\n\t"
      "v_exp_f32_e64 v213, v125\n\t"
      "v_mfma_f32_16x16x32_bf16 v[76:79], a[144:147], v[4:7], v[132:135]\n\t"
      "v_cvt_pk_bf16_f32 v174, v212, v213\n\t"
      "v_mfma_f32_16x16x32_bf16 v[76:79], a[148:151], v[12:15], v[76:79]\n\t"
      "v_exp_f32_e64 v214, v126\n\t"
      "v_mfma_f32_16x16x32_bf16 v[200:203], a[192:195], v[168:171], v[184:187]\n\t"
      "buffer_load_dwordx4 a[208:211], v250, s[52:55], s71 offen sc1\n\t"
      "v_mfma_f32_16x16x32_bf16 v[76:79], a[152:155], v[20:23], v[76:79]\n\t"
      "s_add_u32 s71, s71, s75\n\t"
      "v_exp_f32_e32 v215, v127\n\t"
      "v_mfma_f32_16x16x32_bf16 v[76:79], a[156:159], v[28:31], v[76:79]\n\t"
      "v_cvt_pk_bf16_f32 v175, v214, v215\n\t"
      "v_mfma_f32_16x16x32_bf16 v[80:83], a[128:131], v[32:35], v[136:139]\n\t"
      "buffer_load_dwordx4 v237, s[76:79], s61 offen offset:2048 lds\n\t"
      "v_mfma_f32_16x16x32_bf16 v[80:83], a[132:135], v[40:43], v[80:83]\n\t"
      "v_exp_f32_e64 v208, v64\n\t"
      "v_mfma_f32_16x16x32_bf16 v[80:83], a[136:139], v[48:51], v[80:83]\n\t"
      "v_mfma_f32_16x16x32_bf16 v[80:83], a[140:143], v[56:59], v[80:83]\n\t"
      "v_exp_f32_e64 v209, v65\n\t"
      "v_mfma_f32_16x16x32_bf16 v[204:207], a[192:195], v[172:175], v[188:191]\n\t"
      "v_cvt_pk_bf16_f32 v144, v208, v209\n\t"
      "v_mfma_f32_16x16x32_bf16 v[84:87], a[144:147], v[32:35], v[136:139]\n\t"
      "v_exp_f32_e64 v210, v66\n\t"
      "v_mfma_f32_16x16x32_bf16 v[84:87], a[148:151], v[40:43], v[84:87]\n\t"
      "buffer_load_dwordx4 v238, s[76:79], s61 offen offset:3072 lds\n\t"
      "v_mfma_f32_16x16x32_bf16 v[84:87], a[152:155], v[48:51], v[84:87]\n\t"
      "s_add_u32 s61, s61, 0x4000\n\t"
      "v_exp_f32_e64 v211, v67\n\t"
      "v_mfma_f32_16x16x32_bf16 v[84:87], a[156:159], v[56:59], v[84:87]\n\t"
      "v_max3_f32 v216, v192, v196, v200\n\t"
      "v_cvt_pk_bf16_f32 v145, v210, v211\n\t"
      "v_mfma_f32_16x16x32_bf16 v[88:91], a[128:131], v[36:39], v[140:143]\n\t"
      "v_exp_f32_e64 v212, v68\n\t"
      "v_mfma_f32_16x16x32_bf16 v[88:91], a[132:135], v[44:47], v[88:91]\n\t"
      "ds_read_b128 a[128:131], v230 offset:40960\n\t"
      "v_max_f32_e64 v216, v216, v204\n\t"
      "v_mfma_f32_16x16x32_bf16 v[88:91], a[136:139], v[52:55], v[88:91]\n\t"
      "v_exp_f32_e64 v213, v69\n\t"
      "v_mfma_f32_16x16x32_bf16 v[88:91], a[140:143], v[60:63], v[88:91]\n\t"
      "v_cmp_nge_f32_e64 vcc, s65, v216\n\t"
      "v_cvt_pk_bf16_f32 v146, v212, v213\n\t"
      "v_mfma_f32_16x16x32_bf16 v[92:95], a[144:147], v[36:39], v[140:143]\n\t"
      "s_waitcnt lgkmcnt(1)\n\t"
      "v_exp_f32_e32 v214, v70\n\t"
      "v_mfma_f32_16x16x32_bf16 v[92:95], a[148:151], v[44:47], v[92:95]\n\t"
      "ds_read_b128 a[132:135], v231 offset:40960\n\t"
      "v_mfma_f32_16x16x32_bf16 v[92:95], a[152:155], v[52:55], v[92:95]\n\t"
      "s_cbranch_vccnz L_RARE4\n\t"
      "L_BACK4:\n\t"
      "v_exp_f32_e32 v215, v71\n\t"
      "v_mfma_f32_16x16x32_bf16 v[92:95], a[156:159], v[60:63], v[92:95]\n\t"
      "ds_read_b128 a[136:139], v232 offset:40960\n\t"
      "v_cvt_pk_bf16_f32 v147, v214, v215\n\t"
      "v_mfma_f32_16x16x32_bf16 a[0:3], a[160:163], v[160:163], a[0:3]\n\t"
      "ds_read_b128 a[140:143], v233 offset:40960\n\t"
      "v_mfma_f32_16x16x32_bf16 a[4:7], a[160:163], v[164:167], a[4:7]\n\t"
      "v_exp_f32_e64 v208, v72\n\t"
      "v_mfma_f32_16x16x32_bf16 a[8:11], a[160:163], v[168:171], a[8:11]\n\t"
      "ds_read_b128 a[144:147], v230 offset:45056\n\t"
      "v_mfma_f32_16x16x32_bf16 a[12:15], a[160:163], v[172:175], a[12:15]\n\t"
      "v_exp_f32_e64 v209, v73\n\t"
      "v_mfma_f32_16x16x32_bf16 a[16:19], a[164:167], v[160:163], a[16:19]\n\t"
      "ds_read_b128 a[148:151], v231 offset:45056\n\t"
      "v_cvt_pk_bf16_f32 v148, v208, v209\n\t"
      "v_mfma_f32_16x16x32_bf16 a[20:23], a[164:167], v[164:167], a[20:23]\n\t"
      "v_exp_f32_e64 v210, v74\n\t"
      "v_mfma_f32_16x16x32_bf16 a[24:27], a[164:167], v[168:171], a[24:27]\n\t"
      "ds_read_b128 a[152:155], v232 offset:45056\n\t"
      "ds_read_b64_tr_b16 a[160:161], v234 offset:32768\n\t"
      "v_mfma_f32_16x16x32_bf16 a[28:31], a[164:167], v[172:175], a[28:31]\n\t"
      "v_exp_f32_e64 v211, v75\n\t"
      "v_mfma_f32_16x16x32_bf16 a[32:35], a[168:171], v[160:163], a[32:35]\n\t"
      "ds_read_b128 a[156:159], v233 offset:45056\n\t"
      "v_cvt_pk_bf16_f32 v149, v210, v211\n\t"
      "v_mfma_f32_16x16x32_bf16 a[36:39], a[168:171], v[164:167], a[36:39]\n\t"
      "v_exp_f32_e64 v212, v76\n\t"
      "v_mfma_f32_16x16x32_bf16 a[40:43], a[168:171], v[168:171], a[40:43]\n\t"
      "ds_read_b64_tr_b16 a[162:163], v234 offset:36864\n\t"
      "v_mfma_f32_16x16x32_bf16 a[44:47], a[168:171], v[172:175], a[44:47]\n\t"
      "v_exp_f32_e64 v213, v77\n\t"
      "v_mfma_f32_16x16x32_bf16 a[48:51], a[172:175], v[160:163], a[48:51]\n\t"
      "ds_read_b64_tr_b16 a[164:165], v234 offset:33024\n\t"
      "v_cvt_pk_bf16_f32 v150, v212, v213\n\t"
      "v_mfma_f32_16x16x32_bf16 a[52:55], a[172:175], v[164:167], a[52:55]\n\t"
      "v_exp_f32_e64 v214, v78\n\t"
      "v_mfma_f32_16x16x32_bf16 a[56:59], a[172:175], v[168:171], a[56:59]\n\t"
      "ds_read_b64_tr_b16 a[166:167], v234 offset:37120\n\t"
      "v_mfma_f32_16x16x32_bf16 a[60:63], a[172:175], v[172:175], a[60:63]\n\t"
      "v_exp_f32_e64 v215, v79\n\t"
      "v_mfma_f32_16x16x32_bf16 a[64:67], a[176:179], v[160:163], a[64:67]\n\t"
      "ds_read_b64_tr_b16 a[168:169], v234 offset:33280\n\t"
      "v_cvt_pk_bf16_f32 v151, v214, v215\n\t"
      "v_mfma_f32_16x16x32_bf16 a[68:71], a[176:179], v[164:167], a[68:71]\n\t"
      "ds_read_b64_tr_b16 a[170:171], v234 offset:37376\n\t"
      "v_mfma_f32_16x16x32_bf16 a[72:75], a[176:179], v[168:171], a[72:75]\n\t"
      "v_exp_f32_e64 v208, v80\n\t"
      "v_mfma_f32_16x16x32_bf16 a[76:79], a[176:179], v[172:175], a[76:79]\n\t"
      "ds_read_b64_tr_b16 a[172:173], v234 offset:33536\n\t"
      "v_mfma_f32_16x16x32_bf16 a[80:83], a[180:183], v[160:163], a[80:83]\n\t"
      "v_exp_f32_e64 v209, v81\n\t"
      "v_mfma_f32_16x16x32_bf16 a[84:87], a[180:183], v[164:167], a[84:87]\n\t"
      "ds_read_b64_tr_b16 a[174:175], v234 offset:37632\n\t"
      "v_cvt_pk_bf16_f32 v152, v208, v209\n\t"
      "v_mfma_f32_16x16x32_bf16 a[88:91], a[180:183], v[168:171], a[88:91]\n\t"
      "v_exp_f32_e64 v210, v82\n\t"
      "v_mfma_f32_16x16x32_bf16 a[92:95], a[180:183], v[172:175], a[92:95]\n\t"
      "ds_read_b64_tr_b16 a[176:177], v234 offset:33792\n\t"
      "v_mfma_f32_16x16x32_bf16 a[96:99], a[184:187], v[160:163], a[96:99]\n\t"
      "v_exp_f32_e64 v211, v83\n\t"
      "v_mfma_f32_16x16x32_bf16 a[100:103], a[184:187], v[164:167], a[100:103]\n\t"
      "ds_read_b64_tr_b16 a[178:179], v234 offset:37888\n\t"
      "v_cvt_pk_bf16_f32 v153, v210, v211\n\t"
      "v_mfma_f32_16x16x32_bf16 a[104:107], a[184:187], v[168:171], a[104:107]\n\t"
      "v_exp_f32_e64 v212, v84\n\t"
      "v_mfma_f32_16x16x32_bf16 a[108:111], a[184:187], v[172:175], a[108:111]\n\t"
      "ds_read_b64_tr_b16 a[180:181], v234 offset:34048\n\t"
      "v_mfma_f32_16x16x32_bf16 a[112:115], a[188:191], v[160:163], a[112:115]\n\t"
      "v_exp_f32_e64 v213, v85\n\t"
      "v_mfma_f32_16x16x32_bf16 a[116:119], a[188:191], v[164:167], a[116:119]\n\t"
      "ds_read_b64_tr_b16 a[182:183], v234 offset:38144\n\t"
      "v_cvt_pk_bf16_f32 v154, v212, v213\n\t"
      "v_mfma_f32_16x16x32_bf16 a[120:123], a[188:191], v[168:171], a[120:123]\n\t"
      "s_waitcnt lgkmcnt(11)\n\t"
      "v_exp_f32_e32 v214, v86\n\t"
      "v_mfma_f32_16x16x32_bf16 a[124:127], a[188:191], v[172:175], a[124:127]\n\t"
      "ds_read_b64_tr_b16 a[184:185], v234 offset:34304\n\t"
      "L_H5:\n\t"
      "v_mfma_f32_16x16x32_bf16 v[96:99], a[128:131], v[0:3], v[128:131]\n\t"
      "s_waitcnt vmcnt(12)\n\t"
      "v_exp_f32_e32 v215, v87\n\t"
      "v_mfma_f32_16x16x32_bf16 v[96:99], a[132:135], v[8:11], v[96:99]\n\t"
      "ds_read_b64_tr_b16 a[186:187], v234 offset:38400\n\t"
      "v_cvt_pk_bf16_f32 v155, v214, v215\n\t"
      "v_mfma_f32_16x16x32_bf16 v[96:99], a[136:139], v[16:19], v[96:99]\n\t"
      "ds_read_b64_tr_b16 a[188:189], v234 offset:34560\n\t"
      "v_mfma_f32_16x16x32_bf16 v[96:99], a[140:143], v[24:27], v[96:99]\n\t"
      "s_barrier\n\t"
      "v_exp_f32_e32 v208, v88\n\t"
      "v_mfma_f32_16x16x32_bf16 v[176:179], a[192:195], v[144:147], v[192:195]\n\t"
      "ds_read_b64_tr_b16 a[190:191], v234 offset:38656\n\t"
      "v_mfma_f32_16x16x32_bf16 v[100:103], a[144:147], v[0:3], v[128:131]\n\t"
      "s_add_u32 m0, s60, 16384\n\t"
      "v_exp_f32_e64 v209, v89\n\t"
      "v_mfma_f32_16x16x32_bf16 v[100:103], a[148:151], v[8:11], v[100:103]\n\t"
      "v_cvt_pk_bf16_f32 v156, v208, v209\n\t"
      "v_mfma_f32_16x16x32_bf16 v[100:103], a[152:155], v[16:19], v[100:103]\n\t"
      "v_exp_f32_e64 v210, v90\n\t"
      "v_mfma_f32_16x16x32_bf16 v[100:103], a[156:159], v[24:27], v[100:103]\n\t"
      "buffer_load_dwordx4 v239, s[36:39], s62 offen lds\n\t"
      "v_mfma_f32_16x16x32_bf16 v[104:107], a[128:131], v[4:7], v[132:135]\n\t"
      "v_exp_f32_e64 v211, v91\n\t"
      "v_mfma_f32_16x16x32_bf16 v[180:183], a[192:195], v[148:151], v[196:199]\n\t"
      "v_cvt_pk_bf16_f32 v157, v210, v211\n\t"
      "v_mfma_f32_16x16x32_bf16 v[104:107], a[132:135], v[12:15], v[104:107]\n\t"
      "v_exp_f32_e64 v212, v92\n\t"
      "v_mfma_f32_16x16x32_bf16 v[104:107], a[136:139], v[20:23], v[104:107]\n\t"
      "buffer_load_dwordx4 v240, s[36:39], s62 offen offset:1024 lds\n\t"
      "v_mfma_f32_16x16x32_bf16 v[104:107], a[140:143], v[28:31], v[104:107]\n\t"
      "v_exp_f32_e64 v213, v93\n\t"
      "v_mfma_f32_16x16x32_bf16 v[108:111], a[144:147], v[4:7], v[132:135]\n\t"
      "v_cvt_pk_bf16_f32 v158, v212, v213\n\t"
      "v_mfma_f32_16x16x32_bf16 v[108:111], a[148:151], v[12:15], v[108:111]\n\t"
      "v_exp_f32_e64 v214, v94\n\t"
      "v_mfma_f32_16x16x32_bf16 v[184:187], a[192:195], v[152:155], v[200:203]\n\t"
      "v_mfma_f32_16x16x32_bf16 v[108:111], a[152:155], v[20:23], v[108:111]\n\t"
      "v_exp_f32_e64 v215, v95\n\t"
      "v_mfma_f32_16x16x32_bf16 v[108:111], a[156:159], v[28:31], v[108:111]\n\t"
      "v_cvt_pk_bf16_f32 v159, v214, v215\n\t"
      "v_mfma_f32_16x16x32_bf16 v[112:115], a[128:131], v[32:35], v[136:139]\n\t"
      "buffer_load_dwordx4 v239, s[36:39], s62 offen offset:2048 lds\n\t"
      "v_mfma_f32_16x16x32_bf16 v[112:115], a[132:135], v[40:43], v[112:115]\n\t"
      "v_exp_f32_e64 v208, v96\n\t"
      "v_mfma_f32_16x16x32_bf16 v[112:115], a[136:139], v[48:51], v[112:115]\n\t"
      "v_mfma_f32_16x16x32_bf16 v[112:115], a[140:143], v[56:59], v[112:115]\n\t"
      "v_exp_f32_e64 v209, v97\n\t"
      "v_mfma_f32_16x16x32_bf16 v[188:191], a[192:195], v[156:159], v[204:207]\n\t"
      "v_cvt_pk_bf16_f32 v160, v208, v209\n\t"
      "v_mfma_f32_16x16x32_bf16 v[116:119], a[144:147], v[32:35], v[136:139]\n\t"
      "v_exp_f32_e64 v210, v98\n\t"
      "v_mfma_f32_16x16x32_bf16 v[116:119], a[148:151], v[40:43], v[116:119]\n\t"
      "buffer_load_dwordx4 v240, s[36:39], s62 offen offset:3072 lds\n\t"
      "v_mfma_f32_16x16x32_bf16 v[116:119], a[152:155], v[48:51], v[116:119]\n\t"
      "s_add_u32 s62, s62, 0x4000\n\t"
      "v_exp_f32_e64 v211, v99\n\t"
      "v_mfma_f32_16x16x32_bf16 v[116:119], a[156:159], v[56:59], v[116:119]\n\t"
      "v_max3_f32 v216, v176, v180, v184\n\t"
      "v_cvt_pk_bf16_f32 v161, v210, v211\n\t"
      "v_mfma_f32_16x16x32_bf16 v[120:123], a[128:131], v[36:39], v[140:143]\n\t"
      "v_exp_f32_e64 v212, v100\n\t"
      "v_mfma_f32_16x16x32_bf16 v[120:123], a[132:135], v[44:47], v[120:123]\n\t"
      "ds_read_b128 a[128:131], v230 offset:49152\n\t"
      "v_max_f32_e64 v216, v216, v188\n\t"
      "v_mfma_f32_16x16x32_bf16 v[120:123], a[136:139], v[52:55], v[120:123]\n\t"
      "v_exp_f32_e64 v213, v101\n\t"
      "v_mfma_f32_16x16x32_bf16 v[120:123], a[140:143], v[60:63], v[120:123]\n\t"
      "v_cmp_nge_f32_e64 vcc, s65, v216\n\t"
      "v_cvt_pk_bf16_f32 v162, v212, v213\n\t"
      "v_mfma_f32_16x16x32_bf16 v[124:127], a[144:147], v[36:39], v[140:143]\n\t"
      "s_waitcnt lgkmcnt(1)\n\t"
      "v_exp_f32_e32 v214, v102\n\t"
      "v_mfma_f32_16x16x32_bf16 v[124:127], a[148:151], v[44:47], v[124:127]\n\t"
      "ds_read_b128 a[132:135], v231 offset:49152\n\t"
      "v_mfma_f32_16x16x32_bf16 v[124:127], a[152:155], v[52:55], v[124:127]\n\t"
      "s_cbranch_vccnz L_RARE5\n\t"
      "L_BACK5:\n\t"
      "v_exp_f32_e32 v215, v103\n\t"
      "v_mfma_f32_16x16x32_bf16 v[124:127], a[156:159], v[60:63], v[124:127]\n\t"
      "ds_read_b128 a[136:139], v232 offset:49152\n\t"
      "v_cvt_pk_bf16_f32 v163, v214, v215\n\t"
      "v_mfma_f32_16x16x32_bf16 a[0:3], a[160:163], v[144:147], a[0:3]\n\t"
      "ds_read_b128 a[140:143], v233 offset:49152\n\t"
      "v_mfma_f32_16x16x32_bf16 a[4:7], a[160:163], v[148:151], a[4:7]\n\t"
      "v_exp_f32_e64 v208, v104\n\t"
      "v_mfma_f32_16x16x32_bf16 a[8:11], a[160:163], v[152:155], a[8:11]\n\t"
      "ds_read_b128 a[144:147], v230 offset:53248\n\t"
      "v_mfma_f32_16x16x32_bf16 a[12:15], a[160:163], v[156:159], a[12:15]\n\t"
      "v_exp_f32_e64 v209, v105\n\t"
      "v_mfma_f32_16x16x32_bf16 a[16:19], a[164:167], v[144:147], a[16:19]\n\t"
      "ds_read_b128 a[148:151], v231 offset:53248\n\t"
      "v_cvt_pk_bf16_f32 v164, v208, v209\n\t"
      "v_mfma_f32_16x16x32_bf16 a[20:23], a[164:167], v[148:151], a[20:23]\n\t"
      "v_exp_f32_e64 v210, v106\n\t"
      "v_mfma_f32_16x16x32_bf16 a[24:27], a[164:167], v[152:155], a[24:27]\n\t"
      "ds_read_b128 a[152:155], v232 offset:53248\n\t"
      "ds_read_b64_tr_b16 a[160:161], v234 offset:40960\n\t"
      "v_mfma_f32_16x16x32_bf16 a[28:31], a[164:167], v[156:159], a[28:31]\n\t"
      "v_exp_f32_e64 v211, v107\n\t"
      "v_mfma_f32_16x16x32_bf16 a[32:35], a[168:171], v[144:147], a[32:35]\n\t"
      "ds_read_b128 a[156:159], v233 offset:53248\n\t"
      "v_cvt_pk_bf16_f32 v165, v210, v211\n\t"
      "v_mfma_f32_16x16x32_bf16 a[36:39], a[168:171], v[148:151], a[36:39]\n\t"
      "v_exp_f32_e64 v212, v108\n\t"
      "v_mfma_f32_16x16x32_bf16 a[40:43], a[168:171], v[152:155], a[40:43]\n\t"
      "ds_read_b64_tr_b16 a[162:163], v234 offset:45056\n\t"
      "v_mfma_f32_16x16x32_bf16 a[44:47], a[168:171], v[156:159], a[44:47]\n\t"
      "v_exp_f32_e64 v213, v109\n\t"
      "v_mfma_f32_16x16x32_bf16 a[48:51], a[172:175], v[144:147], a[48:51]\n\t"
      "ds_read_b64_tr_b16 a[164:165], v234 offset:41216\n\t"
      "v_cvt_pk_bf16_f32 v166, v212, v213\n\t"
      "v_mfma_f32_16x16x32_bf16 a[52:55], a[172:175], v[148:151], a[52:55]\n\t"
      "v_exp_f32_e64 v214, v110\n\t"
      "v_mfma_f32_16x16x32_bf16 a[56:59], a[172:175], v[152:155], a[56:59]\n\t"
      "ds_read_b64_tr_b16 a[166:167], v234 offset:45312\n\t"
      "v_mfma_f32_16x16x32_bf16 a[60:63], a[172:175], v[156:159], a[60:63]\n\t"
      "v_exp_f32_e64 v215, v111\n\t"
      "v_mfma_f32_16x16x32_bf16 a[64:67], a[176:179], v[144:147], a[64:67]\n\t"
      "ds_read_b64_tr_b16 a[168:169], v234 offset:41472\n\t"
      "v_cvt_pk_bf16_f32 v167, v214, v215\n\t"
      "v_mfma_f32_16x16x32_bf16 a[68:71], a[176:179], v[148:151], a[68:71]\n\t"
      "ds_read_b64_tr_b16 a[170:171], v234 offset:45568\n\t"
      "v_mfma_f32_16x16x32_bf16 a[72:75], a[176:179], v[152:155], a[72:75]\n\t"
      "v_exp_f32_e64 v208, v112\n\t"
      "v_mfma_f32_16x16x32_bf16 a[76:79], a[176:179], v[156:159], a[76:79]\n\t"
      "ds_read_b64_tr_b16 a[172:173], v234 offset:41728\n\t"
      "v_mfma_f32_16x16x32_bf16 a[80:83], a[180:183], v[144:147], a[80:83]\n\t"
      "v_exp_f32_e64 v209, v113\n\t"
      "v_mfma_f32_16x16x32_bf16 a[84:87], a[180:183], v[148:151], a[84:87]\n\t"
      "ds_read_b64_tr_b16 a[174:175], v234 offset:45824\n\t"
      "v_cvt_pk_bf16_f32 v168, v208, v209\n\t"
      "v_mfma_f32_16x16x32_bf16 a[88:91], a[180:183], v[152:155], a[88:91]\n\t"
      "v_exp_f32_e64 v210, v114\n\t"
      "v_mfma_f32_16x16x32_bf16 a[92:95], a[180:183], v[156:159], a[92:95]\n\t"
      "ds_read_b64_tr_b16 a[176:177], v234 offset:41984\n\t"
      "v_mfma_f32_16x16x32_bf16 a[96:99], a[184:187], v[144:147], a[96:99]\n\t"
      "v_exp_f32_e64 v211, v115\n\t"
      "v_mfma_f32_16x16x32_bf16 a[100:103], a[184:187], v[148:151], a[100:103]\n\t"
      "ds_read_b64_tr_b16 a[178:179], v234 offset:46080\n\t"
      "v_cvt_pk_bf16_f32 v169, v210, v211\n\t"
      "v_mfma_f32_16x16x32_bf16 a[104:107], a[184:187], v[152:155], a[104:107]\n\t"
      "v_exp_f32_e64 v212, v116\n\t"
      "v_mfma_f32_16x16x32_bf16 a[108:111], a[184:187], v[156:159], a[108:111]\n\t"
      "ds_read_b64_tr_b16 a[180:181], v234 offset:42240\n\t"
      "v_mfma_f32_16x16x32_bf16 a[112:115], a[188:191], v[144:147], a[112:115]\n\t"
      "v_exp_f32_e64 v213, v117\n\t"
      "v_mfma_f32_16x16x32_bf16 a[116:119], a[188:191], v[148:151], a[116:119]\n\t"
      "ds_read_b64_tr_b16 a[182:183], v234 offset:46336\n\t"
      "v_cvt_pk_bf16_f32 v170, v212, v213\n\t"
      "v_mfma_f32_16x16x32_bf16 a[120:123], a[188:191], v[152:155], a[120:123]\n\t"
      "s_waitcnt lgkmcnt(11)\n\t"
      "v_exp_f32_e32 v214, v118\n\t"
      "v_mfma_f32_16x16x32_bf16 a[124:127], a[188:191], v[156:159], a[124:127]\n\t"
      "ds_read_b64_tr_b16 a[184:185], v234 offset:42496\n\t"
      "L_H6:\n\t"
      "v_mfma_f32_16x16x32_bf16 v[64:67], a[128:131], v[0:3], v[128:131]\n\t"
      "v_exp_f32_e64 v215, v119\n\t"
      "v_mfma_f32_16x16x32_bf16 v[64:67], a[132:135], v[8:11], v[64:67]\n\t"
      "ds_read_b64_tr_b16 a[186:187], v234 offset:46592\n\t"
      "v_cvt_pk_bf16_f32 v171, v214, v215\n\t"
      "v_mfma_f32_16x16x32_bf16 v[64:67], a[136:139], v[16:19], v[64:67]\n\t"
      "ds_read_b64_tr_b16 a[188:189], v234 offset:42752\n\t"
      "v_mfma_f32_16x16x32_bf16 v[64:67], a[140:143], v[24:27], v[64:67]\n\t"
      "v_exp_f32_e64 v208, v120\n\t"
      "v_mfma_f32_16x16x32_bf16 v[192:195], a[192:195], v[160:163], v[176:179]\n\t"
      "ds_read_b64_tr_b16 a[190:191], v234 offset:46848\n\t"
      "v_mfma_f32_16x16x32_bf16 v[68:71], a[144:147], v[0:3], v[128:131]\n\t"
      "s_add_u32 m0, s59, 32768\n\t"
      "v_exp_f32_e64 v209, v121\n\t"
      "v_mfma_f32_16x16x32_bf16 v[68:71], a[148:151], v[8:11], v[68:71]\n\t"
      "v_cvt_pk_bf16_f32 v172, v208, v209\n\t"
      "v_mfma_f32_16x16x32_bf16 v[68:71], a[152:155], v[16:19], v[68:71]\n\t"
      "v_exp_f32_e64 v210, v122\n\t"
      "v_mfma_f32_16x16x32_bf16 v[68:71], a[156:159], v[24:27], v[68:71]\n\t"
      "buffer_load_dwordx4 v235, s[76:79], s61 offen lds\n\t"
      "v_mfma_f32_16x16x32_bf16 v[72:75], a[128:131], v[4:7], v[132:135]\n\t"
      "v_exp_f32_e64 v211, v123\n\t"
      "v_mfma_f32_16x16x32_bf16 v[196:199], a[192:195], v[164:167], v[180:183]\n\t"
      "v_cvt_pk_bf16_f32 v173, v210, v211\n\t"
      "v_mfma_f32_16x16x32_bf16 v[72:75], a[132:135], v[12:15], v[72:75]\n\t"
      "v_exp_f32_e64 v212, v124\n\t"
      "v_mfma_f32_16x16x32_bf16 v[72:75], a[136:139], v[20:23], v[72:75]\n\t"
      "buffer_load_dwordx4 v236, s[76:79], s61 offen offset:1024 lds\n\t"
      "v_mfma_f32_16x16x32_bf16 v[72:75], a[140:143], v[28:31], v[72:75]\n\t"
      "v_exp_f32_e64 v213, v125\n\t"
      "v_mfma_f32_16x16x32_bf16 v[76:79], a[144:147], v[4:7], v[132:135]\n\t"
      "v_cvt_pk_bf16_f32 v174, v212, v213\n\t"
      "v_mfma_f32_16x16x32_bf16 v[76:79], a[148:151], v[12:15], v[76:79]\n\t"
      "v_exp_f32_e64 v214, v126\n\t"
      "v_mfma_f32_16x16x32_bf16 v[200:203], a[192:195], v[168:171], v[184:187]\n\t"
      "v_mfma_f32_16x16x32_bf16 v[76:79], a[152:155], v[20:23], v[76:79]\n\t"
      "v_exp_f32_e64 v215, v127\n\t"
      "v_mfma_f32_16x16x32_bf16 v[76:79], a[156:159], v[28:31], v[76:79]\n\t"
      "v_cvt_pk_bf16_f32 v175, v214, v215\n\t"
      "v_mfma_f32_16x16x32_bf16 v[80:83], a[128:131], v[32:35], v[136:139]\n\t"
      "buffer_load_dwordx4 v237, s[76:79], s61 offen offset:2048 lds\n\t"
      "v_mfma_f32_16x16x32_bf16 v[80:83], a[132:135], v[40:43], v[80:83]\n\t"
      "v_exp_f32_e64 v208, v64\n\t"
      "v_mfma_f32_16x16x32_bf16 v[80:83], a[136:139], v[48:51], v[80:83]\n\t"
      "v_mfma_f32_16x16x32_bf16 v[80:83], a[140:143], v[56:59], v[80:83]\n\t"
      "v_exp_f32_e64 v209, v65\n\t"
      "v_mfma_f32_16x16x32_bf16 v[204:207], a[192:195], v[172:175], v[188:191]\n\t"
      "v_cvt_pk_bf16_f32 v144, v208, v209\n\t"
      "v_mfma_f32_16x16x32_bf16 v[84:87], a[144:147], v[32:35], v[136:139]\n\t"
      "v_exp_f32_e64 v210, v66\n\t"
      "v_mfma_f32_16x16x32_bf16 v[84:87], a[148:151], v[40:43], v[84:87]\n\t"
      "buffer_load_dwordx4 v238, s[76:79], s61 offen offset:3072 lds\n\t"
      "v_mfma_f32_16x16x32_bf16 v[84:87], a[152:155], v[48:51], v[84:87]\n\t"
      "s_add_u32 s61, s61, 0x4000\n\t"
      "v_exp_f32_e64 v211, v67\n\t"
      "v_mfma_f32_16x16x32_bf16 v[84:87], a[156:159], v[56:59], v[84:87]\n\t"
      "v_max3_f32 v216, v192, v196, v200\n\t"
      "v_cvt_pk_bf16_f32 v145, v210, v211\n\t"
      "v_mfma_f32_16x16x32_bf16 v[88:91], a[128:131], v[36:39], v[140:143]\n\t"
      "v_exp_f32_e64 v212, v68\n\t"
      "v_mfma_f32_16x16x32_bf16 v[88:91], a[132:135], v[44:47], v[88:91]\n\t"
      "ds_read_b128 a[128:131], v230 offset:57344\n\t"
      "v_max_f32_e64 v216, v216, v204\n\t"
      "v_mfma_f32_16x16x32_bf16 v[88:91], a[136:139], v[52:55], v[88:91]\n\t"
      "v_exp_f32_e64 v213, v69\n\t"
      "v_mfma_f32_16x16x32_bf16 v[88:91], a[140:143], v[60:63], v[88:91]\n\t"
      "v_cmp_nge_f32_e64 vcc, s65, v216\n\t"
      "v_cvt_pk_bf16_f32 v146, v212, v213\n\t"
      "v_mfma_f32_16x16x32_bf16 v[92:95], a[144:147], v[36:39], v[140:143]\n\t"
      "s_waitcnt lgkmcnt(1)\n\t"
      "v_exp_f32_e32 v214, v70\n\t"
      "v_mfma_f32_16x16x32_bf16 v[92:95], a[148:151], v[44:47], v[92:95]\n\t"
      "ds_read_b128 a[132:135], v231 offset:57344\n\t"
      "v_mfma_f32_16x16x32_bf16 v[92:95], a[152:155], v[52:55], v[92:95]\n\t"
      "s_cbranch_vccnz L_RARE6\n\t"
      "L_BACK6:\n\t"
      "v_exp_f32_e32 v215, v71\n\t"
      "v_mfma_f32_16x16x32_bf16 v[92:95], a[156:159], v[60:63], v[92:95]\n\t"
      "ds_read_b128 a[136:139], v232 offset:57344\n\t"
      "v_cvt_pk_bf16_f32 v147, v214, v215\n\t"
      "v_mfma_f32_16x16x32_bf16 a[0:3], a[160:163], v[160:163], a[0:3]\n\t"
      "ds_read_b128 a[140:143], v233 offset:57344\n\t"
      "v_mfma_f32_16x16x32_bf16 a[4:7], a[160:163], v[164:167], a[4:7]\n\t"
      "v_exp_f32_e64 v208, v72\n\t"
      "v_mfma_f32_16x16x32_bf16 a[8:11], a[160:163], v[168:171], a[8:11]\n\t"
      "ds_read_b128 a[144:147], v230 offset:61440\n\t"
      "v_mfma_f32_16x16x32_bf16 a[12:15], a[160:163], v[172:175], a[12:15]\n\t"
      "v_exp_f32_e64 v209, v73\n\t"
      "v_mfma_f32_16x16x32_bf16 a[16:19], a[164:167], v[160:163], a[16:19]\n\t"
      "ds_read_b128 a[148:151], v231 offset:61440\n\t"
      "v_cvt_pk_bf16_f32 v148, v208, v209\n\t"
      "v_mfma_f32_16x16x32_bf16 a[20:23], a[164:167], v[164:167], a[20:23]\n\t"
      "v_exp_f32_e64 v210, v74\n\t"
      "v_mfma_f32_16x16x32_bf16 a[24:27], a[164:167], v[168:171], a[24:27]\n\t"
      "ds_read_b128 a[152:155], v232 offset:61440\n\t"
      "ds_read_b64_tr_b16 a[160:161], v234 offset:49152\n\t"
      "v_mfma_f32_16x16x32_bf16 a[28:31], a[164:167], v[172:175], a[28:31]\n\t"
      "v_exp_f32_e64 v211, v75\n\t"
      "v_mfma_f32_16x16x32_bf16 a[32:35], a[168:171], v[160:163], a[32:35]\n\t"
      "ds_read_b128 a[156:159], v233 offset:61440\n\t"
      "v_cvt_pk_bf16_f32 v149, v210, v211\n\t"
      "v_mfma_f32_16x16x32_bf16 a[36:39], a[168:171], v[164:167], a[36:39]\n\t"
      "v_exp_f32_e64 v212, v76\n\t"
      "v_mfma_f32_16x16x32_bf16 a[40:43], a[168:171], v[168:171], a[40:43]\n\t"
      "ds_read_b64_tr_b16 a[162:163], v234 offset:53248\n\t"
      "v_mfma_f32_16x16x32_bf16 a[44:47], a[168:171], v[172:175], a[44:47]\n\t"
      "v_exp_f32_e64 v213, v77\n\t"
      "v_mfma_f32_16x16x32_bf16 a[48:51], a[172:175], v[160:163], a[48:51]\n\t"
      "ds_read_b64_tr_b16 a[164:165], v234 offset:49408\n\t"
      "v_cvt_pk_bf16_f32 v150, v212, v213\n\t"
      "v_mfma_f32_16x16x32_bf16 a[52:55], a[172:175], v[164:167], a[52:55]\n\t"
      "v_exp_f32_e64 v214, v78\n\t"
      "v_mfma_f32_16x16x32_bf16 a[56:59], a[172:175], v[168:171], a[56:59]\n\t"
      "ds_read_b64_tr_b16 a[166:167], v234 offset:53504\n\t"
      "v_mfma_f32_16x16x32_bf16 a[60:63], a[172:175], v[172:175], a[60:63]\n\t"
      "v_exp_f32_e64 v215, v79\n\t"
      "v_mfma_f32_16x16x32_bf16 a[64:67], a[176:179], v[160:163], a[64:67]\n\t"
      "ds_read_b64_tr_b16 a[168:169], v234 offset:49664\n\t"
      "v_cvt_pk_bf16_f32 v151, v214, v215\n\t"
      "v_mfma_f32_16x16x32_bf16 a[68:71], a[176:179], v[164:167], a[68:71]\n\t"
      "ds_read_b64_tr_b16 a[170:171], v234 offset:53760\n\t"
      "v_mfma_f32_16x16x32_bf16 a[72:75], a[176:179], v[168:171], a[72:75]\n\t"
      "v_exp_f32_e64 v208, v80\n\t"
      "v_mfma_f32_16x16x32_bf16 a[76:79], a[176:179], v[172:175], a[76:79]\n\t"
      "ds_read_b64_tr_b16 a[172:173], v234 offset:49920\n\t"
      "v_mfma_f32_16x16x32_bf16 a[80:83], a[180:183], v[160:163], a[80:83]\n\t"
      "v_exp_f32_e64 v209, v81\n\t"
      "v_mfma_f32_16x16x32_bf16 a[84:87], a[180:183], v[164:167], a[84:87]\n\t"
      "ds_read_b64_tr_b16 a[174:175], v234 offset:54016\n\t"
      "v_cvt_pk_bf16_f32 v152, v208, v209\n\t"
      "v_mfma_f32_16x16x32_bf16 a[88:91], a[180:183], v[168:171], a[88:91]\n\t"
      "v_exp_f32_e64 v210, v82\n\t"
      "v_mfma_f32_16x16x32_bf16 a[92:95], a[180:183], v[172:175], a[92:95]\n\t"
      "ds_read_b64_tr_b16 a[176:177], v234 offset:50176\n\t"
      "v_mfma_f32_16x16x32_bf16 a[96:99], a[184:187], v[160:163], a[96:99]\n\t"
      "v_exp_f32_e64 v211, v83\n\t"
      "v_mfma_f32_16x16x32_bf16 a[100:103], a[184:187], v[164:167], a[100:103]\n\t"
      "ds_read_b64_tr_b16 a[178:179], v234 offset:54272\n\t"
      "v_cvt_pk_bf16_f32 v153, v210, v211\n\t"
      "v_mfma_f32_16x16x32_bf16 a[104:107], a[184:187], v[168:171], a[104:107]\n\t"
      "v_exp_f32_e64 v212, v84\n\t"
      "v_mfma_f32_16x16x32_bf16 a[108:111], a[184:187], v[172:175], a[108:111]\n\t"
      "ds_read_b64_tr_b16 a[180:181], v234 offset:50432\n\t"
      "v_mfma_f32_16x16x32_bf16 a[112:115], a[188:191], v[160:163], a[112:115]\n\t"
      "v_exp_f32_e64 v213, v85\n\t"
      "v_mfma_f32_16x16x32_bf16 a[116:119], a[188:191], v[164:167], a[116:119]\n\t"
      "ds_read_b64_tr_b16 a[182:183], v234 offset:54528\n\t"
      "v_cvt_pk_bf16_f32 v154, v212, v213\n\t"
      "v_mfma_f32_16x16x32_bf16 a[120:123], a[188:191], v[168:171], a[120:123]\n\t"
      "s_waitcnt lgkmcnt(11)\n\t"
      "v_exp_f32_e32 v214, v86\n\t"
      "v_mfma_f32_16x16x32_bf16 a[124:127], a[188:191], v[172:175], a[124:127]\n\t"
      "ds_read_b64_tr_b16 a[184:185], v234 offset:50688\n\t"
      "L_H7:\n\t"
      "v_mfma_f32_16x16x32_bf16 v[96:99], a[128:131], v[0:3], v[128:131]\n\t"
      "s_waitcnt vmcnt(12)\n\t"
      "v_exp_f32_e32 v215, v87\n\t"
      "v_mfma_f32_16x16x32_bf16 v[96:99], a[132:135], v[8:11], v[96:99]\n\t"
      "ds_read_b64_tr_b16 a[186:187], v234 offset:54784\n\t"
      "v_cvt_pk_bf16_f32 v155, v214, v215\n\t"
      "v_mfma_f32_16x16x32_bf16 v[96:99], a[136:139], v[16:19], v[96:99]\n\t"
      "ds_read_b64_tr_b16 a[188:189], v234 offset:50944\n\t"
      "v_mfma_f32_16x16x32_bf16 v[96:99], a[140:143], v[24:27], v[96:99]\n\t"
      "s_barrier\n\t"
      "v_exp_f32_e32 v208, v88\n\t"
      "v_mfma_f32_16x16x32_bf16 v[176:179], a[192:195], v[144:147], v[192:195]\n\t"
      "ds_read_b64_tr_b16 a[190:191], v234 offset:55040\n\t"
      "v_mfma_f32_16x16x32_bf16 v[100:103], a[144:147], v[0:3], v[128:131]\n\t"
      "s_add_u32 m0, s60, 32768\n\t"
      "v_exp_f32_e64 v209, v89\n\t"
      "v_mfma_f32_16x16x32_bf16 v[100:103], a[148:151], v[8:11], v[100:103]\n\t"
      "v_cvt_pk_bf16_f32 v156, v208, v209\n\t"
      "v_mfma_f32_16x16x32_bf16 v[100:103], a[152:155], v[16:19], v[100:103]\n\t"
      "v_exp_f32_e64 v210, v90\n\t"
      "v_mfma_f32_16x16x32_bf16 v[100:103], a[156:159], v[24:27], v[100:103]\n\t"
      "buffer_load_dwordx4 v239, s[36:39], s62 offen lds\n\t"
      "v_mfma_f32_16x16x32_bf16 v[104:107], a[128:131], v[4:7], v[132:135]\n\t"
      "v_exp_f32_e64 v211, v91\n\t"
      "v_mfma_f32_16x16x32_bf16 v[180:183], a[192:195], v[148:151], v[196:199]\n\t"
      "v_cvt_pk_bf16_f32 v157, v210, v211\n\t"
      "v_mfma_f32_16x16x32_bf16 v[104:107], a[132:135], v[12:15], v[104:107]\n\t"
      "v_exp_f32_e64 v212, v92\n\t"
      "v_mfma_f32_16x16x32_bf16 v[104:107], a[136:139], v[20:23], v[104:107]\n\t"
      "buffer_load_dwordx4 v240, s[36:39], s62 offen offset:1024 lds\n\t"
      "v_mfma_f32_16x16x32_bf16 v[104:107], a[140:143], v[28:31], v[104:107]\n\t"
      "v_exp_f32_e64 v213, v93\n\t"
      "v_mfma_f32_16x16x32_bf16 v[108:111], a[144:147], v[4:7], v[132:135]\n\t"
      "v_cvt_pk_bf16_f32 v158, v212, v213\n\t"
      "v_mfma_f32_16x16x32_bf16 v[108:111], a[148:151], v[12:15], v[108:111]\n\t"
      "v_exp_f32_e64 v214, v94\n\t"
      "v_mfma_f32_16x16x32_bf16 v[184:187], a[192:195], v[152:155], v[200:203]\n\t"
      "v_mfma_f32_16x16x32_bf16 v[108:111], a[152:155], v[20:23], v[108:111]\n\t"
      "v_exp_f32_e64 v215, v95\n\t"
      "v_mfma_f32_16x16x32_bf16 v[108:111], a[156:159], v[28:31], v[108:111]\n\t"
      "v_cvt_pk_bf16_f32 v159, v214, v215\n\t"
      "v_mfma_f32_16x16x32_bf16 v[112:115], a[128:131], v[32:35], v[136:139]\n\t"
      "buffer_load_dwordx4 v239, s[36:39], s62 offen offset:2048 lds\n\t"
      "v_mfma_f32_16x16x32_bf16 v[112:115], a[132:135], v[40:43], v[112:115]\n\t"
      "v_exp_f32_e64 v208, v96\n\t"
      "v_mfma_f32_16x16x32_bf16 v[112:115], a[136:139], v[48:51], v[112:115]\n\t"
      "v_mfma_f32_16x16x32_bf16 v[112:115], a[140:143], v[56:59], v[112:115]\n\t"
      "v_exp_f32_e64 v209, v97\n\t"
      "v_mfma_f32_16x16x32_bf16 v[188:191], a[192:195], v[156:159], v[204:207]\n\t"
      "v_cvt_pk_bf16_f32 v160, v208, v209\n\t"
      "v_mfma_f32_16x16x32_bf16 v[116:119], a[144:147], v[32:35], v[136:139]\n\t"
      "v_exp_f32_e64 v210, v98\n\t"
      "v_mfma_f32_16x16x32_bf16 v[116:119], a[148:151], v[40:43], v[116:119]\n\t"
      "buffer_load_dwordx4 v240, s[36:39], s62 offen offset:3072 lds\n\t"
      "v_mfma_f32_16x16x32_bf16 v[116:119], a[152:155], v[48:51], v[116:119]\n\t"
      "s_add_u32 s62, s62, 0x4000\n\t"
      "v_exp_f32_e64 v211, v99\n\t"
      "v_mfma_f32_16x16x32_bf16 v[116:119], a[156:159], v[56:59], v[116:119]\n\t"
      "v_max3_f32 v216, v176, v180, v184\n\t"
      "v_cvt_pk_bf16_f32 v161, v210, v211\n\t"
      "v_mfma_f32_16x16x32_bf16 v[120:123], a[128:131], v[36:39], v[140:143]\n\t"
      "v_exp_f32_e64 v212, v100\n\t"
      "v_mfma_f32_16x16x32_bf16 v[120:123], a[132:135], v[44:47], v[120:123]\n\t"
      "ds_read_b128 a[128:131], v230 offset:0\n\t"
      "v_max_f32_e64 v216, v216, v188\n\t"
      "v_mfma_f32_16x16x32_bf16 v[120:123], a[136:139], v[52:55], v[120:123]\n\t"
      "v_exp_f32_e64 v213, v101\n\t"
      "v_mfma_f32_16x16x32_bf16 v[120:123], a[140:143], v[60:63], v[120:123]\n\t"
      "v_cmp_nge_f32_e64 vcc, s65, v216\n\t"
      "v_cvt_pk_bf16_f32 v162, v212, v213\n\t"
      "v_mfma_f32_16x16x32_bf16 v[124:127], a[144:147], v[36:39], v[140:143]\n\t"
      "s_waitcnt lgkmcnt(1)\n\t"
      "v_exp_f32_e32 v214, v102\n\t"
      "v_mfma_f32_16x16x32_bf16 v[124:127], a[148:151], v[44:47], v[124:127]\n\t"
      "ds_read_b128 a[132:135], v231 offset:0\n\t"
      "v_mfma_f32_16x16x32_bf16 v[124:127], a[152:155], v[52:55], v[124:127]\n\t"
      "s_cbranch_vccnz L_RARE7\n\t"
      "L_BACK7:\n\t"
      "v_exp_f32_e32 v215, v103\n\t"
      "v_mfma_f32_16x16x32_bf16 v[124:127], a[156:159], v[60:63], v[124:127]\n\t"
      "ds_read_b128 a[136:139], v232 offset:0\n\t"
      "v_cvt_pk_bf16_f32 v163, v214, v215\n\t"
      "v_mfma_f32_16x16x32_bf16 a[0:3], a[160:163], v[144:147], a[0:3]\n\t"
      "ds_read_b128 a[140:143], v233 offset:0\n\t"
      "v_mfma_f32_16x16x32_bf16 a[4:7], a[160:163], v[148:151], a[4:7]\n\t"
      "v_exp_f32_e64 v208, v104\n\t"
      "v_mfma_f32_16x16x32_bf16 a[8:11], a[160:163], v[152:155], a[8:11]\n\t"
      "ds_read_b128 a[144:147], v230 offset:4096\n\t"
      "v_mfma_f32_16x16x32_bf16 a[12:15], a[160:163], v[156:159], a[12:15]\n\t"
      "v_exp_f32_e64 v209, v105\n\t"
      "v_mfma_f32_16x16x32_bf16 a[16:19], a[164:167], v[144:147], a[16:19]\n\t"
      "ds_read_b128 a[148:151], v231 offset:4096\n\t"
      "v_cvt_pk_bf16_f32 v164, v208, v209\n\t"
      "v_mfma_f32_16x16x32_bf16 a[20:23], a[164:167], v[148:151], a[20:23]\n\t"
      "v_exp_f32_e64 v210, v106\n\t"
      "v_mfma_f32_16x16x32_bf16 a[24:27], a[164:167], v[152:155], a[24:27]\n\t"
      "ds_read_b128 a[152:155], v232 offset:4096\n\t"
      "ds_read_b64_tr_b16 a[160:161], v234 offset:57344\n\t"
      "v_mfma_f32_16x16x32_bf16 a[28:31], a[164:167], v[156:159], a[28:31]\n\t"
      "v_exp_f32_e64 v211, v107\n\t"
      "v_mfma_f32_16x16x32_bf16 a[32:35], a[168:171], v[144:147], a[32:35]\n\t"
      "ds_read_b128 a[156:159], v233 offset:4096\n\t"
      "v_cvt_pk_bf16_f32 v165, v210, v211\n\t"
      "v_mfma_f32_16x16x32_bf16 a[36:39], a[168:171], v[148:151], a[36:39]\n\t"
      "v_exp_f32_e64 v212, v108\n\t"
      "v_mfma_f32_16x16x32_bf16 a[40:43], a[168:171], v[152:155], a[40:43]\n\t"
      "ds_read_b64_tr_b16 a[162:163], v234 offset:61440\n\t"
      "v_mfma_f32_16x16x32_bf16 a[44:47], a[168:171], v[156:159], a[44:47]\n\t"
      "v_exp_f32_e64 v213, v109\n\t"
      "v_mfma_f32_16x16x32_bf16 a[48:51], a[172:175], v[144:147], a[48:51]\n\t"
      "ds_read_b64_tr_b16 a[164:165], v234 offset:57600\n\t"
      "v_cvt_pk_bf16_f32 v166, v212, v213\n\t"
      "v_mfma_f32_16x16x32_bf16 a[52:55], a[172:175], v[148:151], a[52:55]\n\t"
      "v_exp_f32_e64 v214, v110\n\t"
      "v_mfma_f32_16x16x32_bf16 a[56:59], a[172:175], v[152:155], a[56:59]\n\t"
      "ds_read_b64_tr_b16 a[166:167], v234 offset:61696\n\t"
      "v_mfma_f32_16x16x32_bf16 a[60:63], a[172:175], v[156:159], a[60:63]\n\t"
      "v_exp_f32_e64 v215, v111\n\t"
      "v_mfma_f32_16x16x32_bf16 a[64:67], a[176:179], v[144:147], a[64:67]\n\t"
      "ds_read_b64_tr_b16 a[168:169], v234 offset:57856\n\t"
      "v_cvt_pk_bf16_f32 v167, v214, v215\n\t"
      "v_mfma_f32_16x16x32_bf16 a[68:71], a[176:179], v[148:151], a[68:71]\n\t"
      "ds_read_b64_tr_b16 a[170:171], v234 offset:61952\n\t"
      "v_mfma_f32_16x16x32_bf16 a[72:75], a[176:179], v[152:155], a[72:75]\n\t"
      "v_exp_f32_e64 v208, v112\n\t"
      "v_mfma_f32_16x16x32_bf16 a[76:79], a[176:179], v[156:159], a[76:79]\n\t"
      "ds_read_b64_tr_b16 a[172:173], v234 offset:58112\n\t"
      "v_mfma_f32_16x16x32_bf16 a[80:83], a[180:183], v[144:147], a[80:83]\n\t"
      "v_exp_f32_e64 v209, v113\n\t"
      "v_mfma_f32_16x16x32_bf16 a[84:87], a[180:183], v[148:151], a[84:87]\n\t"
      "ds_read_b64_tr_b16 a[174:175], v234 offset:62208\n\t"
      "v_cvt_pk_bf16_f32 v168, v208, v209\n\t"
      "v_mfma_f32_16x16x32_bf16 a[88:91], a[180:183], v[152:155], a[88:91]\n\t"
      "v_exp_f32_e64 v210, v114\n\t"
      "v_mfma_f32_16x16x32_bf16 a[92:95], a[180:183], v[156:159], a[92:95]\n\t"
      "ds_read_b64_tr_b16 a[176:177], v234 offset:58368\n\t"
      "v_mfma_f32_16x16x32_bf16 a[96:99], a[184:187], v[144:147], a[96:99]\n\t"
      "v_exp_f32_e64 v211, v115\n\t"
      "v_mfma_f32_16x16x32_bf16 a[100:103], a[184:187], v[148:151], a[100:103]\n\t"
      "ds_read_b64_tr_b16 a[178:179], v234 offset:62464\n\t"
      "v_cvt_pk_bf16_f32 v169, v210, v211\n\t"
      "v_mfma_f32_16x16x32_bf16 a[104:107], a[184:187], v[152:155], a[104:107]\n\t"
      "s_add_u32 s63, s63, 1\n\t"
      "v_exp_f32_e32 v212, v116\n\t"
      "v_mfma_f32_16x16x32_bf16 a[108:111], a[184:187], v[156:159], a[108:111]\n\t"
      "ds_read_b64_tr_b16 a[180:181], v234 offset:58624\n\t"
      "v_mfma_f32_16x16x32_bf16 a[112:115], a[188:191], v[144:147], a[112:115]\n\t"
      "s_cmp_lt_u32 s63, 8\n\t"
      "v_exp_f32_e32 v213, v117\n\t"
      "v_mfma_f32_16x16x32_bf16 a[116:119], a[188:191], v[148:151], a[116:119]\n\t"
      "ds_read_b64_tr_b16 a[182:183], v234 offset:62720\n\t"
      "v_cvt_pk_bf16_f32 v170, v212, v213\n\t"
      "v_mfma_f32_16x16x32_bf16 a[120:123], a[188:191], v[152:155], a[120:123]\n\t"
      "s_waitcnt lgkmcnt(11)\n\t"
      "v_exp_f32_e32 v214, v118\n\t"
      "v_mfma_f32_16x16x32_bf16 a[124:127], a[188:191], v[156:159], a[124:127]\n\t"
      "ds_read_b64_tr_b16 a[184:185], v234 offset:58880\n\t"
      "s_cbranch_scc1 L_H0\n\t"
      ".p2align 4\n\t"
      "L_POST:\n\t"
      "s_lshl_b32 s73, s74, 1\n\t"
      "s_add_u32 s73, s73, 0\n\t"
      "s_and_b32 s73, s73, 7\n\t"
      "s_lshl_b32 s73, s73, 16\n\t"
      "s_add_u32 m0, s59, 0\n\t"
      "s_nop 0\n\t"
      "buffer_load_dwordx4 v244, s[40:43], s73 offen lds\n\t"
      "v_exp_f32_e32 v215, v119\n\t"
      "s_nop 0\n\t"
      "ds_read_b64_tr_b16 a[186:187], v234 offset:62976\n\t"
      "v_cvt_pk_bf16_f32 v171, v214, v215\n\t"
      "s_add_u32 m0, s59, 1024\n\t"
      "s_nop 0\n\t"
      "s_nop 0\n\t"
      "buffer_load_dwordx4 v245, s[40:43], s73 offen lds\n\t"
      "ds_read_b64_tr_b16 a[188:189], v234 offset:59136\n\t"
      "s_nop 0\n\t"
      "v_exp_f32_e64 v208, v120\n\t"
      "s_nop 1\n\t"
      "v_mfma_f32_16x16x32_bf16 v[192:195], a[192:195], v[160:163], v[176:179]\n\t"
      "s_add_u32 s73, s73, 0x2000\n\t"
      "s_add_u32 m0, s59, 2048\n\t"
      "s_nop 0\n\t"
      "s_nop 0\n\t"
      "buffer_load_dwordx4 v244, s[40:43], s73 offen lds\n\t"
      "ds_read_b64_tr_b16 a[190:191], v234 offset:63232\n\t"
      "s_nop 0\n\t"
      "v_exp_f32_e32 v209, v121\n\t"
      "s_add_u32 m0, s59, 3072\n\t"
      "s_nop 0\n\t"
      "s_nop 0\n\t"
      "buffer_load_dwordx4 v245, s[40:43], s73 offen lds\n\t"
      "v_cvt_pk_bf16_f32 v172, v208, v209\n\t"
      "s_nop 0\n\t"
      "v_exp_f32_e32 v210, v122\n\t"
      "s_lshl_b32 s73, s74, 1\n\t"
      "s_add_u32 s73, s73, 1\n\t"
      "s_and_b32 s73, s73, 7\n\t"
      "s_lshl_b32 s73, s73, 16\n\t"
      "s_add_u32 m0, s59, 65536\n\t"
      "s_nop 0\n\t"
      "s_nop 0\n\t"
      "buffer_load_dwordx4 v244, s[40:43], s73 offen lds\n\t"
      "s_nop 0\n\t"
      "v_exp_f32_e64 v211, v123\n\t"
      "s_nop 1\n\t"
      "v_mfma_f32_16x16x32_bf16 v[196:199], a[192:195], v[164:167], v[180:183]\n\t"
      "s_add_u32 m0, s59, 66560\n\t"
      "s_nop 0\n\t"
      "s_nop 0\n\t"
      "buffer_load_dwordx4 v245, s[40:43], s73 offen lds\n\t"
      "v_cvt_pk_bf16_f32 v173, v210, v211\n\t"
      "s_nop 0\n\t"
      "v_exp_f32_e32 v212, v124\n\t"
      "s_add_u32 s73, s73, 0x2000\n\t"
      "s_add_u32 m0, s59, 67584\n\t"
      "s_nop 0\n\t"
      "s_nop 0\n\t"
      "buffer_load_dwordx4 v244, s[40:43], s73 offen lds\n\t"
      "s_nop 0\n\t"
      "v_exp_f32_e32 v213, v125\n\t"
      "s_add_u32 m0, s59, 68608\n\t"
      "s_nop 0\n\t"
      "s_nop 0\n\t"
      "buffer_load_dwordx4 v245, s[40:43], s73 offen lds\n\t"
      "v_cvt_pk_bf16_f32 v174, v212, v213\n\t"
      "s_nop 0\n\t"
      "v_exp_f32_e64 v214, v126\n\t"
      "s_nop 1\n\t"
      "v_mfma_f32_16x16x32_bf16 v[200:203], a[192:195], v[168:171], v[184:187]\n\t"
      "s_lshl_b32 s73, s74, 1\n\t"
      "s_add_u32 s73, s73, 2\n\t"
      "s_and_b32 s73, s73, 7\n\t"
      "s_lshl_b32 s73, s73, 16\n\t"
      "s_add_u32 m0, s59, 16384\n\t"
      "s_nop 0\n\t"
      "s_nop 0\n\t"
      "buffer_load_dwordx4 v244, s[40:43], s73 offen lds\n\t"
      "s_nop 0\n\t"
      "v_exp_f32_e32 v215, v127\n\t"
      "s_add_u32 m0, s59, 17408\n\t"
      "s_nop 0\n\t"
      "s_nop 0\n\t"
      "buffer_load_dwordx4 v245, s[40:43], s73 offen lds\n\t"
      "v_cvt_pk_bf16_f32 v175, v214, v215\n\t"
      "s_nop 0\n\t"
      "s_nop 0\n\t"
      "s_add_u32 s73, s73, 0x2000\n\t"
      "s_add_u32 m0, s59, 18432\n\t"
      "s_nop 0\n\t"
      "s_nop 0\n\t"
      "buffer_load_dwordx4 v244, s[40:43], s73 offen lds\n\t"
      "s_nop 0\n\t"
      "s_nop 0\n\t"
      "s_add_u32 m0, s59, 19456\n\t"
      "s_nop 0\n\t"
      "s_nop 0\n\t"
      "buffer_load_dwordx4 v245, s[40:43], s73 offen lds\n\t"
      "s_nop 1\n\t"
      "s_nop 0\n\t"
      "v_mfma_f32_16x16x32_bf16 v[204:207], a[192:195], v[172:175], v[188:191]\n\t"
      "s_nop 0\n\t"
      "s_lshl_b32 s73, s74, 1\n\t"
      "s_add_u32 s73, s73, 3\n\t"
      "s_and_b32 s73, s73, 7\n\t"
      "s_lshl_b32 s73, s73, 16\n\t"
      "s_nop 0\n\t"
      "s_add_u32 m0, s59, 81920\n\t"
      "s_nop 0\n\t"
      "s_nop 0\n\t"
      "buffer_load_dwordx4 v244, s[40:43], s73 offen lds\n\t"
      "s_nop 0\n\t"
      "s_nop 0\n\t"
      "s_add_u32 m0, s59, 82944\n\t"
      "s_nop 0\n\t"
      "s_nop 0\n\t"
      "buffer_load_dwordx4 v245, s[40:43], s73 offen lds\n\t"
      "s_nop 0\n\t"
      "s_nop 5\n\t"
      "v_max3_f32 v216, v192, v196, v200\n\t"
      "s_add_u32 s73, s73, 0x2000\n\t"
      "s_add_u32 m0, s59, 83968\n\t"
      "s_nop 0\n\t"
      "s_nop 0\n\t"
      "buffer_load_dwordx4 v244, s[40:43], s73 offen lds\n\t"
      "s_nop 0\n\t"
      "s_nop 5\n\t"
      "v_max_f32_e64 v216, v216, v204\n\t"
      "s_add_u32 m0, s59, 84992\n\t"
      "s_nop 0\n\t"
      "s_nop 0\n\t"
      "buffer_load_dwordx4 v245, s[40:43], s73 offen lds\n\t"
      "s_nop 0\n\t"
      "v_cmp_nge_f32_e32 vcc, s65, v216\n\t"
      "s_lshl_b32 s73, s74, 1\n\t"
      "s_add_u32 s73, s73, 4\n\t"
      "s_and_b32 s73, s73, 7\n\t"
      "s_lshl_b32 s73, s73, 16\n\t"
      "s_add_u32 m0, s59, 32768\n\t"
      "s_nop 0\n\t"
      "s_nop 0\n\t"
      "buffer_load_dwordx4 v244, s[40:43], s73 offen lds\n\t"
      "s_waitcnt lgkmcnt(0)\n\t"
      "s_nop 0\n\t"
      "s_add_u32 m0, s59, 33792\n\t"
      "s_nop 0\n\t"
      "s_nop 0\n\t"
      "buffer_load_dwordx4 v245, s[40:43], s73 offen lds\n\t"
      "s_cbranch_vccnz L_RARE8\n\t"
      "L_BACK8:\n\t"
      "s_nop 0\n\t"
      "v_mfma_f32_16x16x32_bf16 a[0:3], a[160:163], v[160:163], a[0:3]\n\t"
      "s_add_u32 s73, s73, 0x2000\n\t"
      "s_add_u32 m0, s59, 34816\n\t"
      "s_nop 0\n\t"
      "s_nop 0\n\t"
      "buffer_load_dwordx4 v244, s[40:43], s73 offen lds\n\t"
      "v_mov_b32_e32 v218, v192\n\t"
      "v_mov_b32_e32 v219, v196\n\t"
      "v_mov_b32_e32 v220, v200\n\t"
      "v_mov_b32_e64 v221, v204\n\t"
      "s_nop 1\n\t"
      "v_mfma_f32_16x16x32_bf16 a[4:7], a[160:163], v[164:167], a[4:7]\n\t"
      "v_permlane16_swap_b32_e32 v192, v218\n\t"
      "v_permlane16_swap_b32_e32 v196, v219\n\t"
      "v_permlane16_swap_b32_e32 v200, v220\n\t"
      "v_permlane16_swap_b32_e32 v204, v221\n\t"
      "s_nop 0\n\t"
      "s_nop 0\n\t"
      "v_mfma_f32_16x16x32_bf16 a[8:11], a[160:163], v[168:171], a[8:11]\n\t"
      "v_add_f32_e32 v192, v192, v218\n\t"
      "v_add_f32_e32 v196, v196, v219\n\t"
      "v_add_f32_e32 v200, v200, v220\n\t"
      "v_add_f32_e32 v204, v204, v221\n\t"
      "v_mov_b32_e64 v218, v192\n\t"
      "v_mfma_f32_16x16x32_bf16 a[12:15], a[160:163], v[172:175], a[12:15]\n\t"
      "s_add_u32 m0, s59, 35840\n\t"
      "s_nop 0\n\t"
      "s_nop 0\n\t"
      "buffer_load_dwordx4 v245, s[40:43], s73 offen lds\n\t"
      "v_mov_b32_e32 v219, v196\n\t"
      "v_mov_b32_e32 v220, v200\n\t"
      "v_mov_b32_e64 v221, v204\n\t"
      "s_nop 1\n\t"
      "v_permlane32_swap_b32_e32 v192, v218\n\t"
      "v_mfma_f32_16x16x32_bf16 a[16:19], a[164:167], v[160:163], a[16:19]\n\t"
      "v_permlane32_swap_b32_e32 v196, v219\n\t"
      "v_permlane32_swap_b32_e32 v200, v220\n\t"
      "v_permlane32_swap_b32_e32 v204, v221\n\t"
      "s_nop 0\n\t"
      "v_add_f32_e64 v192, v192, v218\n\t"
      "v_mfma_f32_16x16x32_bf16 a[20:23], a[164:167], v[164:167], a[20:23]\n\t"
      "v_add_f32_e32 v196, v196, v219\n\t"
      "v_add_f32_e32 v200, v200, v220\n\t"
      "v_add_f32_e32 v204, v204, v221\n\t"
      "v_rcp_f32_e32 v192, v192\n\t"
      "v_rcp_f32_e64 v196, v196\n\t"
      "v_mfma_f32_16x16x32_bf16 a[24:27], a[164:167], v[168:171], a[24:27]\n\t"
      "s_lshl_b32 s73, s74, 1\n\t"
      "s_add_u32 s73, s73, 5\n\t"
      "s_and_b32 s73, s73, 7\n\t"
      "s_lshl_b32 s73, s73, 16\n\t"
      "s_add_u32 m0, s59, 98304\n\t"
      "s_nop 0\n\t"
      "s_nop 0\n\t"
      "buffer_load_dwordx4 v244, s[40:43], s73 offen lds\n\t"
      "v_rcp_f32_e32 v200, v200\n\t"
      "v_rcp_f32_e64 v204, v204\n\t"
      "s_nop 0\n\t"
      "v_mfma_f32_16x16x32_bf16 a[28:31], a[164:167], v[172:175], a[28:31]\n\t"
      "v_mfma_f32_16x16x32_bf16 a[32:35], a[168:171], v[160:163], a[32:35]\n\t"
      "v_mfma_f32_16x16x32_bf16 a[36:39], a[168:171], v[164:167], a[36:39]\n\t"
      "s_add_u32 m0, s59, 99328\n\t"
      "s_nop 0\n\t"
      "s_nop 0\n\t"
      "buffer_load_dwordx4 v245, s[40:43], s73 offen lds\n\t"
      "v_mfma_f32_16x16x32_bf16 a[40:43], a[168:171], v[168:171], a[40:43]\n\t"
      "v_accvgpr_read_b32 v128, a0\n\t"
      "v_accvgpr_read_b32 v129, a1\n\t"
      "v_accvgpr_read_b32 v130, a2\n\t"
      "v_accvgpr_read_b32 v131, a3\n\t"
      "v_accvgpr_read_b32 v132, a16\n\t"
      "v_accvgpr_read_b32 v133, a17\n\t"
      "v_accvgpr_read_b32 v134, a18\n\t"
      "v_accvgpr_read_b32 v135, a19\n\t"
      "v_mfma_f32_16x16x32_bf16 a[44:47], a[168:171], v[172:175], a[44:47]\n\t"
      "v_mul_f32_e32 v128, v128, v192\n\t"
      "v_mul_f32_e32 v129, v129, v192\n\t"
      "v_mul_f32_e32 v130, v130, v192\n\t"
      "v_mul_f32_e32 v131, v131, v192\n\t"
      "v_mul_f32_e32 v132, v132, v192\n\t"
      "v_mul_f32_e32 v133, v133, v192\n\t"
      "v_mul_f32_e32 v134, v134, v192\n\t"
      "v_mul_f32_e32 v135, v135, v192\n\t"
      "v_mfma_f32_16x16x32_bf16 a[48:51], a[172:175], v[160:163], a[48:51]\n\t"
      "s_add_u32 s73, s73, 0x2000\n\t"
      "s_add_u32 m0, s59, 100352\n\t"
      "s_nop 0\n\t"
      "s_nop 0\n\t"
      "buffer_load_dwordx4 v244, s[40:43], s73 offen lds\n\t"
      "v_cvt_pk_bf16_f32 v64, v128, v129\n\t"
      "v_cvt_pk_bf16_f32 v65, v130, v131\n\t"
      "v_cvt_pk_bf16_f32 v66, v132, v133\n\t"
      "v_cvt_pk_bf16_f32 v67, v134, v135\n\t"
      "v_accvgpr_read_b32 v136, a4\n\t"
      "v_accvgpr_read_b32 v137, a5\n\t"
      "v_accvgpr_read_b32 v138, a6\n\t"
      "v_mfma_f32_16x16x32_bf16 a[52:55], a[172:175], v[164:167], a[52:55]\n\t"
      "v_accvgpr_read_b32 v139, a7\n\t"
      "v_accvgpr_read_b32 v140, a20\n\t"
      "v_accvgpr_read_b32 v141, a21\n\t"
      "v_accvgpr_read_b32 v142, a22\n\t"
      "v_accvgpr_read_b32 v143, a23\n\t"
      "v_mul_f32_e32 v136, v136, v196\n\t"
      "v_mul_f32_e32 v137, v137, v196\n\t"
      "v_mul_f32_e64 v138, v138, v196\n\t"
      "v_mfma_f32_16x16x32_bf16 a[56:59], a[172:175], v[168:171], a[56:59]\n\t"
      "v_mul_f32_e32 v139, v139, v196\n\t"
      "v_mul_f32_e32 v140, v140, v196\n\t"
      "v_mul_f32_e32 v141, v141, v196\n\t"
      "v_mul_f32_e32 v142, v142, v196\n\t"
      "v_mul_f32_e64 v143, v143, v196\n\t"
      "v_cvt_pk_bf16_f32 v72, v136, v137\n\t"
      "v_cvt_pk_bf16_f32 v73, v138, v139\n\t"
      "v_cvt_pk_bf16_f32 v74, v140, v141\n\t"
      "v_mfma_f32_16x16x32_bf16 a[60:63], a[172:175], v[172:175], a[60:63]\n\t"
      "s_add_u32 m0, s59, 101376\n\t"
      "s_nop 0\n\t"
      "s_nop 0\n\t"
      "buffer_load_dwordx4 v245, s[40:43], s73 offen lds\n\t"
      "v_cvt_pk_bf16_f32 v75, v142, v143\n\t"
      "buffer_store_dwordx4 v[64:67], v246, s[48:51], 0 offen sc1\n\t"
      "v_accvgpr_read_b32 v128, a8\n\t"
      "v_accvgpr_read_b32 v129, a9\n\t"
      "v_accvgpr_read_b32 v130, a10\n\t"
      "v_accvgpr_read_b32 v131, a11\n\t"
      "v_accvgpr_read_b32 v132, a24\n\t"
      "v_mfma_f32_16x16x32_bf16 a[64:67], a[176:179], v[160:163], a[64:67]\n\t"
      "v_accvgpr_read_b32 v133, a25\n\t"
      "v_accvgpr_read_b32 v134, a26\n\t"
      "v_accvgpr_read_b32 v135, a27\n\t"
      "v_mul_f32_e32 v128, v128, v200\n\t"
      "v_mul_f32_e32 v129, v129, v200\n\t"
      "v_mul_f32_e32 v130, v130, v200\n\t"
      "v_mul_f32_e32 v131, v131, v200\n\t"
      "v_mul_f32_e64 v132, v132, v200\n\t"
      "v_mfma_f32_16x16x32_bf16 a[68:71], a[176:179], v[164:167], a[68:71]\n\t"
      "v_mul_f32_e32 v133, v133, v200\n\t"
      "v_mul_f32_e32 v134, v134, v200\n\t"
      "v_mul_f32_e64 v135, v135, v200\n\t"
      "v_cvt_pk_bf16_f32 v80, v128, v129\n\t"
      "v_cvt_pk_bf16_f32 v81, v130, v131\n\t"
      "v_cvt_pk_bf16_f32 v82, v132, v133\n\t"
      "v_cvt_pk_bf16_f32 v83, v134, v135\n\t"
      "v_mfma_f32_16x16x32_bf16 a[72:75], a[176:179], v[168:171], a[72:75]\n\t"
      "s_lshl_b32 s73, s74, 1\n\t"
      "s_add_u32 s73, s73, 6\n\t"
      "s_and_b32 s73, s73, 7\n\t"
      "s_lshl_b32 s73, s73, 16\n\t"
      "s_add_u32 m0, s59, 49152\n\t"
      "s_nop 0\n\t"
      "s_nop 0\n\t"
      "buffer_load_dwordx4 v244, s[40:43], s73 offen lds\n\t"
      "v_accvgpr_read_b32 v136, a12\n\t"
      "v_accvgpr_read_b32 v137, a13\n\t"
      "v_accvgpr_read_b32 v138, a14\n\t"
      "v_accvgpr_read_b32 v139, a15\n\t"
      "v_accvgpr_read_b32 v140, a28\n\t"
      "v_accvgpr_read_b32 v141, a29\n\t"
      "v_accvgpr_read_b32 v142, a30\n\t"
      "v_accvgpr_read_b32 v143, a31\n\t"
      "v_mfma_f32_16x16x32_bf16 a[76:79], a[176:179], v[172:175], a[76:79]\n\t"
      "v_mul_f32_e32 v136, v136, v204\n\t"
      "v_mul_f32_e32 v137, v137, v204\n\t"
      "v_mul_f32_e32 v138, v138, v204\n\t"
      "v_mul_f32_e32 v139, v139, v204\n\t"
      "v_mul_f32_e32 v140, v140, v204\n\t"
      "v_mul_f32_e32 v141, v141, v204\n\t"
      "v_mul_f32_e32 v142, v142, v204\n\t"
      "v_mul_f32_e32 v143, v143, v204\n\t"
      "v_mfma_f32_16x16x32_bf16 a[80:83], a[180:183], v[160:163], a[80:83]\n\t"
      "v_cvt_pk_bf16_f32 v88, v136, v137\n\t"
      "v_cvt_pk_bf16_f32 v89, v138, v139\n\t"
      "v_cvt_pk_bf16_f32 v90, v140, v141\n\t"
      "v_cvt_pk_bf16_f32 v91, v142, v143\n\t"
      "buffer_store_dwordx4 v[72:75], v247, s[48:51], 0 offen sc1\n\t"
      "v_accvgpr_read_b32 v128, a32\n\t"
      "v_accvgpr_read_b32 v129, a33\n\t"
      "v_mfma_f32_16x16x32_bf16 a[84:87], a[180:183], v[164:167], a[84:87]\n\t"
      "s_add_u32 m0, s59, 50176\n\t"
      "s_nop 0\n\t"
      "s_nop 0\n\t"
      "buffer_load_dwordx4 v245, s[40:43], s73 offen lds\n\t"
      "v_accvgpr_read_b32 v130, a34\n\t"
      "v_accvgpr_read_b32 v131, a35\n\t"
      "v_accvgpr_read_b32 v132, a48\n\t"
      "v_accvgpr_read_b32 v133, a49\n\t"
      "v_accvgpr_read_b32 v134, a50\n\t"
      "v_accvgpr_read_b32 v135, a51\n\t"
      "v_mul_f32_e32 v128, v128, v192\n\t"
      "v_mul_f32_e32 v129, v129, v192\n\t"
      "v_mfma_f32_16x16x32_bf16 a[88:91], a[180:183], v[168:171], a[88:91]\n\t"
      "v_mul_f32_e32 v130, v130, v192\n\t"
      "v_mul_f32_e32 v131, v131, v192\n\t"
      "v_mul_f32_e32 v132, v132, v192\n\t"
      "v_mul_f32_e32 v133, v133, v192\n\t"
      "v_mul_f32_e32 v134, v134, v192\n\t"
      "v_mul_f32_e32 v135, v135, v192\n\t"
      "v_cvt_pk_bf16_f32 v68, v128, v129\n\t"
      "v_cvt_pk_bf16_f32 v69, v130, v131\n\t"
      "v_mfma_f32_16x16x32_bf16 a[92:95], a[180:183], v[172:175], a[92:95]\n\t"
      "v_cvt_pk_bf16_f32 v70, v132, v133\n\t"
      "v_cvt_pk_bf16_f32 v71, v134, v135\n\t"
      "v_accvgpr_read_b32 v136, a36\n\t"
      "v_accvgpr_read_b32 v137, a37\n\t"
      "v_accvgpr_read_b32 v138, a38\n\t"
      "v_accvgpr_read_b32 v139, a39\n\t"
      "v_accvgpr_read_b32 v140, a52\n\t"
      "v_mfma_f32_16x16x32_bf16 a[96:99], a[184:187], v[160:163], a[96:99]\n\t"
      "s_add_u32 s73, s73, 0x2000\n\t"
      "s_add_u32 m0, s59, 51200\n\t"
      "s_nop 0\n\t"
      "s_nop 0\n\t"
      "buffer_load_dwordx4 v244, s[40:43], s73 offen lds\n\t"
      "v_accvgpr_read_b32 v141, a53\n\t"
      "v_accvgpr_read_b32 v142, a54\n\t"
      "v_accvgpr_read_b32 v143, a55\n\t"
      "v_mul_f32_e32 v136, v136, v196\n\t"
      "v_mul_f32_e32 v137, v137, v196\n\t"
      "v_mul_f32_e32 v138, v138, v196\n\t"
      "v_mul_f32_e32 v139, v139, v196\n\t"
      "v_mul_f32_e64 v140, v140, v196\n\t"
      "v_mfma_f32_16x16x32_bf16 a[100:103], a[184:187], v[164:167], a[100:103]\n\t"
      "v_mul_f32_e32 v141, v141, v196\n\t"
      "v_mul_f32_e32 v142, v142, v196\n\t"
      "v_mul_f32_e64 v143, v143, v196\n\t"
      "v_cvt_pk_bf16_f32 v76, v136, v137\n\t"
      "v_cvt_pk_bf16_f32 v77, v138, v139\n\t"
      "v_cvt_pk_bf16_f32 v78, v140, v141\n\t"
      "v_cvt_pk_bf16_f32 v79, v142, v143\n\t"
      "v_mfma_f32_16x16x32_bf16 a[104:107], a[184:187], v[168:171], a[104:107]\n\t"
      "buffer_store_dwordx4 v[80:83], v248, s[48:51], 0 offen sc1\n\t"
      "v_accvgpr_read_b32 v128, a40\n\t"
      "v_accvgpr_read_b32 v129, a41\n\t"
      "v_accvgpr_read_b32 v130, a42\n\t"
      "v_accvgpr_read_b32 v131, a43\n\t"
      "v_accvgpr_read_b32 v132, a56\n\t"
      "v_accvgpr_read_b32 v133, a57\n\t"
      "v_accvgpr_read_b32 v134, a58\n\t"
      "v_mfma_f32_16x16x32_bf16 a[108:111], a[184:187], v[172:175], a[108:111]\n\t"
      "s_add_u32 m0, s59, 52224\n\t"
      "s_nop 0\n\t"
      "s_nop 0\n\t"
      "buffer_load_dwordx4 v245, s[40:43], s73 offen lds\n\t"
      "v_accvgpr_read_b32 v135, a59\n\t"
      "v_mul_f32_e32 v128, v128, v200\n\t"
      "v_mul_f32_e32 v129, v129, v200\n\t"
      "v_mul_f32_e32 v130, v130, v200\n\t"
      "v_mul_f32_e32 v131, v131, v200\n\t"
      "v_mul_f32_e32 v132, v132, v200\n\t"
      "v_mul_f32_e32 v133, v133, v200\n\t"
      "v_mul_f32_e64 v134, v134, v200\n\t"
      "v_mfma_f32_16x16x32_bf16 a[112:115], a[188:191], v[160:163], a[112:115]\n\t"
      "v_mul_f32_e64 v135, v135, v200\n\t"
      "v_cvt_pk_bf16_f32 v84, v128, v129\n\t"
      "v_cvt_pk_bf16_f32 v85, v130, v131\n\t"
      "v_cvt_pk_bf16_f32 v86, v132, v133\n\t"
      "v_cvt_pk_bf16_f32 v87, v134, v135\n\t"
      "v_accvgpr_read_b32 v136, a44\n\t"
      "v_accvgpr_read_b32 v137, a45\n\t"
      "v_mfma_f32_16x16x32_bf16 a[116:119], a[188:191], v[164:167], a[116:119]\n\t"
      "v_accvgpr_read_b32 v138, a46\n\t"
      "v_accvgpr_read_b32 v139, a47\n\t"
      "v_accvgpr_read_b32 v140, a60\n\t"
      "v_accvgpr_read_b32 v141, a61\n\t"
      "v_accvgpr_read_b32 v142, a62\n\t"
      "v_accvgpr_read_b32 v143, a63\n\t"
      "v_mul_f32_e32 v136, v136, v204\n\t"
      "v_mul_f32_e32 v137, v137, v204\n\t"
      "v_mfma_f32_16x16x32_bf16 a[120:123], a[188:191], v[168:171], a[120:123]\n\t"
      "v_mul_f32_e32 v138, v138, v204\n\t"
      "v_mul_f32_e32 v139, v139, v204\n\t"
      "v_mul_f32_e32 v140, v140, v204\n\t"
      "v_mul_f32_e32 v141, v141, v204\n\t"
      "v_mul_f32_e32 v142, v142, v204\n\t"
      "v_mul_f32_e32 v143, v143, v204\n\t"
      "v_cvt_pk_bf16_f32 v92, v136, v137\n\t"
      "v_cvt_pk_bf16_f32 v93, v138, v139\n\t"
      "v_mfma_f32_16x16x32_bf16 a[124:127], a[188:191], v[172:175], a[124:127]\n\t"
      "v_cvt_pk_bf16_f32 v94, v140, v141\n\t"
      "v_cvt_pk_bf16_f32 v95, v142, v143\n\t"
      "buffer_store_dwordx4 v[88:91], v249, s[48:51], 0 offen sc1\n\t"
      "v_accvgpr_read_b32 v128, a64\n\t"
      "v_accvgpr_read_b32 v129, a65\n\t"
      "v_accvgpr_read_b32 v130, a66\n\t"
      "v_accvgpr_read_b32 v131, a67\n\t"
      "v_accvgpr_read_b32 v132, a80\n\t"
      "v_accvgpr_read_b32 v133, a81\n\t"
      "v_accvgpr_read_b32 v134, a82\n\t"
      "v_accvgpr_read_b32 v135, a83\n\t"
      "v_mul_f32_e32 v128, v128, v192\n\t"
      "v_mul_f32_e32 v129, v129, v192\n\t"
      "v_mul_f32_e32 v130, v130, v192\n\t"
      "v_mul_f32_e32 v131, v131, v192\n\t"
      "v_mul_f32_e32 v132, v132, v192\n\t"
      "v_mul_f32_e32 v133, v133, v192\n\t"
      "v_mul_f32_e32 v134, v134, v192\n\t"
      "v_mul_f32_e32 v135, v135, v192\n\t"
      "v_cvt_pk_bf16_f32 v96, v128, v129\n\t"
      "v_cvt_pk_bf16_f32 v97, v130, v131\n\t"
      "v_cvt_pk_bf16_f32 v98, v132, v133\n\t"
      "v_cvt_pk_bf16_f32 v99, v134, v135\n\t"
      "v_accvgpr_read_b32 v136, a68\n\t"
      "v_accvgpr_read_b32 v137, a69\n\t"
      "v_accvgpr_read_b32 v138, a70\n\t"
      "v_accvgpr_read_b32 v139, a71\n\t"
      "v_accvgpr_read_b32 v140, a84\n\t"
      "v_accvgpr_read_b32 v141, a85\n\t"
      "v_accvgpr_read_b32 v142, a86\n\t"
      "v_accvgpr_read_b32 v143, a87\n\t"
      "v_mul_f32_e32 v136, v136, v196\n\t"
      "v_mul_f32_e32 v137, v137, v196\n\t"
      "v_mul_f32_e32 v138, v138, v196\n\t"
      "v_mul_f32_e32 v139, v139, v196\n\t"
      "v_mul_f32_e32 v140, v140, v196\n\t"
      "v_mul_f32_e32 v141, v141, v196\n\t"
      "v_mul_f32_e32 v142, v142, v196\n\t"
      "v_mul_f32_e32 v143, v143, v196\n\t"
      "v_cvt_pk_bf16_f32 v104, v136, v137\n\t"
      "v_cvt_pk_bf16_f32 v105, v138, v139\n\t"
      "v_cvt_pk_bf16_f32 v106, v140, v141\n\t"
      "v_cvt_pk_bf16_f32 v107, v142, v143\n\t"
      "buffer_store_dwordx4 v[68:71], v246, s[48:51], 0 offen offset:1024 sc1\n\t"
      "v_accvgpr_read_b32 v128, a72\n\t"
      "v_accvgpr_read_b32 v129, a73\n\t"
      "v_accvgpr_read_b32 v130, a74\n\t"
      "v_accvgpr_read_b32 v131, a75\n\t"
      "v_accvgpr_read_b32 v132, a88\n\t"
      "v_accvgpr_read_b32 v133, a89\n\t"
      "v_accvgpr_read_b32 v134, a90\n\t"
      "v_accvgpr_read_b32 v135, a91\n\t"
      "v_mul_f32_e32 v128, v128, v200\n\t"
      "v_mul_f32_e32 v129, v129, v200\n\t"
      "v_mul_f32_e32 v130, v130, v200\n\t"
      "v_mul_f32_e32 v131, v131, v200\n\t"
      "v_mul_f32_e32 v132, v132, v200\n\t"
      "v_mul_f32_e32 v133, v133, v200\n\t"
      "v_mul_f32_e32 v134, v134, v200\n\t"
      "v_mul_f32_e32 v135, v135, v200\n\t"
      "v_cvt_pk_bf16_f32 v112, v128, v129\n\t"
      "v_cvt_pk_bf16_f32 v113, v130, v131\n\t"
      "v_cvt_pk_bf16_f32 v114, v132, v133\n\t"
      "v_cvt_pk_bf16_f32 v115, v134, v135\n\t"
      "v_accvgpr_read_b32 v136, a76\n\t"
      "v_accvgpr_read_b32 v137, a77\n\t"
      "v_accvgpr_read_b32 v138, a78\n\t"
      "v_accvgpr_read_b32 v139, a79\n\t"
      "v_accvgpr_read_b32 v140, a92\n\t"
      "v_accvgpr_read_b32 v141, a93\n\t"
      "v_accvgpr_read_b32 v142, a94\n\t"
      "v_accvgpr_read_b32 v143, a95\n\t"
      "v_mul_f32_e32 v136, v136, v204\n\t"
      "v_mul_f32_e32 v137, v137, v204\n\t"
      "v_mul_f32_e32 v138, v138, v204\n\t"
      "v_mul_f32_e32 v139, v139, v204\n\t"
      "v_mul_f32_e32 v140, v140, v204\n\t"
      "v_mul_f32_e32 v141, v141, v204\n\t"
      "v_mul_f32_e32 v142, v142, v204\n\t"
      "v_mul_f32_e32 v143, v143, v204\n\t"
      "v_cvt_pk_bf16_f32 v120, v136, v137\n\t"
      "v_cvt_pk_bf16_f32 v121, v138, v139\n\t"
      "v_cvt_pk_bf16_f32 v122, v140, v141\n\t"
      "v_cvt_pk_bf16_f32 v123, v142, v143\n\t"
      "buffer_store_dwordx4 v[76:79], v247, s[48:51], 0 offen offset:1024 sc1\n\t"
      "v_accvgpr_read_b32 v128, a96\n\t"
      "v_accvgpr_read_b32 v129, a97\n\t"
      "v_accvgpr_read_b32 v130, a98\n\t"
      "v_accvgpr_read_b32 v131, a99\n\t"
      "v_accvgpr_read_b32 v132, a112\n\t"
      "v_accvgpr_read_b32 v133, a113\n\t"
      "v_accvgpr_read_b32 v134, a114\n\t"
      "v_accvgpr_read_b32 v135, a115\n\t"
      "v_mul_f32_e32 v128, v128, v192\n\t"
      "v_mul_f32_e32 v129, v129, v192\n\t"
      "v_mul_f32_e32 v130, v130, v192\n\t"
      "v_mul_f32_e32 v131, v131, v192\n\t"
      "v_mul_f32_e32 v132, v132, v192\n\t"
      "v_mul_f32_e32 v133, v133, v192\n\t"
      "v_mul_f32_e32 v134, v134, v192\n\t"
      "v_mul_f32_e32 v135, v135, v192\n\t"
      "v_cvt_pk_bf16_f32 v100, v128, v129\n\t"
      "v_cvt_pk_bf16_f32 v101, v130, v131\n\t"
      "v_cvt_pk_bf16_f32 v102, v132, v133\n\t"
      "v_cvt_pk_bf16_f32 v103, v134, v135\n\t"
      "v_accvgpr_read_b32 v136, a100\n\t"
      "v_accvgpr_read_b32 v137, a101\n\t"
      "v_accvgpr_read_b32 v138, a102\n\t"
      "v_accvgpr_read_b32 v139, a103\n\t"
      "v_accvgpr_read_b32 v140, a116\n\t"
      "v_accvgpr_read_b32 v141, a117\n\t"
      "v_accvgpr_read_b32 v142, a118\n\t"
      "v_accvgpr_read_b32 v143, a119\n\t"
      "v_mul_f32_e32 v136, v136, v196\n\t"
      "v_mul_f32_e32 v137, v137, v196\n\t"
      "v_mul_f32_e32 v138, v138, v196\n\t"
      "v_mul_f32_e32 v139, v139, v196\n\t"
      "v_mul_f32_e32 v140, v140, v196\n\t"
      "v_mul_f32_e32 v141, v141, v196\n\t"
      "v_mul_f32_e32 v142, v142, v196\n\t"
      "v_mul_f32_e32 v143, v143, v196\n\t"
      "v_cvt_pk_bf16_f32 v108, v136, v137\n\t"
      "v_cvt_pk_bf16_f32 v109, v138, v139\n\t"
      "v_cvt_pk_bf16_f32 v110, v140, v141\n\t"
      "v_cvt_pk_bf16_f32 v111, v142, v143\n\t"
      "buffer_store_dwordx4 v[84:87], v248, s[48:51], 0 offen offset:1024 sc1\n\t"
      "v_accvgpr_read_b32 v128, a104\n\t"
      "v_accvgpr_read_b32 v129, a105\n\t"
      "v_accvgpr_read_b32 v130, a106\n\t"
      "v_accvgpr_read_b32 v131, a107\n\t"
      "v_accvgpr_read_b32 v132, a120\n\t"
      "v_accvgpr_read_b32 v133, a121\n\t"
      "v_accvgpr_read_b32 v134, a122\n\t"
      "v_accvgpr_read_b32 v135, a123\n\t"
      "v_mul_f32_e32 v128, v128, v200\n\t"
      "v_mul_f32_e32 v129, v129, v200\n\t"
      "v_mul_f32_e32 v130, v130, v200\n\t"
      "v_mul_f32_e32 v131, v131, v200\n\t"
      "v_mul_f32_e32 v132, v132, v200\n\t"
      "v_mul_f32_e32 v133, v133, v200\n\t"
      "v_mul_f32_e32 v134, v134, v200\n\t"
      "v_mul_f32_e32 v135, v135, v200\n\t"
      "v_cvt_pk_bf16_f32 v116, v128, v129\n\t"
      "v_cvt_pk_bf16_f32 v117, v130, v131\n\t"
      "v_cvt_pk_bf16_f32 v118, v132, v133\n\t"
      "v_cvt_pk_bf16_f32 v119, v134, v135\n\t"
      "v_accvgpr_read_b32 v136, a108\n\t"
      "v_accvgpr_read_b32 v137, a109\n\t"
      "v_accvgpr_read_b32 v138, a110\n\t"
      "v_accvgpr_read_b32 v139, a111\n\t"
      "v_accvgpr_read_b32 v140, a124\n\t"
      "v_accvgpr_read_b32 v141, a125\n\t"
      "v_accvgpr_read_b32 v142, a126\n\t"
      "v_accvgpr_read_b32 v143, a127\n\t"
      "v_mul_f32_e32 v136, v136, v204\n\t"
      "v_mul_f32_e32 v137, v137, v204\n\t"
      "v_mul_f32_e32 v138, v138, v204\n\t"
      "v_mul_f32_e32 v139, v139, v204\n\t"
      "v_mul_f32_e32 v140, v140, v204\n\t"
      "v_mul_f32_e32 v141, v141, v204\n\t"
      "v_mul_f32_e32 v142, v142, v204\n\t"
      "v_mul_f32_e32 v143, v143, v204\n\t"
      "v_cvt_pk_bf16_f32 v124, v136, v137\n\t"
      "v_cvt_pk_bf16_f32 v125, v138, v139\n\t"
      "v_cvt_pk_bf16_f32 v126, v140, v141\n\t"
      "v_cvt_pk_bf16_f32 v127, v142, v143\n\t"
      "buffer_store_dwordx4 v[92:95], v249, s[48:51], 0 offen offset:1024 sc1\n\t"
      "s_waitcnt vmcnt(32) lgkmcnt(0)\n\t"
      "s_barrier\n\t"
      "s_lshl_b32 s73, s74, 1\n\t"
      "s_add_u32 s73, s73, 7\n\t"
      "s_and_b32 s73, s73, 7\n\t"
      "s_lshl_b32 s73, s73, 16\n\t"
      "s_add_u32 m0, s59, 114688\n\t"
      "s_nop 0\n\t"
      "s_nop 0\n\t"
      "buffer_load_dwordx4 v244, s[40:43], s73 offen lds\n\t"
      "s_add_u32 m0, s59, 115712\n\t"
      "s_nop 0\n\t"
      "s_nop 0\n\t"
      "buffer_load_dwordx4 v245, s[40:43], s73 offen lds\n\t"
      "s_add_u32 s73, s73, 0x2000\n\t"
      "s_add_u32 m0, s59, 116736\n\t"
      "s_nop 0\n\t"
      "s_nop 0\n\t"
      "buffer_load_dwordx4 v244, s[40:43], s73 offen lds\n\t"
      "s_add_u32 m0, s59, 117760\n\t"
      "s_nop 0\n\t"
      "s_nop 0\n\t"
      "buffer_load_dwordx4 v245, s[40:43], s73 offen lds\n\t"
      "ds_read_b64_tr_b16 v[0:1], v242 offset:0\n\t"
      "ds_read_b64_tr_b16 v[2:3], v242 offset:4096\n\t"
      "ds_read_b64_tr_b16 v[4:5], v242 offset:8\n\t"
      "ds_read_b64_tr_b16 v[6:7], v242 offset:4104\n\t"
      "ds_read_b64_tr_b16 v[8:9], v242 offset:512\n\t"
      "ds_read_b64_tr_b16 v[10:11], v242 offset:4608\n\t"
      "ds_read_b64_tr_b16 v[12:13], v242 offset:520\n\t"
      "ds_read_b64_tr_b16 v[14:15], v242 offset:4616\n\t"
      "ds_read_b64_tr_b16 v[16:17], v242 offset:1024\n\t"
      "ds_read_b64_tr_b16 v[18:19], v242 offset:5120\n\t"
      "ds_read_b64_tr_b16 v[20:21], v242 offset:1032\n\t"
      "ds_read_b64_tr_b16 v[22:23], v242 offset:5128\n\t"
      "ds_read_b64_tr_b16 v[24:25], v242 offset:1536\n\t"
      "ds_read_b64_tr_b16 v[26:27], v242 offset:5632\n\t"
      "ds_read_b64_tr_b16 v[28:29], v242 offset:1544\n\t"
      "ds_read_b64_tr_b16 v[30:31], v242 offset:5640\n\t"
      "ds_read_b64_tr_b16 v[32:33], v242 offset:8192\n\t"
      "ds_read_b64_tr_b16 v[34:35], v242 offset:12288\n\t"
      "ds_read_b64_tr_b16 v[36:37], v242 offset:8200\n\t"
      "ds_read_b64_tr_b16 v[38:39], v242 offset:12296\n\t"
      "ds_read_b64_tr_b16 v[40:41], v242 offset:8704\n\t"
      "ds_read_b64_tr_b16 v[42:43], v242 offset:12800\n\t"
      "ds_read_b64_tr_b16 v[44:45], v242 offset:8712\n\t"
      "ds_read_b64_tr_b16 v[46:47], v242 offset:12808\n\t"
      "ds_read_b64_tr_b16 v[48:49], v242 offset:9216\n\t"
      "ds_read_b64_tr_b16 v[50:51], v242 offset:13312\n\t"
      "ds_read_b64_tr_b16 v[52:53], v242 offset:9224\n\t"
      "ds_read_b64_tr_b16 v[54:55], v242 offset:13320\n\t"
      "ds_read_b64_tr_b16 v[56:57], v242 offset:9728\n\t"
      "ds_read_b64_tr_b16 v[58:59], v242 offset:13824\n\t"
      "ds_read_b64_tr_b16 v[60:61], v242 offset:9736\n\t"
      "ds_read_b64_tr_b16 v[62:63], v242 offset:13832\n\t"
      "s_waitcnt lgkmcnt(15)\n\t"
      "s_nop 0\n\t"
      "v_mfma_f32_16x16x32_bf16 a[128:131], v[0:3], v[64:67], 0\n\t"
      "v_mfma_f32_16x16x32_bf16 a[132:135], v[0:3], v[72:75], 0\n\t"
      "v_mfma_f32_16x16x32_bf16 a[136:139], v[0:3], v[80:83], 0\n\t"
      "s_waitcnt vmcnt(32)\n\t"
      "s_nop 0\n\t"
      "v_mfma_f32_16x16x32_bf16 a[140:143], v[0:3], v[88:91], 0\n\t"
      "s_barrier\n\t"
      "s_nop 0\n\t"
      "v_mfma_f32_16x16x32_bf16 a[144:147], v[4:7], v[64:67], 0\n\t"
      "ds_read_b64_tr_b16 v[0:1], v243 offset:0\n\t"
      "v_mfma_f32_16x16x32_bf16 a[148:151], v[4:7], v[72:75], 0\n\t"
      "buffer_store_dwordx4 v[96:99], v246, s[48:51], 0 offen offset:2048 sc1\n\t"
      "v_mfma_f32_16x16x32_bf16 a[152:155], v[4:7], v[80:83], 0\n\t"
      "ds_read_b64_tr_b16 v[2:3], v243 offset:4096\n\t"
      "v_mfma_f32_16x16x32_bf16 a[156:159], v[4:7], v[88:91], 0\n\t"
      "v_mfma_f32_16x16x32_bf16 a[160:163], v[8:11], v[64:67], 0\n\t"
      "ds_read_b64_tr_b16 v[4:5], v243 offset:8\n\t"
      "v_mfma_f32_16x16x32_bf16 a[164:167], v[8:11], v[72:75], 0\n\t"
      "v_mfma_f32_16x16x32_bf16 a[168:171], v[8:11], v[80:83], 0\n\t"
      "ds_read_b64_tr_b16 v[6:7], v243 offset:4104\n\t"
      "v_mfma_f32_16x16x32_bf16 a[172:175], v[8:11], v[88:91], 0\n\t"
      "v_mfma_f32_16x16x32_bf16 a[176:179], v[12:15], v[64:67], 0\n\t"
      "ds_read_b64_tr_b16 v[8:9], v243 offset:512\n\t"
      "v_mfma_f32_16x16x32_bf16 a[180:183], v[12:15], v[72:75], 0\n\t"
      "v_mfma_f32_16x16x32_bf16 a[184:187], v[12:15], v[80:83], 0\n\t"
      "ds_read_b64_tr_b16 v[10:11], v243 offset:4608\n\t"
      "v_mfma_f32_16x16x32_bf16 a[188:191], v[12:15], v[88:91], 0\n\t"
      "s_waitcnt lgkmcnt(15)\n\t"
      "s_nop 0\n\t"
      "v_mfma_f32_16x16x32_bf16 a[192:195], v[16:19], v[64:67], 0\n\t"
      "ds_read_b64_tr_b16 v[12:13], v243 offset:520\n\t"
      "v_mfma_f32_16x16x32_bf16 a[196:199], v[16:19], v[72:75], 0\n\t"
      "v_mfma_f32_16x16x32_bf16 a[200:203], v[16:19], v[80:83], 0\n\t"
      "ds_read_b64_tr_b16 v[14:15], v243 offset:4616\n\t"
      "v_mfma_f32_16x16x32_bf16 a[204:207], v[16:19], v[88:91], 0\n\t"
      "buffer_store_dwordx4 v[104:107], v247, s[48:51], 0 offen offset:2048 sc1\n\t"
      "v_mfma_f32_16x16x32_bf16 a[208:211], v[20:23], v[64:67], 0\n\t"
      "ds_read_b64_tr_b16 v[16:17], v243 offset:1024\n\t"
      "v_mfma_f32_16x16x32_bf16 a[212:215], v[20:23], v[72:75], 0\n\t"
      "v_mfma_f32_16x16x32_bf16 a[216:219], v[20:23], v[80:83], 0\n\t"
      "ds_read_b64_tr_b16 v[18:19], v243 offset:5120\n\t"
      "v_mfma_f32_16x16x32_bf16 a[220:223], v[20:23], v[88:91], 0\n\t"
      "v_mfma_f32_16x16x32_bf16 a[224:227], v[24:27], v[64:67], 0\n\t"
      "ds_read_b64_tr_b16 v[20:21], v243 offset:1032\n\t"
      "v_mfma_f32_16x16x32_bf16 a[228:231], v[24:27], v[72:75], 0\n\t"
      "v_mfma_f32_16x16x32_bf16 a[232:235], v[24:27], v[80:83], 0\n\t"
      "ds_read_b64_tr_b16 v[22:23], v243 offset:5128\n\t"
      "v_mfma_f32_16x16x32_bf16 a[236:239], v[24:27], v[88:91], 0\n\t"
      "v_mfma_f32_16x16x32_bf16 a[240:243], v[28:31], v[64:67], 0\n\t"
      "ds_read_b64_tr_b16 v[24:25], v243 offset:1536\n\t"
      "v_mfma_f32_16x16x32_bf16 a[244:247], v[28:31], v[72:75], 0\n\t"
      "v_mfma_f32_16x16x32_bf16 a[248:251], v[28:31], v[80:83], 0\n\t"
      "ds_read_b64_tr_b16 v[26:27], v243 offset:5632\n\t"
      "v_mfma_f32_16x16x32_bf16 a[252:255], v[28:31], v[88:91], 0\n\t"
      "s_waitcnt lgkmcnt(15)\n\t"
      "s_nop 0\n\t"
      "v_mfma_f32_16x16x32_bf16 a[128:131], v[32:35], v[68:71], a[128:131]\n\t"
      "ds_read_b64_tr_b16 v[28:29], v243 offset:1544\n\t"
      "v_mfma_f32_16x16x32_bf16 a[132:135], v[32:35], v[76:79], a[132:135]\n\t"
      "buffer_store_dwordx4 v[112:115], v248, s[48:51], 0 offen offset:2048 sc1\n\t"
      "v_mfma_f32_16x16x32_bf16 a[136:139], v[32:35], v[84:87], a[136:139]\n\t"
      "ds_read_b64_tr_b16 v[30:31], v243 offset:5640\n\t"
      "v_mfma_f32_16x16x32_bf16 a[140:143], v[32:35], v[92:95], a[140:143]\n\t"
      "v_mfma_f32_16x16x32_bf16 a[144:147], v[36:39], v[68:71], a[144:147]\n\t"
      "ds_read_b64_tr_b16 v[32:33], v243 offset:8192\n\t"
      "v_mfma_f32_16x16x32_bf16 a[148:151], v[36:39], v[76:79], a[148:151]\n\t"
      "v_mfma_f32_16x16x32_bf16 a[152:155], v[36:39], v[84:87], a[152:155]\n\t"
      "ds_read_b64_tr_b16 v[34:35], v243 offset:12288\n\t"
      "v_mfma_f32_16x16x32_bf16 a[156:159], v[36:39], v[92:95], a[156:159]\n\t"
      "v_mfma_f32_16x16x32_bf16 a[160:163], v[40:43], v[68:71], a[160:163]\n\t"
      "ds_read_b64_tr_b16 v[36:37], v243 offset:8200\n\t"
      "v_mfma_f32_16x16x32_bf16 a[164:167], v[40:43], v[76:79], a[164:167]\n\t"
      "v_mfma_f32_16x16x32_bf16 a[168:171], v[40:43], v[84:87], a[168:171]\n\t"
      "ds_read_b64_tr_b16 v[38:39], v243 offset:12296\n\t"
      "v_mfma_f32_16x16x32_bf16 a[172:175], v[40:43], v[92:95], a[172:175]\n\t"
      "v_mfma_f32_16x16x32_bf16 a[176:179], v[44:47], v[68:71], a[176:179]\n\t"
      "ds_read_b64_tr_b16 v[40:41], v243 offset:8704\n\t"
      "v_mfma_f32_16x16x32_bf16 a[180:183], v[44:47], v[76:79], a[180:183]\n\t"
      "v_mfma_f32_16x16x32_bf16 a[184:187], v[44:47], v[84:87], a[184:187]\n\t"
      "ds_read_b64_tr_b16 v[42:43], v243 offset:12800\n\t"
      "v_mfma_f32_16x16x32_bf16 a[188:191], v[44:47], v[92:95], a[188:191]\n\t"
      "buffer_store_dwordx4 v[120:123], v249, s[48:51], 0 offen offset:2048 sc1\n\t"
      "s_waitcnt lgkmcnt(15)\n\t"
      "s_nop 0\n\t"
      "v_mfma_f32_16x16x32_bf16 a[192:195], v[48:51], v[68:71], a[192:195]\n\t"
      "ds_read_b64_tr_b16 v[44:45], v243 offset:8712\n\t"
      "v_mfma_f32_16x16x32_bf16 a[196:199], v[48:51], v[76:79], a[196:199]\n\t"
      "v_mfma_f32_16x16x32_bf16 a[200:203], v[48:51], v[84:87], a[200:203]\n\t"
      "ds_read_b64_tr_b16 v[46:47], v243 offset:12808\n\t"
      "v_mfma_f32_16x16x32_bf16 a[204:207], v[48:51], v[92:95], a[204:207]\n\t"
      "v_mfma_f32_16x16x32_bf16 a[208:211], v[52:55], v[68:71], a[208:211]\n\t"
      "ds_read_b64_tr_b16 v[48:49], v243 offset:9216\n\t"
      "v_mfma_f32_16x16x32_bf16 a[212:215], v[52:55], v[76:79], a[212:215]\n\t"
      "v_mfma_f32_16x16x32_bf16 a[216:219], v[52:55], v[84:87], a[216:219]\n\t"
      "ds_read_b64_tr_b16 v[50:51], v243 offset:13312\n\t"
      "v_mfma_f32_16x16x32_bf16 a[220:223], v[52:55], v[92:95], a[220:223]\n\t"
      "v_mfma_f32_16x16x32_bf16 a[224:227], v[56:59], v[68:71], a[224:227]\n\t"
      "ds_read_b64_tr_b16 v[52:53], v243 offset:9224\n\t"
      "v_mfma_f32_16x16x32_bf16 a[228:231], v[56:59], v[76:79], a[228:231]\n\t"
      "v_mfma_f32_16x16x32_bf16 a[232:235], v[56:59], v[84:87], a[232:235]\n\t"
      "ds_read_b64_tr_b16 v[54:55], v243 offset:13320\n\t"
      "v_mfma_f32_16x16x32_bf16 a[236:239], v[56:59], v[92:95], a[236:239]\n\t"
      "v_mfma_f32_16x16x32_bf16 a[240:243], v[60:63], v[68:71], a[240:243]\n\t"
      "ds_read_b64_tr_b16 v[56:57], v243 offset:9728\n\t"
      "v_mfma_f32_16x16x32_bf16 a[244:247], v[60:63], v[76:79], a[244:247]\n\t"
      "buffer_store_dwordx4 v[100:103], v246, s[48:51], 0 offen offset:3072 sc1\n\t"
      "v_mfma_f32_16x16x32_bf16 a[248:251], v[60:63], v[84:87], a[248:251]\n\t"
      "ds_read_b64_tr_b16 v[58:59], v243 offset:13824\n\t"
      "v_mfma_f32_16x16x32_bf16 a[252:255], v[60:63], v[92:95], a[252:255]\n\t"
      "s_waitcnt lgkmcnt(15)\n\t"
      "s_nop 0\n\t"
      "v_mfma_f32_16x16x32_bf16 a[128:131], v[0:3], v[96:99], a[128:131]\n\t"
      "v_mfma_f32_16x16x32_bf16 a[132:135], v[0:3], v[104:107], a[132:135]\n\t"
      "v_mfma_f32_16x16x32_bf16 a[136:139], v[0:3], v[112:115], a[136:139]\n\t"
      "s_waitcnt vmcnt(33)\n\t"
      "s_nop 0\n\t"
      "v_mfma_f32_16x16x32_bf16 a[140:143], v[0:3], v[120:123], a[140:143]\n\t"
      "s_barrier\n\t"
      "s_nop 0\n\t"
      "v_mfma_f32_16x16x32_bf16 a[144:147], v[4:7], v[96:99], a[144:147]\n\t"
      "ds_read_b64_tr_b16 v[0:1], v242 offset:16384\n\t"
      "ds_read_b64_tr_b16 v[60:61], v243 offset:9736\n\t"
      "v_mfma_f32_16x16x32_bf16 a[148:151], v[4:7], v[104:107], a[148:151]\n\t"
      "ds_read_b64_tr_b16 v[62:63], v243 offset:13832\n\t"
      "v_mfma_f32_16x16x32_bf16 a[152:155], v[4:7], v[112:115], a[152:155]\n\t"
      "ds_read_b64_tr_b16 v[2:3], v242 offset:20480\n\t"
      "v_mfma_f32_16x16x32_bf16 a[156:159], v[4:7], v[120:123], a[156:159]\n\t"
      "v_mfma_f32_16x16x32_bf16 a[160:163], v[8:11], v[96:99], a[160:163]\n\t"
      "ds_read_b64_tr_b16 v[4:5], v242 offset:16392\n\t"
      "v_mfma_f32_16x16x32_bf16 a[164:167], v[8:11], v[104:107], a[164:167]\n\t"
      "v_mfma_f32_16x16x32_bf16 a[168:171], v[8:11], v[112:115], a[168:171]\n\t"
      "ds_read_b64_tr_b16 v[6:7], v242 offset:20488\n\t"
      "v_mfma_f32_16x16x32_bf16 a[172:175], v[8:11], v[120:123], a[172:175]\n\t"
      "buffer_store_dwordx4 v[108:111], v247, s[48:51], 0 offen offset:3072 sc1\n\t"
      "v_mfma_f32_16x16x32_bf16 a[176:179], v[12:15], v[96:99], a[176:179]\n\t"
      "ds_read_b64_tr_b16 v[8:9], v242 offset:16896\n\t"
      "v_mfma_f32_16x16x32_bf16 a[180:183], v[12:15], v[104:107], a[180:183]\n\t"
      "v_mfma_f32_16x16x32_bf16 a[184:187], v[12:15], v[112:115], a[184:187]\n\t"
      "ds_read_b64_tr_b16 v[10:11], v242 offset:20992\n\t"
      "v_mfma_f32_16x16x32_bf16 a[188:191], v[12:15], v[120:123], a[188:191]\n\t"
      "s_waitcnt lgkmcnt(15)\n\t"
      "s_nop 0\n\t"
      "v_mfma_f32_16x16x32_bf16 a[192:195], v[16:19], v[96:99], a[192:195]\n\t"
      "ds_read_b64_tr_b16 v[12:13], v242 offset:16904\n\t"
      "v_mfma_f32_16x16x32_bf16 a[196:199], v[16:19], v[104:107], a[196:199]\n\t"
      "v_mfma_f32_16x16x32_bf16 a[200:203], v[16:19], v[112:115], a[200:203]\n\t"
      "ds_read_b64_tr_b16 v[14:15], v242 offset:21000\n\t"
      "v_mfma_f32_16x16x32_bf16 a[204:207], v[16:19], v[120:123], a[204:207]\n\t"
      "v_mfma_f32_16x16x32_bf16 a[208:211], v[20:23], v[96:99], a[208:211]\n\t"
      "ds_read_b64_tr_b16 v[16:17], v242 offset:17408\n\t"
      "v_mfma_f32_16x16x32_bf16 a[212:215], v[20:23], v[104:107], a[212:215]\n\t"
      "v_mfma_f32_16x16x32_bf16 a[216:219], v[20:23], v[112:115], a[216:219]\n\t"
      "ds_read_b64_tr_b16 v[18:19], v242 offset:21504\n\t"
      "v_mfma_f32_16x16x32_bf16 a[220:223], v[20:23], v[120:123], a[220:223]\n\t"
      "v_mfma_f32_16x16x32_bf16 a[224:227], v[24:27], v[96:99], a[224:227]\n\t"
      "ds_read_b64_tr_b16 v[20:21], v242 offset:17416\n\t"
      "v_mfma_f32_16x16x32_bf16 a[228:231], v[24:27], v[104:107], a[228:231]\n\t"
      "buffer_store_dwordx4 v[116:119], v248, s[48:51], 0 offen offset:3072 sc1\n\t"
      "v_mfma_f32_16x16x32_bf16 a[232:235], v[24:27], v[112:115], a[232:235]\n\t"
      "ds_read_b64_tr_b16 v[22:23], v242 offset:21512\n\t"
      "v_mfma_f32_16x16x32_bf16 a[236:239], v[24:27], v[120:123], a[236:239]\n\t"
      "v_mfma_f32_16x16x32_bf16 a[240:243], v[28:31], v[96:99], a[240:243]\n\t"
      "ds_read_b64_tr_b16 v[24:25], v242 offset:17920\n\t"
      "v_mfma_f32_16x16x32_bf16 a[244:247], v[28:31], v[104:107], a[244:247]\n\t"
      "v_mfma_f32_16x16x32_bf16 a[248:251], v[28:31], v[112:115], a[248:251]\n\t"
      "ds_read_b64_tr_b16 v[26:27], v242 offset:22016\n\t"
      "v_mfma_f32_16x16x32_bf16 a[252:255], v[28:31], v[120:123], a[252:255]\n\t"
      "s_waitcnt lgkmcnt(15)\n\t"
      "s_nop 0\n\t"
      "v_mfma_f32_16x16x32_bf16 a[128:131], v[32:35], v[100:103], a[128:131]\n\t"
      "ds_read_b64_tr_b16 v[28:29], v242 offset:17928\n\t"
      "v_mfma_f32_16x16x32_bf16 a[132:135], v[32:35], v[108:111], a[132:135]\n\t"
      "v_mfma_f32_16x16x32_bf16 a[136:139], v[32:35], v[116:119], a[136:139]\n\t"
      "ds_read_b64_tr_b16 v[30:31], v242 offset:22024\n\t"
      "v_mfma_f32_16x16x32_bf16 a[140:143], v[32:35], v[124:127], a[140:143]\n\t"
      "v_mfma_f32_16x16x32_bf16 a[144:147], v[36:39], v[100:103], a[144:147]\n\t"
      "ds_read_b64_tr_b16 v[32:33], v242 offset:24576\n\t"
      "v_mfma_f32_16x16x32_bf16 a[148:151], v[36:39], v[108:111], a[148:151]\n\t"
      "v_mfma_f32_16x16x32_bf16 a[152:155], v[36:39], v[116:119], a[152:155]\n\t"
      "ds_read_b64_tr_b16 v[34:35], v242 offset:28672\n\t"
      "v_mfma_f32_16x16x32_bf16 a[156:159], v[36:39], v[124:127], a[156:159]\n\t"
      "buffer_store_dwordx4 v[124:127], v249, s[48:51], 0 offen offset:3072 sc1\n\t"
      "v_mfma_f32_16x16x32_bf16 a[160:163], v[40:43], v[100:103], a[160:163]\n\t"
      "ds_read_b64_tr_b16 v[36:37], v242 offset:24584\n\t"
      "v_mfma_f32_16x16x32_bf16 a[164:167], v[40:43], v[108:111], a[164:167]\n\t"
      "v_mfma_f32_16x16x32_bf16 a[168:171], v[40:43], v[116:119], a[168:171]\n\t"
      "ds_read_b64_tr_b16 v[38:39], v242 offset:28680\n\t"
      "v_mfma_f32_16x16x32_bf16 a[172:175], v[40:43], v[124:127], a[172:175]\n\t"
      "v_mfma_f32_16x16x32_bf16 a[176:179], v[44:47], v[100:103], a[176:179]\n\t"
      "ds_read_b64_tr_b16 v[40:41], v242 offset:25088\n\t"
      "v_mfma_f32_16x16x32_bf16 a[180:183], v[44:47], v[108:111], a[180:183]\n\t"
      "v_mfma_f32_16x16x32_bf16 a[184:187], v[44:47], v[116:119], a[184:187]\n\t"
      "ds_read_b64_tr_b16 v[42:43], v242 offset:29184\n\t"
      "v_mfma_f32_16x16x32_bf16 a[188:191], v[44:47], v[124:127], a[188:191]\n\t"
      "s_waitcnt lgkmcnt(15)\n\t"
      "s_nop 0\n\t"
      "v_mfma_f32_16x16x32_bf16 a[192:195], v[48:51], v[100:103], a[192:195]\n\t"
      "ds_read_b64_tr_b16 v[44:45], v242 offset:25096\n\t"
      "v_mfma_f32_16x16x32_bf16 a[196:199], v[48:51], v[108:111], a[196:199]\n\t"
      "v_mfma_f32_16x16x32_bf16 a[200:203], v[48:51], v[116:119], a[200:203]\n\t"
      "ds_read_b64_tr_b16 v[46:47], v242 offset:29192\n\t"
      "v_mfma_f32_16x16x32_bf16 a[204:207], v[48:51], v[124:127], a[204:207]\n\t"
      "v_mfma_f32_16x16x32_bf16 a[208:211], v[52:55], v[100:103], a[208:211]\n\t"
      "ds_read_b64_tr_b16 v[48:49], v242 offset:25600\n\t"
      "v_mfma_f32_16x16x32_bf16 a[212:215], v[52:55], v[108:111], a[212:215]\n\t"
      "v_mfma_f32_16x16x32_bf16 a[216:219], v[52:55], v[116:119], a[216:219]\n\t"
      "ds_read_b64_tr_b16 v[50:51], v242 offset:29696\n\t"
      "v_mfma_f32_16x16x32_bf16 a[220:223], v[52:55], v[124:127], a[220:223]\n\t"
      "v_mfma_f32_16x16x32_bf16 a[224:227], v[56:59], v[100:103], a[224:227]\n\t"
      "ds_read_b64_tr_b16 v[52:53], v242 offset:25608\n\t"
      "v_mfma_f32_16x16x32_bf16 a[228:231], v[56:59], v[108:111], a[228:231]\n\t"
      "v_mfma_f32_16x16x32_bf16 a[232:235], v[56:59], v[116:119], a[232:235]\n\t"
      "ds_read_b64_tr_b16 v[54:55], v242 offset:29704\n\t"
      "v_mfma_f32_16x16x32_bf16 a[236:239], v[56:59], v[124:127], a[236:239]\n\t"
      "v_mfma_f32_16x16x32_bf16 a[240:243], v[60:63], v[100:103], a[240:243]\n\t"
      "ds_read_b64_tr_b16 v[56:57], v242 offset:26112\n\t"
      "v_mfma_f32_16x16x32_bf16 a[244:247], v[60:63], v[108:111], a[244:247]\n\t"
      "v_mfma_f32_16x16x32_bf16 a[248:251], v[60:63], v[116:119], a[248:251]\n\t"
      "ds_read_b64_tr_b16 v[58:59], v242 offset:30208\n\t"
      "v_mfma_f32_16x16x32_bf16 a[252:255], v[60:63], v[124:127], a[252:255]\n\t"
      "s_waitcnt vmcnt(0)\n\t"
      "s_barrier\n\t"
      "v_mov_b32_e32 v218, 0\n\t"
      "v_mov_b32_e32 v219, %10\n\t"
      "s_lshl_b32 s68, s74, 2\n\t"
      "s_cmp_lg_u32 s56, 0\n\t"
      "s_cbranch_scc1 L_NOADD\n\t"
      "s_mov_b64 exec, 1\n\t"
      "buffer_store_dword v219, v218, s[80:83], s68 offen sc1\n\t"
      "s_mov_b64 exec, -1\n\t"
      "L_NOADD:\n\t"
      "s_mov_b32 s75, 0\n\t"
      "L_POLLA:\n\t"
      "buffer_load_dwordx4 v[222:225], v218, s[80:83], 0 offen sc1\n\t"
      "s_add_u32 s75, s75, 1\n\t"
      "s_waitcnt vmcnt(0)\n\t"
      "v_xor_b32_e32 v222, %10, v222\n\t"
      "v_xor_b32_e32 v223, %10, v223\n\t"
      "v_or_b32_e32 v222, v222, v223\n\t"
      "v_xor_b32_e32 v224, %10, v224\n\t"
      "v_or_b32_e32 v222, v222, v224\n\t"
      "v_xor_b32_e32 v225, %10, v225\n\t"
      "v_or_b32_e32 v222, v222, v225\n\t"
      "v_cmp_ne_u32_e32 vcc, 0, v222\n\t"
      "s_cmp_ge_u32 s75, 0x10000\n\t"
      "s_cbranch_scc1 L_POLLEDA\n\t"
      "s_cbranch_vccnz L_POLLA\n\t"
      "L_POLLEDA:\n\t"
      "buffer_load_dwordx4 v[128:131], v246, s[44:47], s72 offen offset:0 sc1\n\t"
      "buffer_load_dwordx4 v[136:139], v247, s[44:47], s72 offen offset:0 sc1\n\t"
      "buffer_load_dwordx4 v[144:147], v248, s[44:47], s72 offen offset:0 sc1\n\t"
      "buffer_load_dwordx4 v[152:155], v249, s[44:47], s72 offen offset:0 sc1\n\t"
      "buffer_load_dwordx4 v[132:135], v246, s[44:47], s72 offen offset:1024 sc1\n\t"
      "buffer_load_dwordx4 v[140:143], v247, s[44:47], s72 offen offset:1024 sc1\n\t"
      "buffer_load_dwordx4 v[148:151], v248, s[44:47], s72 offen offset:1024 sc1\n\t"
      "buffer_load_dwordx4 v[156:159], v249, s[44:47], s72 offen offset:1024 sc1\n\t"
      "s_add_u32 s72, s72, 0x800\n\t"
      "s_and_b32 s72, s72, 0x3fff\n\t"
      "s_waitcnt vmcnt(4) lgkmcnt(15)\n\t"
      "s_nop 0\n\t"
      "v_mfma_f32_16x16x32_bf16 a[128:131], v[0:3], v[128:131], a[128:131]\n\t"
      "ds_read_b64_tr_b16 v[60:61], v242 offset:26120\n\t"
      "v_mfma_f32_16x16x32_bf16 a[132:135], v[0:3], v[136:139], a[132:135]\n\t"
      "buffer_load_dwordx4 v[64:67], v246, s[44:47], s72 offen offset:0 sc1\n\t"
      "v_mfma_f32_16x16x32_bf16 a[136:139], v[0:3], v[144:147], a[136:139]\n\t"
      "ds_read_b64_tr_b16 v[62:63], v242 offset:30216\n\t"
      "v_mfma_f32_16x16x32_bf16 a[140:143], v[0:3], v[152:155], a[140:143]\n\t"
      "v_mfma_f32_16x16x32_bf16 a[144:147], v[4:7], v[128:131], a[144:147]\n\t"
      "ds_read_b64_tr_b16 v[0:1], v243 offset:16384\n\t"
      "v_mfma_f32_16x16x32_bf16 a[148:151], v[4:7], v[136:139], a[148:151]\n\t"
      "buffer_load_dwordx4 v[72:75], v247, s[44:47], s72 offen offset:0 sc1\n\t"
      "v_mfma_f32_16x16x32_bf16 a[152:155], v[4:7], v[144:147], a[152:155]\n\t"
      "ds_read_b64_tr_b16 v[2:3], v243 offset:20480\n\t"
      "v_mfma_f32_16x16x32_bf16 a[156:159], v[4:7], v[152:155], a[156:159]\n\t"
      "v_mfma_f32_16x16x32_bf16 a[160:163], v[8:11], v[128:131], a[160:163]\n\t"
      "ds_read_b64_tr_b16 v[4:5], v243 offset:16392\n\t"
      "v_mfma_f32_16x16x32_bf16 a[164:167], v[8:11], v[136:139], a[164:167]\n\t"
      "buffer_load_dwordx4 v[80:83], v248, s[44:47], s72 offen offset:0 sc1\n\t"
      "v_mfma_f32_16x16x32_bf16 a[168:171], v[8:11], v[144:147], a[168:171]\n\t"
      "ds_read_b64_tr_b16 v[6:7], v243 offset:20488\n\t"
      "v_mfma_f32_16x16x32_bf16 a[172:175], v[8:11], v[152:155], a[172:175]\n\t"
      "v_mfma_f32_16x16x32_bf16 a[176:179], v[12:15], v[128:131], a[176:179]\n\t"
      "ds_read_b64_tr_b16 v[8:9], v243 offset:16896\n\t"
      "v_mfma_f32_16x16x32_bf16 a[180:183], v[12:15], v[136:139], a[180:183]\n\t"
      "buffer_load_dwordx4 v[88:91], v249, s[44:47], s72 offen offset:0 sc1\n\t"
      "v_mfma_f32_16x16x32_bf16 a[184:187], v[12:15], v[144:147], a[184:187]\n\t"
      "ds_read_b64_tr_b16 v[10:11], v243 offset:20992\n\t"
      "v_mfma_f32_16x16x32_bf16 a[188:191], v[12:15], v[152:155], a[188:191]\n\t"
      "s_waitcnt lgkmcnt(15)\n\t"
      "s_nop 0\n\t"
      "v_mfma_f32_16x16x32_bf16 a[192:195], v[16:19], v[128:131], a[192:195]\n\t"
      "ds_read_b64_tr_b16 v[12:13], v243 offset:16904\n\t"
      "v_mfma_f32_16x16x32_bf16 a[196:199], v[16:19], v[136:139], a[196:199]\n\t"
      "buffer_load_dwordx4 v[68:71], v246, s[44:47], s72 offen offset:1024 sc1\n\t"
      "v_mfma_f32_16x16x32_bf16 a[200:203], v[16:19], v[144:147], a[200:203]\n\t"
      "ds_read_b64_tr_b16 v[14:15], v243 offset:21000\n\t"
      "v_mfma_f32_16x16x32_bf16 a[204:207], v[16:19], v[152:155], a[204:207]\n\t"
      "v_mfma_f32_16x16x32_bf16 a[208:211], v[20:23], v[128:131], a[208:211]\n\t"
      "ds_read_b64_tr_b16 v[16:17], v243 offset:17408\n\t"
      "v_mfma_f32_16x16x32_bf16 a[212:215], v[20:23], v[136:139], a[212:215]\n\t"
      "buffer_load_dwordx4 v[76:79], v247, s[44:47], s72 offen offset:1024 sc1\n\t"
      "v_mfma_f32_16x16x32_bf16 a[216:219], v[20:23], v[144:147], a[216:219]\n\t"
      "ds_read_b64_tr_b16 v[18:19], v243 offset:21504\n\t"
      "v_mfma_f32_16x16x32_bf16 a[220:223], v[20:23], v[152:155], a[220:223]\n\t"
      "v_mfma_f32_16x16x32_bf16 a[224:227], v[24:27], v[128:131], a[224:227]\n\t"
      "ds_read_b64_tr_b16 v[20:21], v243 offset:17416\n\t"
      "v_mfma_f32_16x16x32_bf16 a[228:231], v[24:27], v[136:139], a[228:231]\n\t"
      "buffer_load_dwordx4 v[84:87], v248, s[44:47], s72 offen offset:1024 sc1\n\t"
      "v_mfma_f32_16x16x32_bf16 a[232:235], v[24:27], v[144:147], a[232:235]\n\t"
      "ds_read_b64_tr_b16 v[22:23], v243 offset:21512\n\t"
      "v_mfma_f32_16x16x32_bf16 a[236:239], v[24:27], v[152:155], a[236:239]\n\t"
      "v_mfma_f32_16x16x32_bf16 a[240:243], v[28:31], v[128:131], a[240:243]\n\t"
      "ds_read_b64_tr_b16 v[24:25], v243 offset:17920\n\t"
      "v_mfma_f32_16x16x32_bf16 a[244:247], v[28:31], v[136:139], a[244:247]\n\t"
      "buffer_load_dwordx4 v[92:95], v249, s[44:47], s72 offen offset:1024 sc1\n\t"
      "v_mfma_f32_16x16x32_bf16 a[248:251], v[28:31], v[144:147], a[248:251]\n\t"
      "ds_read_b64_tr_b16 v[26:27], v243 offset:22016\n\t"
      "v_mfma_f32_16x16x32_bf16 a[252:255], v[28:31], v[152:155], a[252:255]\n\t"
      "s_waitcnt vmcnt(8) lgkmcnt(15)\n\t"
      "s_nop 0\n\t"
      "v_mfma_f32_16x16x32_bf16 a[128:131], v[32:35], v[132:135], a[128:131]\n\t"
      "s_lshr_b32 s70, s57, 2\n\t"
      "s_nop 0\n\t"
      "ds_read_b64_tr_b16 v[28:29], v243 offset:17928\n\t"
      "v_mfma_f32_16x16x32_bf16 a[132:135], v[32:35], v[140:143], a[132:135]\n\t"
      "s_lshl_b32 s67, s58, 2\n\t"
      "s_nop 0\n\t"
      "s_add_u32 s72, s72, 0x800\n\t"
      "s_and_b32 s72, s72, 0x3fff\n\t"
      "v_mfma_f32_16x16x32_bf16 a[136:139], v[32:35], v[148:151], a[136:139]\n\t"
      "s_add_u32 s67, s67, s56\n\t"
      "s_nop 0\n\t"
      "ds_read_b64_tr_b16 v[30:31], v243 offset:22024\n\t"
      "v_mfma_f32_16x16x32_bf16 a[140:143], v[32:35], v[156:159], a[140:143]\n\t"
      "v_mfma_f32_16x16x32_bf16 a[144:147], v[36:39], v[132:135], a[144:147]\n\t"
      "s_lshl_b32 s68, s70, 21\n\t"
      "s_nop 0\n\t"
      "ds_read_b64_tr_b16 v[32:33], v243 offset:24576\n\t"
      "v_mfma_f32_16x16x32_bf16 a[148:151], v[36:39], v[140:143], a[148:151]\n\t"
      "s_lshl_b32 s69, s67, 16\n\t"
      "s_nop 0\n\t"
      "buffer_load_dwordx4 v[96:99], v246, s[44:47], s72 offen offset:0 sc1\n\t"
      "v_mfma_f32_16x16x32_bf16 a[152:155], v[36:39], v[148:151], a[152:155]\n\t"
      "s_add_u32 s68, s68, s69\n\t"
      "s_nop 0\n\t"
      "ds_read_b64_tr_b16 v[34:35], v243 offset:28672\n\t"
      "v_mfma_f32_16x16x32_bf16 a[156:159], v[36:39], v[156:159], a[156:159]\n\t"
      "v_mfma_f32_16x16x32_bf16 a[160:163], v[40:43], v[132:135], a[160:163]\n\t"
      "s_lshl_b32 s69, s74, 12\n\t"
      "s_nop 0\n\t"
      "ds_read_b64_tr_b16 v[36:37], v243 offset:24584\n\t"
      "v_mfma_f32_16x16x32_bf16 a[164:167], v[40:43], v[140:143], a[164:167]\n\t"
      "s_add_u32 s68, s68, s69\n\t"
      "s_nop 0\n\t"
      "buffer_load_dwordx4 v[104:107], v247, s[44:47], s72 offen offset:0 sc1\n\t"
      "v_mfma_f32_16x16x32_bf16 a[168:171], v[40:43], v[148:151], a[168:171]\n\t"
      "s_sub_u32 s69, 0x10000, s69\n\t"
      "ds_read_b64_tr_b16 v[38:39], v243 offset:28680\n\t"
      "v_mfma_f32_16x16x32_bf16 a[172:175], v[40:43], v[156:159], a[172:175]\n\t"
      "v_mfma_f32_16x16x32_bf16 a[176:179], v[44:47], v[132:135], a[176:179]\n\t"
      "s_mov_b64 s[52:53], %1\n\t"
      "s_nop 0\n\t"
      "ds_read_b64_tr_b16 v[40:41], v243 offset:25088\n\t"
      "v_mfma_f32_16x16x32_bf16 a[180:183], v[44:47], v[140:143], a[180:183]\n\t"
      "s_add_u32 s52, s52, 8388608\n\t"
      "buffer_load_dwordx4 v[112:115], v248, s[44:47], s72 offen offset:0 sc1\n\t"
      "v_mfma_f32_16x16x32_bf16 a[184:187], v[44:47], v[148:151], a[184:187]\n\t"
      "s_addc_u32 s53, s53, 0\n\t"
      "s_nop 0\n\t"
      "ds_read_b64_tr_b16 v[42:43], v243 offset:29184\n\t"
      "v_mfma_f32_16x16x32_bf16 a[188:191], v[44:47], v[156:159], a[188:191]\n\t"
      "s_waitcnt lgkmcnt(15)\n\t"
      "s_nop 0\n\t"
      "v_mfma_f32_16x16x32_bf16 a[192:195], v[48:51], v[132:135], a[192:195]\n\t"
      "s_add_u32 s52, s52, s68\n\t"
      "s_nop 0\n\t"
      "ds_read_b64_tr_b16 v[44:45], v243 offset:25096\n\t"
      "v_mfma_f32_16x16x32_bf16 a[196:199], v[48:51], v[140:143], a[196:199]\n\t"
      "s_addc_u32 s53, s53, 0\n\t"
      "s_nop 0\n\t"
      "buffer_load_dwordx4 v[120:123], v249, s[44:47], s72 offen offset:0 sc1\n\t"
      "v_mfma_f32_16x16x32_bf16 a[200:203], v[48:51], v[148:151], a[200:203]\n\t"
      "s_and_b32 s53, s53, 0xffff\n\t"
      "ds_read_b64_tr_b16 v[46:47], v243 offset:29192\n\t"
      "v_mfma_f32_16x16x32_bf16 a[204:207], v[48:51], v[156:159], a[204:207]\n\t"
      "v_mfma_f32_16x16x32_bf16 a[208:211], v[52:55], v[132:135], a[208:211]\n\t"
      "s_mov_b32 s54, s69\n\t"
      "s_nop 0\n\t"
      "ds_read_b64_tr_b16 v[48:49], v243 offset:25600\n\t"
      "v_mfma_f32_16x16x32_bf16 a[212:215], v[52:55], v[140:143], a[212:215]\n\t"
      "s_mov_b32 s55, 0x20000\n\t"
      "buffer_load_dwordx4 v[100:103], v246, s[44:47], s72 offen offset:1024 sc1\n\t"
      "v_mfma_f32_16x16x32_bf16 a[216:219], v[52:55], v[148:151], a[216:219]\n\t"
      "ds_read_b64_tr_b16 v[50:51], v243 offset:29696\n\t"
      "v_mfma_f32_16x16x32_bf16 a[220:223], v[52:55], v[156:159], a[220:223]\n\t"
      "v_mfma_f32_16x16x32_bf16 a[224:227], v[56:59], v[132:135], a[224:227]\n\t"
      "ds_read_b64_tr_b16 v[52:53], v243 offset:25608\n\t"
      "v_mfma_f32_16x16x32_bf16 a[228:231], v[56:59], v[140:143], a[228:231]\n\t"
      "buffer_load_dwordx4 v[108:111], v247, s[44:47], s72 offen offset:1024 sc1\n\t"
      "v_mfma_f32_16x16x32_bf16 a[232:235], v[56:59], v[148:151], a[232:235]\n\t"
      "ds_read_b64_tr_b16 v[54:55], v243 offset:29704\n\t"
      "v_mfma_f32_16x16x32_bf16 a[236:239], v[56:59], v[156:159], a[236:239]\n\t"
      "v_mfma_f32_16x16x32_bf16 a[240:243], v[60:63], v[132:135], a[240:243]\n\t"
      "ds_read_b64_tr_b16 v[56:57], v243 offset:26112\n\t"
      "v_mfma_f32_16x16x32_bf16 a[244:247], v[60:63], v[140:143], a[244:247]\n\t"
      "buffer_load_dwordx4 v[116:119], v248, s[44:47], s72 offen offset:1024 sc1\n\t"
      "v_mfma_f32_16x16x32_bf16 a[248:251], v[60:63], v[148:151], a[248:251]\n\t"
      "ds_read_b64_tr_b16 v[58:59], v243 offset:30208\n\t"
      "v_mfma_f32_16x16x32_bf16 a[252:255], v[60:63], v[156:159], a[252:255]\n\t"
      "s_waitcnt vmcnt(11) lgkmcnt(15)\n\t"
      "s_nop 0\n\t"
      "v_mfma_f32_16x16x32_bf16 a[128:131], v[0:3], v[64:67], a[128:131]\n\t"
      "ds_read_b64_tr_b16 v[60:61], v243 offset:26120\n\t"
      "v_mfma_f32_16x16x32_bf16 a[132:135], v[0:3], v[72:75], a[132:135]\n\t"
      "buffer_load_dwordx4 v[124:127], v249, s[44:47], s72 offen offset:1024 sc1\n\t"
      "v_mfma_f32_16x16x32_bf16 a[136:139], v[0:3], v[80:83], a[136:139]\n\t"
      "ds_read_b64_tr_b16 v[62:63], v243 offset:30216\n\t"
      "v_mfma_f32_16x16x32_bf16 a[140:143], v[0:3], v[88:91], a[140:143]\n\t"
      "v_mfma_f32_16x16x32_bf16 a[144:147], v[4:7], v[64:67], a[144:147]\n\t"
      "ds_read_b64_tr_b16 v[0:1], v242 offset:32768\n\t"
      "v_mfma_f32_16x16x32_bf16 a[148:151], v[4:7], v[72:75], a[148:151]\n\t"
      "s_add_u32 s72, s72, 0x800\n\t"
      "s_and_b32 s72, s72, 0x3fff\n\t"
      "v_mfma_f32_16x16x32_bf16 a[152:155], v[4:7], v[80:83], a[152:155]\n\t"
      "ds_read_b64_tr_b16 v[2:3], v242 offset:36864\n\t"
      "v_mfma_f32_16x16x32_bf16 a[156:159], v[4:7], v[88:91], a[156:159]\n\t"
      "v_mfma_f32_16x16x32_bf16 a[160:163], v[8:11], v[64:67], a[160:163]\n\t"
      "ds_read_b64_tr_b16 v[4:5], v242 offset:32776\n\t"
      "v_mfma_f32_16x16x32_bf16 a[164:167], v[8:11], v[72:75], a[164:167]\n\t"
      "buffer_load_dwordx4 a[0:3], v246, s[44:47], s72 offen offset:0 sc1\n\t"
      "v_mfma_f32_16x16x32_bf16 a[168:171], v[8:11], v[80:83], a[168:171]\n\t"
      "ds_read_b64_tr_b16 v[6:7], v242 offset:36872\n\t"
      "v_mfma_f32_16x16x32_bf16 a[172:175], v[8:11], v[88:91], a[172:175]\n\t"
      "v_mfma_f32_16x16x32_bf16 a[176:179], v[12:15], v[64:67], a[176:179]\n\t"
      "ds_read_b64_tr_b16 v[8:9], v242 offset:40960\n\t"
      "v_mfma_f32_16x16x32_bf16 a[180:183], v[12:15], v[72:75], a[180:183]\n\t"
      "buffer_load_dwordx4 a[8:11], v247, s[44:47], s72 offen offset:0 sc1\n\t"
      "v_mfma_f32_16x16x32_bf16 a[184:187], v[12:15], v[80:83], a[184:187]\n\t"
      "ds_read_b64_tr_b16 v[10:11], v242 offset:45056\n\t"
      "v_mfma_f32_16x16x32_bf16 a[188:191], v[12:15], v[88:91], a[188:191]\n\t"
      "s_waitcnt lgkmcnt(15)\n\t"
      "s_nop 0\n\t"
      "v_mfma_f32_16x16x32_bf16 a[192:195], v[16:19], v[64:67], a[192:195]\n\t"
      "ds_read_b64_tr_b16 v[12:13], v242 offset:40968\n\t"
      "v_mfma_f32_16x16x32_bf16 a[196:199], v[16:19], v[72:75], a[196:199]\n\t"
      "buffer_load_dwordx4 a[16:19], v248, s[44:47], s72 offen offset:0 sc1\n\t"
      "v_mfma_f32_16x16x32_bf16 a[200:203], v[16:19], v[80:83], a[200:203]\n\t"
      "ds_read_b64_tr_b16 v[14:15], v242 offset:45064\n\t"
      "v_mfma_f32_16x16x32_bf16 a[204:207], v[16:19], v[88:91], a[204:207]\n\t"
      "v_mfma_f32_16x16x32_bf16 a[208:211], v[20:23], v[64:67], a[208:211]\n\t"
      "ds_read_b64_tr_b16 v[16:17], v243 offset:32768\n\t"
      "v_mfma_f32_16x16x32_bf16 a[212:215], v[20:23], v[72:75], a[212:215]\n\t"
      "buffer_load_dwordx4 a[24:27], v249, s[44:47], s72 offen offset:0 sc1\n\t"
      "v_mfma_f32_16x16x32_bf16 a[216:219], v[20:23], v[80:83], a[216:219]\n\t"
      "ds_read_b64_tr_b16 v[18:19], v243 offset:36864\n\t"
      "v_mfma_f32_16x16x32_bf16 a[220:223], v[20:23], v[88:91], a[220:223]\n\t"
      "v_mfma_f32_16x16x32_bf16 a[224:227], v[24:27], v[64:67], a[224:227]\n\t"
      "ds_read_b64_tr_b16 v[20:21], v243 offset:32776\n\t"
      "v_mfma_f32_16x16x32_bf16 a[228:231], v[24:27], v[72:75], a[228:231]\n\t"
      "buffer_load_dwordx4 a[4:7], v246, s[44:47], s72 offen offset:1024 sc1\n\t"
      "v_mfma_f32_16x16x32_bf16 a[232:235], v[24:27], v[80:83], a[232:235]\n\t"
      "ds_read_b64_tr_b16 v[22:23], v243 offset:36872\n\t"
      "v_mfma_f32_16x16x32_bf16 a[236:239], v[24:27], v[88:91], a[236:239]\n\t"
      "v_mfma_f32_16x16x32_bf16 a[240:243], v[28:31], v[64:67], a[240:243]\n\t"
      "ds_read_b64_tr_b16 v[24:25], v243 offset:40960\n\t"
      "v_mfma_f32_16x16x32_bf16 a[244:247], v[28:31], v[72:75], a[244:247]\n\t"
      "buffer_load_dwordx4 a[12:15], v247, s[44:47], s72 offen offset:1024 sc1\n\t"
      "v_mfma_f32_16x16x32_bf16 a[248:251], v[28:31], v[80:83], a[248:251]\n\t"
      "ds_read_b64_tr_b16 v[26:27], v243 offset:45056\n\t"
      "v_mfma_f32_16x16x32_bf16 a[252:255], v[28:31], v[88:91], a[252:255]\n\t"
      "s_waitcnt vmcnt(14) lgkmcnt(15)\n\t"
      "s_nop 0\n\t"
      "v_mfma_f32_16x16x32_bf16 a[128:131], v[32:35], v[68:71], a[128:131]\n\t"
      "ds_read_b64_tr_b16 v[28:29], v243 offset:40968\n\t"
      "v_mfma_f32_16x16x32_bf16 a[132:135], v[32:35], v[76:79], a[132:135]\n\t"
      "buffer_load_dwordx4 a[20:23], v248, s[44:47], s72 offen offset:1024 sc1\n\t"
      "v_mfma_f32_16x16x32_bf16 a[136:139], v[32:35], v[84:87], a[136:139]\n\t"
      "ds_read_b64_tr_b16 v[30:31], v243 offset:45064\n\t"
      "v_mfma_f32_16x16x32_bf16 a[140:143], v[32:35], v[92:95], a[140:143]\n\t"
      "v_mfma_f32_16x16x32_bf16 a[144:147], v[36:39], v[68:71], a[144:147]\n\t"
      "ds_read_b64_tr_b16 v[32:33], v242 offset:49152\n\t"
      "v_mfma_f32_16x16x32_bf16 a[148:151], v[36:39], v[76:79], a[148:151]\n\t"
      "buffer_load_dwordx4 a[28:31], v249, s[44:47], s72 offen offset:1024 sc1\n\t"
      "v_mfma_f32_16x16x32_bf16 a[152:155], v[36:39], v[84:87], a[152:155]\n\t"
      "ds_read_b64_tr_b16 v[34:35], v242 offset:53248\n\t"
      "v_mfma_f32_16x16x32_bf16 a[156:159], v[36:39], v[92:95], a[156:159]\n\t"
      "v_mfma_f32_16x16x32_bf16 a[160:163], v[40:43], v[68:71], a[160:163]\n\t"
      "ds_read_b64_tr_b16 v[36:37], v242 offset:49160\n\t"
      "v_mfma_f32_16x16x32_bf16 a[164:167], v[40:43], v[76:79], a[164:167]\n\t"
      "s_add_u32 s72, s72, 0x800\n\t"
      "s_and_b32 s72, s72, 0x3fff\n\t"
      "v_mfma_f32_16x16x32_bf16 a[168:171], v[40:43], v[84:87], a[168:171]\n\t"
      "ds_read_b64_tr_b16 v[38:39], v242 offset:53256\n\t"
      "v_mfma_f32_16x16x32_bf16 a[172:175], v[40:43], v[92:95], a[172:175]\n\t"
      "v_mfma_f32_16x16x32_bf16 a[176:179], v[44:47], v[68:71], a[176:179]\n\t"
      "ds_read_b64_tr_b16 v[40:41], v242 offset:57344\n\t"
      "v_mfma_f32_16x16x32_bf16 a[180:183], v[44:47], v[76:79], a[180:183]\n\t"
      "buffer_load_dwordx4 a[32:35], v246, s[44:47], s72 offen offset:0 sc1\n\t"
      "v_mfma_f32_16x16x32_bf16 a[184:187], v[44:47], v[84:87], a[184:187]\n\t"
      "ds_read_b64_tr_b16 v[42:43], v242 offset:61440\n\t"
      "v_mfma_f32_16x16x32_bf16 a[188:191], v[44:47], v[92:95], a[188:191]\n\t"
      "s_waitcnt lgkmcnt(15)\n\t"
      "s_nop 0\n\t"
      "v_mfma_f32_16x16x32_bf16 a[192:195], v[48:51], v[68:71], a[192:195]\n\t"
      "ds_read_b64_tr_b16 v[44:45], v242 offset:57352\n\t"
      "v_mfma_f32_16x16x32_bf16 a[196:199], v[48:51], v[76:79], a[196:199]\n\t"
      "buffer_load_dwordx4 a[40:43], v247, s[44:47], s72 offen offset:0 sc1\n\t"
      "v_mfma_f32_16x16x32_bf16 a[200:203], v[48:51], v[84:87], a[200:203]\n\t"
      "ds_read_b64_tr_b16 v[46:47], v242 offset:61448\n\t"
      "v_mfma_f32_16x16x32_bf16 a[204:207], v[48:51], v[92:95], a[204:207]\n\t"
      "v_mfma_f32_16x16x32_bf16 a[208:211], v[52:55], v[68:71], a[208:211]\n\t"
      "ds_read_b64_tr_b16 v[48:49], v243 offset:49152\n\t"
      "v_mfma_f32_16x16x32_bf16 a[212:215], v[52:55], v[76:79], a[212:215]\n\t"
      "buffer_load_dwordx4 a[48:51], v248, s[44:47], s72 offen offset:0 sc1\n\t"
      "v_mfma_f32_16x16x32_bf16 a[216:219], v[52:55], v[84:87], a[216:219]\n\t"
      "ds_read_b64_tr_b16 v[50:51], v243 offset:53248\n\t"
      "v_mfma_f32_16x16x32_bf16 a[220:223], v[52:55], v[92:95], a[220:223]\n\t"
      "v_mfma_f32_16x16x32_bf16 a[224:227], v[56:59], v[68:71], a[224:227]\n\t"
      "ds_read_b64_tr_b16 v[52:53], v243 offset:49160\n\t"
      "v_mfma_f32_16x16x32_bf16 a[228:231], v[56:59], v[76:79], a[228:231]\n\t"
      "buffer_load_dwordx4 a[56:59], v249, s[44:47], s72 offen offset:0 sc1\n\t"
      "v_mfma_f32_16x16x32_bf16 a[232:235], v[56:59], v[84:87], a[232:235]\n\t"
      "ds_read_b64_tr_b16 v[54:55], v243 offset:53256\n\t"
      "v_mfma_f32_16x16x32_bf16 a[236:239], v[56:59], v[92:95], a[236:239]\n\t"
      "v_mfma_f32_16x16x32_bf16 a[240:243], v[60:63], v[68:71], a[240:243]\n\t"
      "ds_read_b64_tr_b16 v[56:57], v243 offset:57344\n\t"
      "v_mfma_f32_16x16x32_bf16 a[244:247], v[60:63], v[76:79], a[244:247]\n\t"
      "buffer_load_dwordx4 a[36:39], v246, s[44:47], s72 offen offset:1024 sc1\n\t"
      "v_mfma_f32_16x16x32_bf16 a[248:251], v[60:63], v[84:87], a[248:251]\n\t"
      "ds_read_b64_tr_b16 v[58:59], v243 offset:61440\n\t"
      "v_mfma_f32_16x16x32_bf16 a[252:255], v[60:63], v[92:95], a[252:255]\n\t"
      "s_waitcnt vmcnt(17) lgkmcnt(15)\n\t"
      "s_nop 0\n\t"
      "v_mfma_f32_16x16x32_bf16 a[128:131], v[0:3], v[96:99], a[128:131]\n\t"
      "ds_read_b64_tr_b16 v[60:61], v243 offset:57352\n\t"
      "v_add_u32_e32 v236, 0x4000, v246\n\t"
      "v_add_u32_e32 v237, 0x8000, v246\n\t"
      "v_add_u32_e32 v238, 0xc000, v246\n\t"
      "v_mov_b32_e64 v235, v246\n\t"
      "v_mfma_f32_16x16x32_bf16 a[132:135], v[0:3], v[104:107], a[132:135]\n\t"
      "buffer_load_dwordx4 a[44:47], v247, s[44:47], s72 offen offset:1024 sc1\n\t"
      "v_mfma_f32_16x16x32_bf16 a[136:139], v[0:3], v[112:115], a[136:139]\n\t"
      "ds_read_b64_tr_b16 v[62:63], v243 offset:61448\n\t"
      "v_mfma_f32_16x16x32_bf16 a[140:143], v[0:3], v[120:123], a[140:143]\n\t"
      "v_mfma_f32_16x16x32_bf16 a[144:147], v[4:7], v[96:99], a[144:147]\n\t"
      "ds_read_b64_tr_b16 v[0:1], v242 offset:33280\n\t"
      "v_mfma_f32_16x16x32_bf16 a[148:151], v[4:7], v[104:107], a[148:151]\n\t"
      "buffer_load_dwordx4 a[52:55], v248, s[44:47], s72 offen offset:1024 sc1\n\t"
      "v_mfma_f32_16x16x32_bf16 a[152:155], v[4:7], v[112:115], a[152:155]\n\t"
      "ds_read_b64_tr_b16 v[2:3], v242 offset:37376\n\t"
      "v_mfma_f32_16x16x32_bf16 a[156:159], v[4:7], v[120:123], a[156:159]\n\t"
      "s_waitcnt vmcnt(15)\n\t"
      "s_nop 0\n\t"
      "v_mfma_f32_16x16x32_bf16 a[128:131], v[8:11], v[100:103], a[128:131]\n\t"
      "ds_read_b64_tr_b16 v[4:5], v242 offset:33288\n\t"
      "v_mfma_f32_16x16x32_bf16 a[132:135], v[8:11], v[108:111], a[132:135]\n\t"
      "buffer_load_dwordx4 a[60:63], v249, s[44:47], s72 offen offset:1024 sc1\n\t"
      "v_mfma_f32_16x16x32_bf16 a[136:139], v[8:11], v[116:119], a[136:139]\n\t"
      "ds_read_b64_tr_b16 v[6:7], v242 offset:37384\n\t"
      "v_mfma_f32_16x16x32_bf16 a[140:143], v[8:11], v[124:127], a[140:143]\n\t"
      "v_mfma_f32_16x16x32_bf16 a[144:147], v[12:15], v[100:103], a[144:147]\n\t"
      "ds_read_b64_tr_b16 v[8:9], v242 offset:41472\n\t"
      "v_mfma_f32_16x16x32_bf16 a[148:151], v[12:15], v[108:111], a[148:151]\n\t"
      "s_add_u32 s72, s72, 0x800\n\t"
      "s_and_b32 s72, s72, 0x3fff\n\t"
      "v_mfma_f32_16x16x32_bf16 a[152:155], v[12:15], v[116:119], a[152:155]\n\t"
      "ds_read_b64_tr_b16 v[10:11], v242 offset:45568\n\t"
      "v_mfma_f32_16x16x32_bf16 a[156:159], v[12:15], v[124:127], a[156:159]\n\t"
      "s_waitcnt vmcnt(12) lgkmcnt(15)\n\t"
      "s_nop 0\n\t"
      "v_mfma_f32_16x16x32_bf16 a[128:131], v[16:19], a[0:3], a[128:131]\n\t"
      "ds_read_b64_tr_b16 v[12:13], v242 offset:41480\n\t"
      "v_mfma_f32_16x16x32_bf16 a[132:135], v[16:19], a[8:11], a[132:135]\n\t"
      "buffer_load_dwordx4 a[64:67], v246, s[44:47], s72 offen offset:0 sc1\n\t"
      "v_mfma_f32_16x16x32_bf16 a[136:139], v[16:19], a[16:19], a[136:139]\n\t"
      "ds_read_b64_tr_b16 v[14:15], v242 offset:45576\n\t"
      "v_mfma_f32_16x16x32_bf16 a[140:143], v[16:19], a[24:27], a[140:143]\n\t"
      "v_mfma_f32_16x16x32_bf16 a[144:147], v[20:23], a[0:3], a[144:147]\n\t"
      "ds_read_b64_tr_b16 v[16:17], v243 offset:33280\n\t"
      "v_mfma_f32_16x16x32_bf16 a[148:151], v[20:23], a[8:11], a[148:151]\n\t"
      "buffer_load_dwordx4 a[72:75], v247, s[44:47], s72 offen offset:0 sc1\n\t"
      "v_mfma_f32_16x16x32_bf16 a[152:155], v[20:23], a[16:19], a[152:155]\n\t"
      "ds_read_b64_tr_b16 v[18:19], v243 offset:37376\n\t"
      "v_mfma_f32_16x16x32_bf16 a[156:159], v[20:23], a[24:27], a[156:159]\n\t"
      "s_waitcnt vmcnt(10)\n\t"
      "s_nop 0\n\t"
      "v_mfma_f32_16x16x32_bf16 a[128:131], v[24:27], a[4:7], a[128:131]\n\t"
      "ds_read_b64_tr_b16 v[20:21], v243 offset:33288\n\t"
      "v_mfma_f32_16x16x32_bf16 a[132:135], v[24:27], a[12:15], a[132:135]\n\t"
      "buffer_load_dwordx4 a[80:83], v248, s[44:47], s72 offen offset:0 sc1\n\t"
      "v_mfma_f32_16x16x32_bf16 a[136:139], v[24:27], a[20:23], a[136:139]\n\t"
      "ds_read_b64_tr_b16 v[22:23], v243 offset:37384\n\t"
      "v_mfma_f32_16x16x32_bf16 a[140:143], v[24:27], a[28:31], a[140:143]\n\t"
      "v_mfma_f32_16x16x32_bf16 a[144:147], v[28:31], a[4:7], a[144:147]\n\t"
      "ds_read_b64_tr_b16 v[24:25], v243 offset:41472\n\t"
      "v_mfma_f32_16x16x32_bf16 a[148:151], v[28:31], a[12:15], a[148:151]\n\t"
      "buffer_load_dwordx4 a[88:91], v249, s[44:47], s72 offen offset:0 sc1\n\t"
      "v_mfma_f32_16x16x32_bf16 a[152:155], v[28:31], a[20:23], a[152:155]\n\t"
      "ds_read_b64_tr_b16 v[26:27], v243 offset:45568\n\t"
      "v_mfma_f32_16x16x32_bf16 a[156:159], v[28:31], a[28:31], a[156:159]\n\t"
      "s_waitcnt vmcnt(8) lgkmcnt(15)\n\t"
      "s_nop 0\n\t"
      "v_mfma_f32_16x16x32_bf16 a[128:131], v[32:35], a[32:35], a[128:131]\n\t"
      "ds_read_b64_tr_b16 v[28:29], v243 offset:41480\n\t"
      "v_mfma_f32_16x16x32_bf16 a[132:135], v[32:35], a[40:43], a[132:135]\n\t"
      "buffer_load_dwordx4 a[68:71], v246, s[44:47], s72 offen offset:1024 sc1\n\t"
      "v_mfma_f32_16x16x32_bf16 a[136:139], v[32:35], a[48:51], a[136:139]\n\t"
      "ds_read_b64_tr_b16 v[30:31], v243 offset:45576\n\t"
      "v_mfma_f32_16x16x32_bf16 a[140:143], v[32:35], a[56:59], a[140:143]\n\t"
      "v_mfma_f32_16x16x32_bf16 a[144:147], v[36:39], a[32:35], a[144:147]\n\t"
      "ds_read_b64_tr_b16 v[32:33], v242 offset:49664\n\t"
      "v_mfma_f32_16x16x32_bf16 a[148:151], v[36:39], a[40:43], a[148:151]\n\t"
      "buffer_load_dwordx4 a[76:79], v247, s[44:47], s72 offen offset:1024 sc1\n\t"
      "v_mfma_f32_16x16x32_bf16 a[152:155], v[36:39], a[48:51], a[152:155]\n\t"
      "ds_read_b64_tr_b16 v[34:35], v242 offset:53760\n\t"
      "v_mfma_f32_16x16x32_bf16 a[156:159], v[36:39], a[56:59], a[156:159]\n\t"
      "s_waitcnt vmcnt(6)\n\t"
      "s_nop 0\n\t"
      "v_mfma_f32_16x16x32_bf16 a[128:131], v[40:43], a[36:39], a[128:131]\n\t"
      "ds_read_b64_tr_b16 v[36:37], v242 offset:49672\n\t"
      "v_mfma_f32_16x16x32_bf16 a[132:135], v[40:43], a[44:47], a[132:135]\n\t"
      "buffer_load_dwordx4 a[84:87], v248, s[44:47], s72 offen offset:1024 sc1\n\t"
      "v_mfma_f32_16x16x32_bf16 a[136:139], v[40:43], a[52:55], a[136:139]\n\t"
      "ds_read_b64_tr_b16 v[38:39], v242 offset:53768\n\t"
      "v_mfma_f32_16x16x32_bf16 a[140:143], v[40:43], a[60:63], a[140:143]\n\t"
      "v_mfma_f32_16x16x32_bf16 a[144:147], v[44:47], a[36:39], a[144:147]\n\t"
      "ds_read_b64_tr_b16 v[40:41], v242 offset:57856\n\t"
      "v_mfma_f32_16x16x32_bf16 a[148:151], v[44:47], a[44:47], a[148:151]\n\t"
      "buffer_load_dwordx4 a[92:95], v249, s[44:47], s72 offen offset:1024 sc1\n\t"
      "v_mfma_f32_16x16x32_bf16 a[152:155], v[44:47], a[52:55], a[152:155]\n\t"
      "ds_read_b64_tr_b16 v[42:43], v242 offset:61952\n\t"
      "v_mfma_f32_16x16x32_bf16 a[156:159], v[44:47], a[60:63], a[156:159]\n\t"
      "s_waitcnt vmcnt(4) lgkmcnt(15)\n\t"
      "s_nop 0\n\t"
      "v_mfma_f32_16x16x32_bf16 a[128:131], v[48:51], a[64:67], a[128:131]\n\t"
      "ds_read_b64_tr_b16 v[44:45], v242 offset:57864\n\t"
      "v_mfma_f32_16x16x32_bf16 a[132:135], v[48:51], a[72:75], a[132:135]\n\t"
      "s_add_u32 s72, s72, 0x800\n\t"
      "s_and_b32 s72, s72, 0x3fff\n\t"
      "v_mfma_f32_16x16x32_bf16 a[136:139], v[48:51], a[80:83], a[136:139]\n\t"
      "ds_read_b64_tr_b16 v[46:47], v242 offset:61960\n\t"
      "v_mfma_f32_16x16x32_bf16 a[140:143], v[48:51], a[88:91], a[140:143]\n\t"
      "v_mfma_f32_16x16x32_bf16 a[144:147], v[52:55], a[64:67], a[144:147]\n\t"
      "ds_read_b64_tr_b16 v[48:49], v243 offset:49664\n\t"
      "v_mfma_f32_16x16x32_bf16 a[148:151], v[52:55], a[72:75], a[148:151]\n\t"
      "v_mfma_f32_16x16x32_bf16 a[152:155], v[52:55], a[80:83], a[152:155]\n\t"
      "ds_read_b64_tr_b16 v[50:51], v243 offset:53760\n\t"
      "v_mfma_f32_16x16x32_bf16 a[156:159], v[52:55], a[88:91], a[156:159]\n\t"
      "s_waitcnt vmcnt(0)\n\t"
      "s_nop 0\n\t"
      "v_mfma_f32_16x16x32_bf16 a[128:131], v[56:59], a[68:71], a[128:131]\n\t"
      "ds_read_b64_tr_b16 v[52:53], v243 offset:49672\n\t"
      "v_mfma_f32_16x16x32_bf16 a[132:135], v[56:59], a[76:79], a[132:135]\n\t"
      "v_mfma_f32_16x16x32_bf16 a[136:139], v[56:59], a[84:87], a[136:139]\n\t"
      "ds_read_b64_tr_b16 v[54:55], v243 offset:53768\n\t"
      "v_mfma_f32_16x16x32_bf16 a[140:143], v[56:59], a[92:95], a[140:143]\n\t"
      "v_mfma_f32_16x16x32_bf16 a[144:147], v[60:63], a[68:71], a[144:147]\n\t"
      "ds_read_b64_tr_b16 v[56:57], v243 offset:57856\n\t"
      "v_mfma_f32_16x16x32_bf16 a[148:151], v[60:63], a[76:79], a[148:151]\n\t"
      "v_mfma_f32_16x16x32_bf16 a[152:155], v[60:63], a[84:87], a[152:155]\n\t"
      "ds_read_b64_tr_b16 v[58:59], v243 offset:61952\n\t"
      "v_mfma_f32_16x16x32_bf16 a[156:159], v[60:63], a[92:95], a[156:159]\n\t"
      "s_waitcnt lgkmcnt(15)\n\t"
      "s_nop 0\n\t"
      "v_mfma_f32_16x16x32_bf16 a[160:163], v[0:3], v[96:99], a[160:163]\n\t"
      "ds_read_b64_tr_b16 v[60:61], v243 offset:57864\n\t"
      "v_mfma_f32_16x16x32_bf16 a[164:167], v[0:3], v[104:107], a[164:167]\n\t"
      "v_mfma_f32_16x16x32_bf16 a[168:171], v[0:3], v[112:115], a[168:171]\n\t"
      "ds_read_b64_tr_b16 v[62:63], v243 offset:61960\n\t"
      "v_mfma_f32_16x16x32_bf16 a[172:175], v[0:3], v[120:123], a[172:175]\n\t"
      "v_accvgpr_read_b32 v192, a128\n\t"
      "v_accvgpr_read_b32 v193, a129\n\t"
      "v_mfma_f32_16x16x32_bf16 a[176:179], v[4:7], v[96:99], a[176:179]\n\t"
      "ds_read_b64_tr_b16 v[0:1], v242 offset:33792\n\t"
      "v_accvgpr_read_b32 v194, a130\n\t"
      "v_accvgpr_read_b32 v195, a131\n\t"
      "v_mfma_f32_16x16x32_bf16 a[180:183], v[4:7], v[104:107], a[180:183]\n\t"
      "v_accvgpr_read_b32 v196, a144\n\t"
      "v_accvgpr_read_b32 v197, a145\n\t"
      "v_mfma_f32_16x16x32_bf16 a[184:187], v[4:7], v[112:115], a[184:187]\n\t"
      "ds_read_b64_tr_b16 v[2:3], v242 offset:37888\n\t"
      "v_accvgpr_read_b32 v198, a146\n\t"
      "v_accvgpr_read_b32 v199, a147\n\t"
      "v_mfma_f32_16x16x32_bf16 a[188:191], v[4:7], v[120:123], a[188:191]\n\t"
      "v_cvt_pk_bf16_f32 v160, v192, v193\n\t"
      "v_cvt_pk_bf16_f32 v161, v194, v195\n\t"
      "v_mfma_f32_16x16x32_bf16 a[160:163], v[8:11], v[100:103], a[160:163]\n\t"
      "ds_read_b64_tr_b16 v[4:5], v242 offset:33800\n\t"
      "v_cvt_pk_bf16_f32 v162, v196, v197\n\t"
      "v_cvt_pk_bf16_f32 v163, v198, v199\n\t"
      "v_mfma_f32_16x16x32_bf16 a[164:167], v[8:11], v[108:111], a[164:167]\n\t"
      "buffer_store_dwordx4 v[160:163], v235, s[52:55], 0 offen sc1\n\t"
      "v_accvgpr_read_b32 v200, a132\n\t"
      "v_mfma_f32_16x16x32_bf16 a[168:171], v[8:11], v[116:119], a[168:171]\n\t"
      "ds_read_b64_tr_b16 v[6:7], v242 offset:37896\n\t"
      "v_accvgpr_read_b32 v201, a133\n\t"
      "v_accvgpr_read_b32 v202, a134\n\t"
      "v_mfma_f32_16x16x32_bf16 a[172:175], v[8:11], v[124:127], a[172:175]\n\t"
      "v_accvgpr_read_b32 v203, a135\n\t"
      "v_accvgpr_read_b32 v204, a148\n\t"
      "v_mfma_f32_16x16x32_bf16 a[176:179], v[12:15], v[100:103], a[176:179]\n\t"
      "ds_read_b64_tr_b16 v[8:9], v242 offset:41984\n\t"
      "v_accvgpr_read_b32 v205, a149\n\t"
      "v_accvgpr_read_b32 v206, a150\n\t"
      "v_mfma_f32_16x16x32_bf16 a[180:183], v[12:15], v[108:111], a[180:183]\n\t"
      "v_accvgpr_read_b32 v207, a151\n\t"
      "v_cvt_pk_bf16_f32 v164, v200, v201\n\t"
      "v_mfma_f32_16x16x32_bf16 a[184:187], v[12:15], v[116:119], a[184:187]\n\t"
      "ds_read_b64_tr_b16 v[10:11], v242 offset:46080\n\t"
      "v_cvt_pk_bf16_f32 v165, v202, v203\n\t"
      "v_cvt_pk_bf16_f32 v166, v204, v205\n\t"
      "v_mfma_f32_16x16x32_bf16 a[188:191], v[12:15], v[124:127], a[188:191]\n\t"
      "v_cvt_pk_bf16_f32 v167, v206, v207\n\t"
      "buffer_store_dwordx4 v[164:167], v236, s[52:55], 0 offen sc1\n\t"
      "s_waitcnt lgkmcnt(15)\n\t"
      "s_nop 0\n\t"
      "v_mfma_f32_16x16x32_bf16 a[160:163], v[16:19], a[0:3], a[160:163]\n\t"
      "ds_read_b64_tr_b16 v[12:13], v242 offset:41992\n\t"
      "v_accvgpr_read_b32 v192, a136\n\t"
      "v_accvgpr_read_b32 v193, a137\n\t"
      "v_mfma_f32_16x16x32_bf16 a[164:167], v[16:19], a[8:11], a[164:167]\n\t"
      "v_accvgpr_read_b32 v194, a138\n\t"
      "v_accvgpr_read_b32 v195, a139\n\t"
      "v_mfma_f32_16x16x32_bf16 a[168:171], v[16:19], a[16:19], a[168:171]\n\t"
      "ds_read_b64_tr_b16 v[14:15], v242 offset:46088\n\t"
      "v_accvgpr_read_b32 v196, a152\n\t"
      "v_accvgpr_read_b32 v197, a153\n\t"
      "v_mfma_f32_16x16x32_bf16 a[172:175], v[16:19], a[24:27], a[172:175]\n\t"
      "v_accvgpr_read_b32 v198, a154\n\t"
      "v_accvgpr_read_b32 v199, a155\n\t"
      "v_mfma_f32_16x16x32_bf16 a[176:179], v[20:23], a[0:3], a[176:179]\n\t"
      "ds_read_b64_tr_b16 v[16:17], v243 offset:33792\n\t"
      "v_cvt_pk_bf16_f32 v168, v192, v193\n\t"
      "v_cvt_pk_bf16_f32 v169, v194, v195\n\t"
      "v_mfma_f32_16x16x32_bf16 a[180:183], v[20:23], a[8:11], a[180:183]\n\t"
      "v_cvt_pk_bf16_f32 v170, v196, v197\n\t"
      "v_cvt_pk_bf16_f32 v171, v198, v199\n\t"
      "v_mfma_f32_16x16x32_bf16 a[184:187], v[20:23], a[16:19], a[184:187]\n\t"
      "ds_read_b64_tr_b16 v[18:19], v243 offset:37888\n\t"
      "buffer_store_dwordx4 v[168:171], v237, s[52:55], 0 offen sc1\n\t"
      "v_accvgpr_read_b32 v200, a140\n\t"
      "v_mfma_f32_16x16x32_bf16 a[188:191], v[20:23], a[24:27], a[188:191]\n\t"
      "v_accvgpr_read_b32 v201, a141\n\t"
      "v_accvgpr_read_b32 v202, a142\n\t"
      "v_mfma_f32_16x16x32_bf16 a[160:163], v[24:27], a[4:7], a[160:163]\n\t"
      "ds_read_b64_tr_b16 v[20:21], v243 offset:33800\n\t"
      "v_accvgpr_read_b32 v203, a143\n\t"
      "v_accvgpr_read_b32 v204, a156\n\t"
      "v_mfma_f32_16x16x32_bf16 a[164:167], v[24:27], a[12:15], a[164:167]\n\t"
      "v_accvgpr_read_b32 v205, a157\n\t"
      "v_accvgpr_read_b32 v206, a158\n\t"
      "v_mfma_f32_16x16x32_bf16 a[168:171], v[24:27], a[20:23], a[168:171]\n\t"
      "ds_read_b64_tr_b16 v[22:23], v243 offset:37896\n\t"
      "v_accvgpr_read_b32 v207, a159\n\t"
      "v_cvt_pk_bf16_f32 v172, v200, v201\n\t"
      "v_mfma_f32_16x16x32_bf16 a[172:175], v[24:27], a[28:31], a[172:175]\n\t"
      "v_cvt_pk_bf16_f32 v173, v202, v203\n\t"
      "v_cvt_pk_bf16_f32 v174, v204, v205\n\t"
      "v_mfma_f32_16x16x32_bf16 a[176:179], v[28:31], a[4:7], a[176:179]\n\t"
      "ds_read_b64_tr_b16 v[24:25], v243 offset:41984\n\t"
      "v_cvt_pk_bf16_f32 v175, v206, v207\n\t"
      "buffer_store_dwordx4 v[172:175], v238, s[52:55], 0 offen sc1\n\t"
      "v_mfma_f32_16x16x32_bf16 a[180:183], v[28:31], a[12:15], a[180:183]\n\t"
      "v_mfma_f32_16x16x32_bf16 a[184:187], v[28:31], a[20:23], a[184:187]\n\t"
      "ds_read_b64_tr_b16 v[26:27], v243 offset:46080\n\t"
      "v_mfma_f32_16x16x32_bf16 a[188:191], v[28:31], a[28:31], a[188:191]\n\t"
      "s_waitcnt lgkmcnt(15)\n\t"
      "s_nop 0\n\t"
      "v_mfma_f32_16x16x32_bf16 a[160:163], v[32:35], a[32:35], a[160:163]\n\t"
      "ds_read_b64_tr_b16 v[28:29], v243 offset:41992\n\t"
      "v_mfma_f32_16x16x32_bf16 a[164:167], v[32:35], a[40:43], a[164:167]\n\t"
      "v_mfma_f32_16x16x32_bf16 a[168:171], v[32:35], a[48:51], a[168:171]\n\t"
      "ds_read_b64_tr_b16 v[30:31], v243 offset:46088\n\t"
      "v_mfma_f32_16x16x32_bf16 a[172:175], v[32:35], a[56:59], a[172:175]\n\t"
      "v_mfma_f32_16x16x32_bf16 a[176:179], v[36:39], a[32:35], a[176:179]\n\t"
      "ds_read_b64_tr_b16 v[32:33], v242 offset:50176\n\t"
      "v_mfma_f32_16x16x32_bf16 a[180:183], v[36:39], a[40:43], a[180:183]\n\t"
      "v_mfma_f32_16x16x32_bf16 a[184:187], v[36:39], a[48:51], a[184:187]\n\t"
      "ds_read_b64_tr_b16 v[34:35], v242 offset:54272\n\t"
      "v_mfma_f32_16x16x32_bf16 a[188:191], v[36:39], a[56:59], a[188:191]\n\t"
      "v_mfma_f32_16x16x32_bf16 a[160:163], v[40:43], a[36:39], a[160:163]\n\t"
      "ds_read_b64_tr_b16 v[36:37], v242 offset:50184\n\t"
      "v_mfma_f32_16x16x32_bf16 a[164:167], v[40:43], a[44:47], a[164:167]\n\t"
      "v_mfma_f32_16x16x32_bf16 a[168:171], v[40:43], a[52:55], a[168:171]\n\t"
      "ds_read_b64_tr_b16 v[38:39], v242 offset:54280\n\t"
      "v_mfma_f32_16x16x32_bf16 a[172:175], v[40:43], a[60:63], a[172:175]\n\t"
      "v_mfma_f32_16x16x32_bf16 a[176:179], v[44:47], a[36:39], a[176:179]\n\t"
      "ds_read_b64_tr_b16 v[40:41], v242 offset:58368\n\t"
      "v_mfma_f32_16x16x32_bf16 a[180:183], v[44:47], a[44:47], a[180:183]\n\t"
      "v_mfma_f32_16x16x32_bf16 a[184:187], v[44:47], a[52:55], a[184:187]\n\t"
      "ds_read_b64_tr_b16 v[42:43], v242 offset:62464\n\t"
      "v_mfma_f32_16x16x32_bf16 a[188:191], v[44:47], a[60:63], a[188:191]\n\t"
      "s_waitcnt lgkmcnt(15)\n\t"
      "s_nop 0\n\t"
      "v_mfma_f32_16x16x32_bf16 a[160:163], v[48:51], a[64:67], a[160:163]\n\t"
      "ds_read_b64_tr_b16 v[44:45], v242 offset:58376\n\t"
      "v_mfma_f32_16x16x32_bf16 a[164:167], v[48:51], a[72:75], a[164:167]\n\t"
      "v_mfma_f32_16x16x32_bf16 a[168:171], v[48:51], a[80:83], a[168:171]\n\t"
      "ds_read_b64_tr_b16 v[46:47], v242 offset:62472\n\t"
      "v_mfma_f32_16x16x32_bf16 a[172:175], v[48:51], a[88:91], a[172:175]\n\t"
      "v_mfma_f32_16x16x32_bf16 a[176:179], v[52:55], a[64:67], a[176:179]\n\t"
      "ds_read_b64_tr_b16 v[48:49], v243 offset:50176\n\t"
      "v_mfma_f32_16x16x32_bf16 a[180:183], v[52:55], a[72:75], a[180:183]\n\t"
      "v_mfma_f32_16x16x32_bf16 a[184:187], v[52:55], a[80:83], a[184:187]\n\t"
      "ds_read_b64_tr_b16 v[50:51], v243 offset:54272\n\t"
      "v_mfma_f32_16x16x32_bf16 a[188:191], v[52:55], a[88:91], a[188:191]\n\t"
      "v_mfma_f32_16x16x32_bf16 a[160:163], v[56:59], a[68:71], a[160:163]\n\t"
      "ds_read_b64_tr_b16 v[52:53], v243 offset:50184\n\t"
      "v_mfma_f32_16x16x32_bf16 a[164:167], v[56:59], a[76:79], a[164:167]\n\t"
      "v_mfma_f32_16x16x32_bf16 a[168:171], v[56:59], a[84:87], a[168:171]\n\t"
      "ds_read_b64_tr_b16 v[54:55], v243 offset:54280\n\t"
      "v_mfma_f32_16x16x32_bf16 a[172:175], v[56:59], a[92:95], a[172:175]\n\t"
      "v_mfma_f32_16x16x32_bf16 a[176:179], v[60:63], a[68:71], a[176:179]\n\t"
      "ds_read_b64_tr_b16 v[56:57], v243 offset:58368\n\t"
      "v_mfma_f32_16x16x32_bf16 a[180:183], v[60:63], a[76:79], a[180:183]\n\t"
      "v_mfma_f32_16x16x32_bf16 a[184:187], v[60:63], a[84:87], a[184:187]\n\t"
      "ds_read_b64_tr_b16 v[58:59], v243 offset:62464\n\t"
      "v_mfma_f32_16x16x32_bf16 a[188:191], v[60:63], a[92:95], a[188:191]\n\t"
      "s_waitcnt lgkmcnt(15)\n\t"
      "s_nop 0\n\t"
      "v_mfma_f32_16x16x32_bf16 a[192:195], v[0:3], v[96:99], a[192:195]\n\t"
      "ds_read_b64_tr_b16 v[60:61], v243 offset:58376\n\t"
      "v_mfma_f32_16x16x32_bf16 a[196:199], v[0:3], v[104:107], a[196:199]\n\t"
      "v_mfma_f32_16x16x32_bf16 a[200:203], v[0:3], v[112:115], a[200:203]\n\t"
      "ds_read_b64_tr_b16 v[62:63], v243 offset:62472\n\t"
      "v_mfma_f32_16x16x32_bf16 a[204:207], v[0:3], v[120:123], a[204:207]\n\t"
      "v_accvgpr_read_b32 v192, a160\n\t"
      "v_accvgpr_read_b32 v193, a161\n\t"
      "v_mfma_f32_16x16x32_bf16 a[208:211], v[4:7], v[96:99], a[208:211]\n\t"
      "ds_read_b64_tr_b16 v[0:1], v242 offset:34304\n\t"
      "v_accvgpr_read_b32 v194, a162\n\t"
      "v_accvgpr_read_b32 v195, a163\n\t"
      "v_mfma_f32_16x16x32_bf16 a[212:215], v[4:7], v[104:107], a[212:215]\n\t"
      "v_accvgpr_read_b32 v196, a176\n\t"
      "v_accvgpr_read_b32 v197, a177\n\t"
      "v_mfma_f32_16x16x32_bf16 a[216:219], v[4:7], v[112:115], a[216:219]\n\t"
      "ds_read_b64_tr_b16 v[2:3], v242 offset:38400\n\t"
      "v_accvgpr_read_b32 v198, a178\n\t"
      "v_accvgpr_read_b32 v199, a179\n\t"
      "v_mfma_f32_16x16x32_bf16 a[220:223], v[4:7], v[120:123], a[220:223]\n\t"
      "v_cvt_pk_bf16_f32 v176, v192, v193\n\t"
      "v_cvt_pk_bf16_f32 v177, v194, v195\n\t"
      "v_mfma_f32_16x16x32_bf16 a[192:195], v[8:11], v[100:103], a[192:195]\n\t"
      "ds_read_b64_tr_b16 v[4:5], v242 offset:34312\n\t"
      "v_cvt_pk_bf16_f32 v178, v196, v197\n\t"
      "v_cvt_pk_bf16_f32 v179, v198, v199\n\t"
      "v_mfma_f32_16x16x32_bf16 a[196:199], v[8:11], v[108:111], a[196:199]\n\t"
      "buffer_store_dwordx4 v[176:179], v235, s[52:55], 0 offen offset:1024 sc1\n\t"
      "v_accvgpr_read_b32 v200, a164\n\t"
      "v_mfma_f32_16x16x32_bf16 a[200:203], v[8:11], v[116:119], a[200:203]\n\t"
      "ds_read_b64_tr_b16 v[6:7], v242 offset:38408\n\t"
      "v_accvgpr_read_b32 v201, a165\n\t"
      "v_accvgpr_read_b32 v202, a166\n\t"
      "v_mfma_f32_16x16x32_bf16 a[204:207], v[8:11], v[124:127], a[204:207]\n\t"
      "v_accvgpr_read_b32 v203, a167\n\t"
      "v_accvgpr_read_b32 v204, a180\n\t"
      "v_mfma_f32_16x16x32_bf16 a[208:211], v[12:15], v[100:103], a[208:211]\n\t"
      "ds_read_b64_tr_b16 v[8:9], v242 offset:42496\n\t"
      "v_accvgpr_read_b32 v205, a181\n\t"
      "v_accvgpr_read_b32 v206, a182\n\t"
      "v_mfma_f32_16x16x32_bf16 a[212:215], v[12:15], v[108:111], a[212:215]\n\t"
      "v_accvgpr_read_b32 v207, a183\n\t"
      "v_cvt_pk_bf16_f32 v180, v200, v201\n\t"
      "v_mfma_f32_16x16x32_bf16 a[216:219], v[12:15], v[116:119], a[216:219]\n\t"
      "ds_read_b64_tr_b16 v[10:11], v242 offset:46592\n\t"
      "v_cvt_pk_bf16_f32 v181, v202, v203\n\t"
      "v_cvt_pk_bf16_f32 v182, v204, v205\n\t"
      "v_mfma_f32_16x16x32_bf16 a[220:223], v[12:15], v[124:127], a[220:223]\n\t"
      "v_cvt_pk_bf16_f32 v183, v206, v207\n\t"
      "buffer_store_dwordx4 v[180:183], v236, s[52:55], 0 offen offset:1024 sc1\n\t"
      "s_waitcnt lgkmcnt(15)\n\t"
      "s_nop 0\n\t"
      "v_mfma_f32_16x16x32_bf16 a[192:195], v[16:19], a[0:3], a[192:195]\n\t"
      "ds_read_b64_tr_b16 v[12:13], v242 offset:42504\n\t"
      "v_accvgpr_read_b32 v192, a168\n\t"
      "v_accvgpr_read_b32 v193, a169\n\t"
      "v_mfma_f32_16x16x32_bf16 a[196:199], v[16:19], a[8:11], a[196:199]\n\t"
      "v_accvgpr_read_b32 v194, a170\n\t"
      "v_accvgpr_read_b32 v195, a171\n\t"
      "v_mfma_f32_16x16x32_bf16 a[200:203], v[16:19], a[16:19], a[200:203]\n\t"
      "ds_read_b64_tr_b16 v[14:15], v242 offset:46600\n\t"
      "v_accvgpr_read_b32 v196, a184\n\t"
      "v_accvgpr_read_b32 v197, a185\n\t"
      "v_mfma_f32_16x16x32_bf16 a[204:207], v[16:19], a[24:27], a[204:207]\n\t"
      "v_accvgpr_read_b32 v198, a186\n\t"
      "v_accvgpr_read_b32 v199, a187\n\t"
      "v_mfma_f32_16x16x32_bf16 a[208:211], v[20:23], a[0:3], a[208:211]\n\t"
      "ds_read_b64_tr_b16 v[16:17], v243 offset:34304\n\t"
      "v_cvt_pk_bf16_f32 v184, v192, v193\n\t"
      "v_cvt_pk_bf16_f32 v185, v194, v195\n\t"
      "v_mfma_f32_16x16x32_bf16 a[212:215], v[20:23], a[8:11], a[212:215]\n\t"
      "v_cvt_pk_bf16_f32 v186, v196, v197\n\t"
      "v_cvt_pk_bf16_f32 v187, v198, v199\n\t"
      "v_mfma_f32_16x16x32_bf16 a[216:219], v[20:23], a[16:19], a[216:219]\n\t"
      "ds_read_b64_tr_b16 v[18:19], v243 offset:38400\n\t"
      "buffer_store_dwordx4 v[184:187], v237, s[52:55], 0 offen offset:1024 sc1\n\t"
      "v_accvgpr_read_b32 v200, a172\n\t"
      "v_mfma_f32_16x16x32_bf16 a[220:223], v[20:23], a[24:27], a[220:223]\n\t"
      "v_accvgpr_read_b32 v201, a173\n\t"
      "v_accvgpr_read_b32 v202, a174\n\t"
      "v_mfma_f32_16x16x32_bf16 a[192:195], v[24:27], a[4:7], a[192:195]\n\t"
      "ds_read_b64_tr_b16 v[20:21], v243 offset:34312\n\t"
      "v_accvgpr_read_b32 v203, a175\n\t"
      "v_accvgpr_read_b32 v204, a188\n\t"
      "v_mfma_f32_16x16x32_bf16 a[196:199], v[24:27], a[12:15], a[196:199]\n\t"
      "v_accvgpr_read_b32 v205, a189\n\t"
      "v_accvgpr_read_b32 v206, a190\n\t"
      "v_mfma_f32_16x16x32_bf16 a[200:203], v[24:27], a[20:23], a[200:203]\n\t"
      "ds_read_b64_tr_b16 v[22:23], v243 offset:38408\n\t"
      "v_accvgpr_read_b32 v207, a191\n\t"
      "v_cvt_pk_bf16_f32 v188, v200, v201\n\t"
      "v_mfma_f32_16x16x32_bf16 a[204:207], v[24:27], a[28:31], a[204:207]\n\t"
      "v_cvt_pk_bf16_f32 v189, v202, v203\n\t"
      "v_cvt_pk_bf16_f32 v190, v204, v205\n\t"
      "v_mfma_f32_16x16x32_bf16 a[208:211], v[28:31], a[4:7], a[208:211]\n\t"
      "ds_read_b64_tr_b16 v[24:25], v243 offset:42496\n\t"
      "v_cvt_pk_bf16_f32 v191, v206, v207\n\t"
      "buffer_store_dwordx4 v[188:191], v238, s[52:55], 0 offen offset:1024 sc1\n\t"
      "v_mfma_f32_16x16x32_bf16 a[212:215], v[28:31], a[12:15], a[212:215]\n\t"
      "v_mfma_f32_16x16x32_bf16 a[216:219], v[28:31], a[20:23], a[216:219]\n\t"
      "ds_read_b64_tr_b16 v[26:27], v243 offset:46592\n\t"
      "v_mfma_f32_16x16x32_bf16 a[220:223], v[28:31], a[28:31], a[220:223]\n\t"
      "s_waitcnt lgkmcnt(15)\n\t"
      "s_nop 0\n\t"
      "v_mfma_f32_16x16x32_bf16 a[192:195], v[32:35], a[32:35], a[192:195]\n\t"
      "ds_read_b64_tr_b16 v[28:29], v243 offset:42504\n\t"
      "v_mfma_f32_16x16x32_bf16 a[196:199], v[32:35], a[40:43], a[196:199]\n\t"
      "v_mfma_f32_16x16x32_bf16 a[200:203], v[32:35], a[48:51], a[200:203]\n\t"
      "ds_read_b64_tr_b16 v[30:31], v243 offset:46600\n\t"
      "v_mfma_f32_16x16x32_bf16 a[204:207], v[32:35], a[56:59], a[204:207]\n\t"
      "v_mfma_f32_16x16x32_bf16 a[208:211], v[36:39], a[32:35], a[208:211]\n\t"
      "ds_read_b64_tr_b16 v[32:33], v242 offset:50688\n\t"
      "v_mfma_f32_16x16x32_bf16 a[212:215], v[36:39], a[40:43], a[212:215]\n\t"
      "v_mfma_f32_16x16x32_bf16 a[216:219], v[36:39], a[48:51], a[216:219]\n\t"
      "ds_read_b64_tr_b16 v[34:35], v242 offset:54784\n\t"
      "v_mfma_f32_16x16x32_bf16 a[220:223], v[36:39], a[56:59], a[220:223]\n\t"
      "v_mfma_f32_16x16x32_bf16 a[192:195], v[40:43], a[36:39], a[192:195]\n\t"
      "ds_read_b64_tr_b16 v[36:37], v242 offset:50696\n\t"
      "v_mfma_f32_16x16x32_bf16 a[196:199], v[40:43], a[44:47], a[196:199]\n\t"
      "v_mfma_f32_16x16x32_bf16 a[200:203], v[40:43], a[52:55], a[200:203]\n\t"
      "ds_read_b64_tr_b16 v[38:39], v242 offset:54792\n\t"
      "v_mfma_f32_16x16x32_bf16 a[204:207], v[40:43], a[60:63], a[204:207]\n\t"
      "v_mfma_f32_16x16x32_bf16 a[208:211], v[44:47], a[36:39], a[208:211]\n\t"
      "ds_read_b64_tr_b16 v[40:41], v242 offset:58880\n\t"
      "v_mfma_f32_16x16x32_bf16 a[212:215], v[44:47], a[44:47], a[212:215]\n\t"
      "v_mfma_f32_16x16x32_bf16 a[216:219], v[44:47], a[52:55], a[216:219]\n\t"
      "ds_read_b64_tr_b16 v[42:43], v242 offset:62976\n\t"
      "v_mfma_f32_16x16x32_bf16 a[220:223], v[44:47], a[60:63], a[220:223]\n\t"
      "s_waitcnt lgkmcnt(15)\n\t"
      "s_nop 0\n\t"
      "v_mfma_f32_16x16x32_bf16 a[192:195], v[48:51], a[64:67], a[192:195]\n\t"
      "ds_read_b64_tr_b16 v[44:45], v242 offset:58888\n\t"
      "v_mfma_f32_16x16x32_bf16 a[196:199], v[48:51], a[72:75], a[196:199]\n\t"
      "v_mfma_f32_16x16x32_bf16 a[200:203], v[48:51], a[80:83], a[200:203]\n\t"
      "ds_read_b64_tr_b16 v[46:47], v242 offset:62984\n\t"
      "v_mfma_f32_16x16x32_bf16 a[204:207], v[48:51], a[88:91], a[204:207]\n\t"
      "v_mfma_f32_16x16x32_bf16 a[208:211], v[52:55], a[64:67], a[208:211]\n\t"
      "ds_read_b64_tr_b16 v[48:49], v243 offset:50688\n\t"
      "v_mfma_f32_16x16x32_bf16 a[212:215], v[52:55], a[72:75], a[212:215]\n\t"
      "v_mfma_f32_16x16x32_bf16 a[216:219], v[52:55], a[80:83], a[216:219]\n\t"
      "ds_read_b64_tr_b16 v[50:51], v243 offset:54784\n\t"
      "v_mfma_f32_16x16x32_bf16 a[220:223], v[52:55], a[88:91], a[220:223]\n\t"
      "v_mfma_f32_16x16x32_bf16 a[192:195], v[56:59], a[68:71], a[192:195]\n\t"
      "ds_read_b64_tr_b16 v[52:53], v243 offset:50696\n\t"
      "v_mfma_f32_16x16x32_bf16 a[196:199], v[56:59], a[76:79], a[196:199]\n\t"
      "v_mfma_f32_16x16x32_bf16 a[200:203], v[56:59], a[84:87], a[200:203]\n\t"
      "ds_read_b64_tr_b16 v[54:55], v243 offset:54792\n\t"
      "v_mfma_f32_16x16x32_bf16 a[204:207], v[56:59], a[92:95], a[204:207]\n\t"
      "v_mfma_f32_16x16x32_bf16 a[208:211], v[60:63], a[68:71], a[208:211]\n\t"
      "ds_read_b64_tr_b16 v[56:57], v243 offset:58880\n\t"
      "v_mfma_f32_16x16x32_bf16 a[212:215], v[60:63], a[76:79], a[212:215]\n\t"
      "v_mfma_f32_16x16x32_bf16 a[216:219], v[60:63], a[84:87], a[216:219]\n\t"
      "ds_read_b64_tr_b16 v[58:59], v243 offset:62976\n\t"
      "v_mfma_f32_16x16x32_bf16 a[220:223], v[60:63], a[92:95], a[220:223]\n\t"
      "s_waitcnt lgkmcnt(15)\n\t"
      "s_nop 0\n\t"
      "v_mfma_f32_16x16x32_bf16 a[224:227], v[0:3], v[96:99], a[224:227]\n\t"
      "ds_read_b64_tr_b16 v[60:61], v243 offset:58888\n\t"
      "v_mfma_f32_16x16x32_bf16 a[228:231], v[0:3], v[104:107], a[228:231]\n\t"
      "v_mfma_f32_16x16x32_bf16 a[232:235], v[0:3], v[112:115], a[232:235]\n\t"
      "ds_read_b64_tr_b16 v[62:63], v243 offset:62984\n\t"
      "v_mfma_f32_16x16x32_bf16 a[236:239], v[0:3], v[120:123], a[236:239]\n\t"
      "v_accvgpr_read_b32 v192, a192\n\t"
      "v_accvgpr_read_b32 v193, a193\n\t"
      "v_mfma_f32_16x16x32_bf16 a[240:243], v[4:7], v[96:99], a[240:243]\n\t"
      "v_accvgpr_read_b32 v194, a194\n\t"
      "v_accvgpr_read_b32 v195, a195\n\t"
      "v_mfma_f32_16x16x32_bf16 a[244:247], v[4:7], v[104:107], a[244:247]\n\t"
      "v_accvgpr_read_b32 v196, a208\n\t"
      "v_accvgpr_read_b32 v197, a209\n\t"
      "v_mfma_f32_16x16x32_bf16 a[248:251], v[4:7], v[112:115], a[248:251]\n\t"
      "v_accvgpr_read_b32 v198, a210\n\t"
      "v_accvgpr_read_b32 v199, a211\n\t"
      "v_mfma_f32_16x16x32_bf16 a[252:255], v[4:7], v[120:123], a[252:255]\n\t"
      "v_cvt_pk_bf16_f32 v160, v192, v193\n\t"
      "v_cvt_pk_bf16_f32 v161, v194, v195\n\t"
      "v_mfma_f32_16x16x32_bf16 a[224:227], v[8:11], v[100:103], a[224:227]\n\t"
      "v_cvt_pk_bf16_f32 v162, v196, v197\n\t"
      "v_cvt_pk_bf16_f32 v163, v198, v199\n\t"
      "v_mfma_f32_16x16x32_bf16 a[228:231], v[8:11], v[108:111], a[228:231]\n\t"
      "buffer_store_dwordx4 v[160:163], v235, s[52:55], 0 offen offset:2048 sc1\n\t"
      "v_accvgpr_read_b32 v200, a196\n\t"
      "v_mfma_f32_16x16x32_bf16 a[232:235], v[8:11], v[116:119], a[232:235]\n\t"
      "v_accvgpr_read_b32 v201, a197\n\t"
      "v_accvgpr_read_b32 v202, a198\n\t"
      "v_mfma_f32_16x16x32_bf16 a[236:239], v[8:11], v[124:127], a[236:239]\n\t"
      "v_accvgpr_read_b32 v203, a199\n\t"
      "v_accvgpr_read_b32 v204, a212\n\t"
      "v_mfma_f32_16x16x32_bf16 a[240:243], v[12:15], v[100:103], a[240:243]\n\t"
      "v_accvgpr_read_b32 v205, a213\n\t"
      "v_accvgpr_read_b32 v206, a214\n\t"
      "v_mfma_f32_16x16x32_bf16 a[244:247], v[12:15], v[108:111], a[244:247]\n\t"
      "v_accvgpr_read_b32 v207, a215\n\t"
      "v_cvt_pk_bf16_f32 v164, v200, v201\n\t"
      "v_mfma_f32_16x16x32_bf16 a[248:251], v[12:15], v[116:119], a[248:251]\n\t"
      "v_cvt_pk_bf16_f32 v165, v202, v203\n\t"
      "v_cvt_pk_bf16_f32 v166, v204, v205\n\t"
      "v_mfma_f32_16x16x32_bf16 a[252:255], v[12:15], v[124:127], a[252:255]\n\t"
      "v_cvt_pk_bf16_f32 v167, v206, v207\n\t"
      "buffer_store_dwordx4 v[164:167], v236, s[52:55], 0 offen offset:2048 sc1\n\t"
      "s_waitcnt lgkmcnt(15)\n\t"
      "s_nop 0\n\t"
      "v_mfma_f32_16x16x32_bf16 a[224:227], v[16:19], a[0:3], a[224:227]\n\t"
      "v_accvgpr_read_b32 v192, a200\n\t"
      "v_accvgpr_read_b32 v193, a201\n\t"
      "v_mfma_f32_16x16x32_bf16 a[228:231], v[16:19], a[8:11], a[228:231]\n\t"
      "v_accvgpr_read_b32 v194, a202\n\t"
      "v_accvgpr_read_b32 v195, a203\n\t"
      "v_mfma_f32_16x16x32_bf16 a[232:235], v[16:19], a[16:19], a[232:235]\n\t"
      "v_accvgpr_read_b32 v196, a216\n\t"
      "v_accvgpr_read_b32 v197, a217\n\t"
      "v_mfma_f32_16x16x32_bf16 a[236:239], v[16:19], a[24:27], a[236:239]\n\t"
      "v_accvgpr_read_b32 v198, a218\n\t"
      "v_accvgpr_read_b32 v199, a219\n\t"
      "v_mfma_f32_16x16x32_bf16 a[240:243], v[20:23], a[0:3], a[240:243]\n\t"
      "v_cvt_pk_bf16_f32 v168, v192, v193\n\t"
      "v_cvt_pk_bf16_f32 v169, v194, v195\n\t"
      "v_mfma_f32_16x16x32_bf16 a[244:247], v[20:23], a[8:11], a[244:247]\n\t"
      "v_cvt_pk_bf16_f32 v170, v196, v197\n\t"
      "v_cvt_pk_bf16_f32 v171, v198, v199\n\t"
      "v_mfma_f32_16x16x32_bf16 a[248:251], v[20:23], a[16:19], a[248:251]\n\t"
      "buffer_store_dwordx4 v[168:171], v237, s[52:55], 0 offen offset:2048 sc1\n\t"
      "v_accvgpr_read_b32 v200, a204\n\t"
      "v_mfma_f32_16x16x32_bf16 a[252:255], v[20:23], a[24:27], a[252:255]\n\t"
      "v_accvgpr_read_b32 v201, a205\n\t"
      "v_accvgpr_read_b32 v202, a206\n\t"
      "v_mfma_f32_16x16x32_bf16 a[224:227], v[24:27], a[4:7], a[224:227]\n\t"
      "v_accvgpr_read_b32 v203, a207\n\t"
      "v_accvgpr_read_b32 v204, a220\n\t"
      "v_mfma_f32_16x16x32_bf16 a[228:231], v[24:27], a[12:15], a[228:231]\n\t"
      "v_accvgpr_read_b32 v205, a221\n\t"
      "v_accvgpr_read_b32 v206, a222\n\t"
      "v_mfma_f32_16x16x32_bf16 a[232:235], v[24:27], a[20:23], a[232:235]\n\t"
      "v_accvgpr_read_b32 v207, a223\n\t"
      "v_cvt_pk_bf16_f32 v172, v200, v201\n\t"
      "v_mfma_f32_16x16x32_bf16 a[236:239], v[24:27], a[28:31], a[236:239]\n\t"
      "v_cvt_pk_bf16_f32 v173, v202, v203\n\t"
      "v_cvt_pk_bf16_f32 v174, v204, v205\n\t"
      "v_mfma_f32_16x16x32_bf16 a[240:243], v[28:31], a[4:7], a[240:243]\n\t"
      "v_cvt_pk_bf16_f32 v175, v206, v207\n\t"
      "buffer_store_dwordx4 v[172:175], v238, s[52:55], 0 offen offset:2048 sc1\n\t"
      "v_mfma_f32_16x16x32_bf16 a[244:247], v[28:31], a[12:15], a[244:247]\n\t"
      "v_mfma_f32_16x16x32_bf16 a[248:251], v[28:31], a[20:23], a[248:251]\n\t"
      "v_mfma_f32_16x16x32_bf16 a[252:255], v[28:31], a[28:31], a[252:255]\n\t"
      "s_waitcnt lgkmcnt(8)\n\t"
      "s_nop 0\n\t"
      "v_mfma_f32_16x16x32_bf16 a[224:227], v[32:35], a[32:35], a[224:227]\n\t"
      "v_mfma_f32_16x16x32_bf16 a[228:231], v[32:35], a[40:43], a[228:231]\n\t"
      "v_mfma_f32_16x16x32_bf16 a[232:235], v[32:35], a[48:51], a[232:235]\n\t"
      "v_mfma_f32_16x16x32_bf16 a[236:239], v[32:35], a[56:59], a[236:239]\n\t"
      "v_mfma_f32_16x16x32_bf16 a[240:243], v[36:39], a[32:35], a[240:243]\n\t"
      "v_mfma_f32_16x16x32_bf16 a[244:247], v[36:39], a[40:43], a[244:247]\n\t"
      "v_mfma_f32_16x16x32_bf16 a[248:251], v[36:39], a[48:51], a[248:251]\n\t"
      "v_mfma_f32_16x16x32_bf16 a[252:255], v[36:39], a[56:59], a[252:255]\n\t"
      "v_mfma_f32_16x16x32_bf16 a[224:227], v[40:43], a[36:39], a[224:227]\n\t"
      "v_mfma_f32_16x16x32_bf16 a[228:231], v[40:43], a[44:47], a[228:231]\n\t"
      "v_mfma_f32_16x16x32_bf16 a[232:235], v[40:43], a[52:55], a[232:235]\n\t"
      "v_mfma_f32_16x16x32_bf16 a[236:239], v[40:43], a[60:63], a[236:239]\n\t"
      "v_mfma_f32_16x16x32_bf16 a[240:243], v[44:47], a[36:39], a[240:243]\n\t"
      "v_mfma_f32_16x16x32_bf16 a[244:247], v[44:47], a[44:47], a[244:247]\n\t"
      "v_mfma_f32_16x16x32_bf16 a[248:251], v[44:47], a[52:55], a[248:251]\n\t"
      "v_mfma_f32_16x16x32_bf16 a[252:255], v[44:47], a[60:63], a[252:255]\n\t"
      "s_waitcnt lgkmcnt(0)\n\t"
      "s_nop 0\n\t"
      "v_mfma_f32_16x16x32_bf16 a[224:227], v[48:51], a[64:67], a[224:227]\n\t"
      "v_mfma_f32_16x16x32_bf16 a[228:231], v[48:51], a[72:75], a[228:231]\n\t"
      "v_mfma_f32_16x16x32_bf16 a[232:235], v[48:51], a[80:83], a[232:235]\n\t"
      "v_mfma_f32_16x16x32_bf16 a[236:239], v[48:51], a[88:91], a[236:239]\n\t"
      "v_mfma_f32_16x16x32_bf16 a[240:243], v[52:55], a[64:67], a[240:243]\n\t"
      "v_mfma_f32_16x16x32_bf16 a[244:247], v[52:55], a[72:75], a[244:247]\n\t"
      "v_mfma_f32_16x16x32_bf16 a[248:251], v[52:55], a[80:83], a[248:251]\n\t"
      "v_mfma_f32_16x16x32_bf16 a[252:255], v[52:55], a[88:91], a[252:255]\n\t"
      "v_mfma_f32_16x16x32_bf16 a[224:227], v[56:59], a[68:71], a[224:227]\n\t"
      "v_mfma_f32_16x16x32_bf16 a[228:231], v[56:59], a[76:79], a[228:231]\n\t"
      "v_mfma_f32_16x16x32_bf16 a[232:235], v[56:59], a[84:87], a[232:235]\n\t"
      "v_mfma_f32_16x16x32_bf16 a[236:239], v[56:59], a[92:95], a[236:239]\n\t"
      "v_mfma_f32_16x16x32_bf16 a[240:243], v[60:63], a[68:71], a[240:243]\n\t"
      "v_mfma_f32_16x16x32_bf16 a[244:247], v[60:63], a[76:79], a[244:247]\n\t"
      "v_mfma_f32_16x16x32_bf16 a[248:251], v[60:63], a[84:87], a[248:251]\n\t"
      "v_mfma_f32_16x16x32_bf16 a[252:255], v[60:63], a[92:95], a[252:255]\n\t"
      "s_nop 7\n\t"
      "s_nop 0\n\t"
      "v_accvgpr_read_b32 v192, a224\n\t"
      "v_accvgpr_read_b32 v193, a225\n\t"
      "v_accvgpr_read_b32 v194, a226\n\t"
      "v_accvgpr_read_b32 v195, a227\n\t"
      "v_accvgpr_read_b32 v196, a240\n\t"
      "v_accvgpr_read_b32 v197, a241\n\t"
      "v_accvgpr_read_b32 v198, a242\n\t"
      "v_accvgpr_read_b32 v199, a243\n\t"
      "v_cvt_pk_bf16_f32 v176, v192, v193\n\t"
      "v_cvt_pk_bf16_f32 v177, v194, v195\n\t"
      "v_cvt_pk_bf16_f32 v178, v196, v197\n\t"
      "v_cvt_pk_bf16_f32 v179, v198, v199\n\t"
      "buffer_store_dwordx4 v[176:179], v235, s[52:55], 0 offen offset:3072 sc1\n\t"
      "v_accvgpr_read_b32 v200, a228\n\t"
      "v_accvgpr_read_b32 v201, a229\n\t"
      "v_accvgpr_read_b32 v202, a230\n\t"
      "v_accvgpr_read_b32 v203, a231\n\t"
      "v_accvgpr_read_b32 v204, a244\n\t"
      "v_accvgpr_read_b32 v205, a245\n\t"
      "v_accvgpr_read_b32 v206, a246\n\t"
      "v_accvgpr_read_b32 v207, a247\n\t"
      "v_cvt_pk_bf16_f32 v180, v200, v201\n\t"
      "v_cvt_pk_bf16_f32 v181, v202, v203\n\t"
      "v_cvt_pk_bf16_f32 v182, v204, v205\n\t"
      "v_cvt_pk_bf16_f32 v183, v206, v207\n\t"
      "buffer_store_dwordx4 v[180:183], v236, s[52:55], 0 offen offset:3072 sc1\n\t"
      "v_accvgpr_read_b32 v192, a232\n\t"
      "v_accvgpr_read_b32 v193, a233\n\t"
      "v_accvgpr_read_b32 v194, a234\n\t"
      "v_accvgpr_read_b32 v195, a235\n\t"
      "v_accvgpr_read_b32 v196, a248\n\t"
      "v_accvgpr_read_b32 v197, a249\n\t"
      "v_accvgpr_read_b32 v198, a250\n\t"
      "v_accvgpr_read_b32 v199, a251\n\t"
      "v_cvt_pk_bf16_f32 v184, v192, v193\n\t"
      "v_cvt_pk_bf16_f32 v185, v194, v195\n\t"
      "v_cvt_pk_bf16_f32 v186, v196, v197\n\t"
      "v_cvt_pk_bf16_f32 v187, v198, v199\n\t"
      "buffer_store_dwordx4 v[184:187], v237, s[52:55], 0 offen offset:3072 sc1\n\t"
      "v_accvgpr_read_b32 v200, a236\n\t"
      "v_accvgpr_read_b32 v201, a237\n\t"
      "v_accvgpr_read_b32 v202, a238\n\t"
      "v_accvgpr_read_b32 v203, a239\n\t"
      "v_accvgpr_read_b32 v204, a252\n\t"
      "v_accvgpr_read_b32 v205, a253\n\t"
      "v_accvgpr_read_b32 v206, a254\n\t"
      "v_accvgpr_read_b32 v207, a255\n\t"
      "v_cvt_pk_bf16_f32 v188, v200, v201\n\t"
      "v_cvt_pk_bf16_f32 v189, v202, v203\n\t"
      "v_cvt_pk_bf16_f32 v190, v204, v205\n\t"
      "v_cvt_pk_bf16_f32 v191, v206, v207\n\t"
      "buffer_store_dwordx4 v[188:191], v238, s[52:55], 0 offen offset:3072 sc1\n\t"
      "s_waitcnt vmcnt(0)\n\t"
      "v_mov_b32_e32 %0, v251\n\t"
      "s_nop 1\n\t"
      : "+v"(tid)
      : "s"(ws), "s"(wq), "s"(K), "s"(V), "s"(wo), "s"(__builtin_amdgcn_readfirstlane(blockIdx.x)), "s"(wg), "s"(wu), "s"(wd), "s"(tag)
      : "v0", "v1", "v2", "v3", "v4", "v5", "v6", "v7", "v8", "v9", "v10", "v11", "v12", "v13", "v14", "v15", "v16", "v17", "v18", "v19", "v20", "v21", "v22", "v23", "v24", "v25", "v26", "v27", "v28", "v29", "v30", "v31", "v32", "v33", "v34", "v35", "v36", "v37", "v38", "v39", "v40", "v41", "v42", "v43", "v44", "v45", "v46", "v47", "v48", "v49", "v50", "v51", "v52", "v53", "v54", "v55", "v56", "v57", "v58", "v59", "v60", "v61", "v62", "v63", "v64", "v65", "v66", "v67", "v68", "v69", "v70", "v71", "v72", "v73", "v74", "v75", "v76", "v77", "v78", "v79", "v80", "v81", "v82", "v83", "v84", "v85", "v86", "v87", "v88", "v89", "v90", "v91", "v92", "v93", "v94", "v95", "v96", "v97", "v98", "v99", "v100", "v101", "v102", "v103", "v104", "v105", "v106", "v107", "v108", "v109", "v110", "v111", "v112", "v113", "v114", "v115", "v116", "v117", "v118", "v119", "v120", "v121", "v122", "v123", "v124", "v125", "v126", "v127", "v128", "v129", "v130", "v131", "v132", "v133", "v134", "v135", "v136", "v137", "v138", "v139", "v140", "v141", "v142", "v143", "v144", "v145", "v146", "v147", "v148", "v149", "v150", "v151", "v152", "v153", "v154", "v155", "v156", "v157", "v158", "v159", "v160", "v161", "v162", "v163", "v164", "v165", "v166", "v167", "v168", "v169", "v170", "v171", "v172", "v173", "v174", "v175", "v176", "v177", "v178", "v179", "v180", "v181", "v182", "v183", "v184", "v185", "v186", "v187", "v188", "v189", "v190", "v191", "v192", "v193", "v194", "v195", "v196", "v197", "v198", "v199", "v200", "v201", "v202", "v203", "v204", "v205", "v206", "v207", "v208", "v209", "v210", "v211", "v212", "v213", "v214", "v215", "v216", "v217", "v218", "v219", "v220", "v221", "v222", "v223", "v224", "v225", "v226", "v227", "v228", "v229", "v230", "v231", "v232", "v233", "v234", "v235", "v236", "v237", "v238", "v239", "v240", "v241", "v242", "v243", "v244", "v245", "v246", "v247", "v248", "v249", "v250", "v251", "v252", "a0", "a1", "a2", "a3", "a4", "a5", "a6", "a7", "a8", "a9", "a10", "a11", "a12", "a13", "a14", "a15", "a16", "a17", "a18", "a19", "a20", "a21", "a22", "a23", "a24", "a25", "a26", "a27", "a28", "a29", "a30", "a31", "a32", "a33", "a34", "a35", "a36", "a37", "a38", "a39", "a40", "a41", "a42", "a43", "a44", "a45", "a46", "a47", "a48", "a49", "a50", "a51", "a52", "a53", "a54", "a55", "a56", "a57", "a58", "a59", "a60", "a61", "a62", "a63", "a64", "a65", "a66", "a67", "a68", "a69", "a70", "a71", "a72", "a73", "a74", "a75", "a76", "a77", "a78", "a79", "a80", "a81", "a82", "a83", "a84", "a85", "a86", "a87", "a88", "a89", "a90", "a91", "a92", "a93", "a94", "a95", "a96", "a97", "a98", "a99", "a100", "a101", "a102", "a103", "a104", "a105", "a106", "a107", "a108", "a109", "a110", "a111", "a112", "a113", "a114", "a115", "a116", "a117", "a118", "a119", "a120", "a121", "a122", "a123", "a124", "a125", "a126", "a127", "a128", "a129", "a130", "a131", "a132", "a133", "a134", "a135", "a136", "a137", "a138", "a139", "a140", "a141", "a142", "a143", "a144", "a145", "a146", "a147", "a148", "a149", "a150", "a151", "a152", "a153", "a154", "a155", "a156", "a157", "a158", "a159", "a160", "a161", "a162", "a163", "a164", "a165", "a166", "a167", "a168", "a169", "a170", "a171", "a172", "a173", "a174", "a175", "a176", "a177", "a178", "a179", "a180", "a181", "a182", "a183", "a184", "a185", "a186", "a187", "a188", "a189", "a190", "a191", "a192", "a193", "a194", "a195", "a196", "a197", "a198", "a199", "a200", "a201", "a202", "a203", "a204", "a205", "a206", "a207", "a208", "a209", "a210", "a211", "a212", "a213", "a214", "a215", "a216", "a217", "a218", "a219", "a220", "a221", "a222", "a223", "a224", "a225", "a226", "a227", "a228", "a229", "a230", "a231", "a232", "a233", "a234", "a235", "a236", "a237", "a238", "a239", "a240", "a241", "a242", "a243", "a244", "a245", "a246", "a247", "a248", "a249", "a250", "a251", "a252", "a253", "a254", "a255", "s36", "s37", "s38", "s39", "s40", "s41", "s42", "s43", "s44", "s45", "s46", "s47", "s48", "s49", "s50", "s51", "s52", "s53", "s54", "s55", "s56", "s57", "s58", "s59", "s60", "s61", "s62", "s63", "s64", "s65", "s66", "s67", "s68", "s69", "s70", "s71", "s72", "s73", "s74", "s75", "s76", "s77", "s78", "s79", "s80", "s81", "s82", "s83", "s84", "s85", "s86", "s87", "s88", "s89", "s90", "s91", "s92", "s93", "s94", "s95", "s96", "s97", "s98", "s99", "vcc", "scc", "memory");
  gu32* fl = (gu32*)(ws + 16384);
  u16* x1o = (u16*)(ws + (3 << 20)); u16* h2o = (u16*)(ws + (5 << 20)); u16* Pf = (u16*)(ws + (8 << 20));
  TL_BEGIN
  __syncthreads();
  const int b = blockIdx.x, xg = b & 7, j = b >> 3, qblk = j >> 2;
  gu32* fl1 = fl + 512 + qblk * 32;
  gu32* rdy = fl + 1024;
  if (tid == 0) __hip_atomic_fetch_add(fl1, 1u, RLX_AGENT);
  TLP(0);
  float* sc = (float*)(lds + 131072);
  const int lane = tid & 63, wid = tid >> 6, q8 = tid & 7, ch = tid >> 3;
  const int row = qblk * 256 + (xg * 4 + (j & 3)) * 8 + q8, Qb = row >> 4, q = row & 15;
  const bool is_down = j >= 28;
  const __amdgpu_buffer_rsrc_t rM = mkrsrc(ws, 131072u);
  u32x4 xv[2], gav[2], sbv[2], cbv[2], gbv[2], wv[2];
#pragma unroll
  for (int c = 0; c < 2; ++c) {
    const int cidx = ch + 32 * c;
    xv[c] = *(const u32x4*)(x + (size_t)row * D + 8 * cidx);
    gav[c] = ld16_sc1(rM, (1024 + 8 * cidx) * 2u); sbv[c] = ld16_sc1(rM, (1536 + 8 * cidx) * 2u);
    cbv[c] = ld16_sc1(rM, (2048 + 8 * cidx) * 2u); gbv[c] = ld16_sc1(rM, (2560 + 8 * cidx) * 2u);
    wv[c] = *(const u32x4*)(wr + 8 * cidx);
  }
  const int f0 = (xg * 28 + j) * 8, n0 = (xg * 4 + j - 28) * 16;
  TLP(1);
  if (wid == 0) {
_Pragma("unroll 1")
    for (unsigned sp = 0; sp < SPIN_CAP; ++sp) { if (__hip_atomic_load(fl1, RLX_AGENT) >= 32u) break; __builtin_amdgcn_s_sleep(1); }
  }
  __builtin_amdgcn_fence(__ATOMIC_ACQUIRE, "wavefront");
  __syncthreads();
  TLP(2);
  const __amdgpu_buffer_rsrc_t rP = mkrsrc(Pf, 8u * S * D * 2u), rX1 = mkrsrc(x1o, S * D * 2u), rH2 = mkrsrc(h2o, S * D * 2u), rOut = mkrsrc(out, S * D * 2u);
  u32x4 p[2][8];
#pragma unroll
  for (int c = 0; c < 2; ++c) {
    const int cidx = ch + 32 * c, m = cidx >> 2, g = cidx & 3;
    const unsigned off = (unsigned)((Qb * 16 + m) * 64 + g * 16 + q) * 16u;
#pragma unroll
    for (int ks = 0; ks < 8; ++ks) p[c][ks] = ld16_sc1(rP, off + (unsigned)ks * (S * D * 2u));
  }
  __builtin_amdgcn_sched_barrier(0);
  unsigned e0w = 0x00003f80u, e1w = 0x3f800000u;
  asm volatile("" : "+s"(e0w), "+s"(e1w));
  const bf16x2 e0 = __builtin_bit_cast(bf16x2, e0w), e1 = __builtin_bit_cast(bf16x2, e1w);
  float x1[2][8];
  float s = 0.f;
#pragma unroll
  for (int c = 0; c < 2; ++c) {
    float acc[8] = {}, xf[8], ga[8];
#pragma unroll
    for (int ks = 0; ks < 8; ++ks)
#pragma unroll
      for (int d = 0; d < 4; ++d) {
        const unsigned pw = p[c][ks][d];
        const bf16x2 pv = __builtin_bit_cast(bf16x2, pw);
        acc[2 * d] = __builtin_amdgcn_fdot2_f32_bf16(pv, e0, acc[2 * d], false);
        acc[2 * d + 1] = __builtin_amdgcn_fdot2_f32_bf16(pv, e1, acc[2 * d + 1], false);
      }
    unpack8(xv[c], xf); unpack8(gav[c], ga);
#pragma unroll
    for (int e = 0; e < 8; ++e) { x1[c][e] = rbf(xf[e] + rbf(ga[e] * rbf(acc[e]))); s += x1[c][e]; }
  }
  TLP(3);
  s = red8(s);
  const float mw = s * (1.f / 128.f);
  float m2 = 0.f;
#pragma unroll
  for (int c = 0; c < 2; ++c)
#pragma unroll
    for (int e = 0; e < 8; ++e) { const float d = x1[c][e] - mw; m2 += d * d; }
  m2 = red8(m2);
  if (lane < 8) { sc[wid * 8 + lane] = mw; sc[32 + wid * 8 + lane] = m2; }
  bar_lds();
  float h2[2][8];
  {
    const float a0 = sc[q8], a1 = sc[8 + q8], a2 = sc[16 + q8], a3 = sc[24 + q8];
    const float mean = 0.25f * (a0 + a1 + a2 + a3);
    const float M2 = sc[32 + q8] + sc[40 + q8] + sc[48 + q8] + sc[56 + q8] +
                     128.f * ((a0 - mean) * (a0 - mean) + (a1 - mean) * (a1 - mean) + (a2 - mean) * (a2 - mean) + (a3 - mean) * (a3 - mean));
    const float mb = rbf(mean), var = rbf(M2 * (1.f / 512.f));
    const float sd = rbf(sqrtf(rbf(var + 1e-5f)));
    const float rs = 1.f / sd;
    float racc = 0.f;
#pragma unroll
    for (int c = 0; c < 2; ++c) {
      float sb[8], cb[8], w[8];
      unpack8(sbv[c], sb); unpack8(cbv[c], cb); unpack8(wv[c], w);
#pragma unroll
      for (int e = 0; e < 8; ++e) {
        const float y = rbf(fdiv(rbf(x1[c][e] - mb), sd, rs));
        h2[c][e] = rbf(rbf(y * cb[e]) + sb[e]);
        racc += h2[c][e] * w[e];
      }
    }
    racc = red8(racc);
    if (lane < 8) sc[64 + wid * 8 + lane] = racc;
  }
  bar_lds();
  if (tid < 8) {
    const float r = sc[64 + tid] + sc[72 + tid] + sc[80 + tid] + sc[88 + tid];
    const unsigned bb = cvtpk(r, 0.f) & 0xffffu;
    unsigned o = (bb & 0x8000u) ? (~bb & 0xffffu) : (bb | 0x8000u);
    if ((bb & 0x7fffu) > 0x7f80u) o = 0;
    if (bb == 0x8000u) o = 0x8000u;
    __hip_atomic_store(fl + 4096 + row, (o << 16) | (0xffffu - (unsigned)row), RLX_AGENT);
  }
  TLP(4);
#pragma unroll
  for (int c = 0; c < 2; ++c) {
    float gb[8], o[8];
    unpack8(gbv[c], gb);
#pragma unroll
    for (int e = 0; e < 8; ++e) o[e] = x1[c][e] + rbf(gb[e] * h2[c][e]);
    const unsigned off = (unsigned)(row * D + 8 * (ch + 32 * c)) * 2u;
    st16_sc1(rH2, off, pack8(h2[c]));
    st16_sc1(rX1, off, pack8(x1[c]));
    st16_sc1(rOut, off, pack8(o));
  }
  TLP(5);
  const int col = lane & 15, g = lane >> 4;
  float* red = sc + 160;
  gu32* sel = fl + 1536 + xg * 32;
  if (!is_down) {
    bf16x8 bfr[4];
    const unsigned gub = (unsigned)(size_t)lds + KT_W + 8192u * ((lane >> 1) & 1) + 16u * (128u * wid + 8u * g + ((lane >> 2) & 3)) + 8u * (lane & 1);
    tr_frags2<16>(gub, bfr[0], bfr[1]);
    tr_frags2<16>(gub + 1024u, bfr[2], bfr[3]);
    unsigned key = 0;
    if (j < NSEL) {
      unsigned kk[8];
_Pragma("unroll 1")
      for (unsigned sp = 0; sp < SPIN_CAP; ++sp) {
        const u32x4 k0 = ld16_sc1(rM, 32768u + tid * 32u), k1 = ld16_sc1(rM, 32768u + tid * 32u + 16u);
        kk[0] = k0[0]; kk[1] = k0[1]; kk[2] = k0[2]; kk[3] = k0[3]; kk[4] = k1[0]; kk[5] = k1[1]; kk[6] = k1[2]; kk[7] = k1[3];
        if (sp == 0) {
          drain(); __builtin_amdgcn_s_barrier();
          if (tid == 0) __hip_atomic_store(rdy + (row >> 3), 1u, RLX_AGENT);
        }
        const unsigned mn = umin(umin(umin(kk[0], kk[1]), umin(kk[2], kk[3])), umin(umin(kk[4], kk[5]), umin(kk[6], kk[7])));
        if (__all(mn != 0u)) break;
        asm volatile("" ::: "memory");
      }
      __builtin_amdgcn_fence(__ATOMIC_ACQUIRE, "wavefront");
      TLP(7);
      unsigned* cand = (unsigned*)(sc + 96); unsigned* top = (unsigned*)(sc + 128);
#define CE(a, b) do { const unsigned hi_ = umax(kk[a], kk[b]), lo_ = umin(kk[a], kk[b]); kk[a] = hi_; kk[b] = lo_; } while (0)
      CE(0, 1); CE(2, 3); CE(4, 5); CE(6, 7); CE(0, 2); CE(1, 3); CE(4, 6); CE(5, 7); CE(1, 2); CE(5, 6); CE(0, 4); CE(3, 7);
      CE(1, 5); CE(2, 6); CE(1, 4); CE(3, 6); CE(2, 4); CE(3, 5); CE(3, 4);
#undef CE
      unsigned mine = 0;
#pragma unroll
      for (int c = 0; c < 8; ++c) {
        const unsigned m = wave_max_u32(kk[0]);
        const bool hit = kk[0] == m;
#pragma unroll
        for (int e = 0; e < 7 - c; ++e) kk[e] = hit ? kk[e + 1] : kk[e];
        mine = lane == c ? m : mine;
      }
      if (lane < 8) cand[wid * 8 + lane] = mine;
      bar_lds();
      {
        const unsigned me = cand[lane & 31]; int rank = 0;
        const u32x4* c4 = (const u32x4*)cand;
#pragma unroll
        for (int e = 0; e < 8; ++e) { const u32x4 t = c4[e]; rank += (t[0] > me) + (t[1] > me) + (t[2] > me) + (t[3] > me); }
        if (lane < 32 && rank < 8) top[wid * 8 + rank] = me;
      }
      asm volatile("s_waitcnt lgkmcnt(0)" ::: "memory");
      TLP(8);
      key = top[wid * 8 + (col & 7)];
      const unsigned srow = (0xffffu - (key & 0xffffu)) & 2047u;
_Pragma("unroll 1")
      for (unsigned sp = 0; sp < SPIN_CAP; ++sp) {
        if (__all(__hip_atomic_load(rdy + (srow >> 3), RLX_AGENT) != 0u)) break;
      }
      __builtin_amdgcn_fence(__ATOMIC_ACQUIRE, "wavefront");
    if (tid >= 64 && tid < 72) __hip_atomic_store(sel + (tid & 7), key, RLX_AGENT);
    } else {
      drain(); __builtin_amdgcn_s_barrier();
      if (tid == 0) __hip_atomic_store(rdy + (row >> 3), 1u, RLX_AGENT);
_Pragma("unroll 1")
      for (unsigned sp = 0; sp < SPIN_CAP; ++sp) {
        key = __hip_atomic_load(sel + (col & 7), RLX_AGENT);
        if (__all(key != 0u)) break;
      }
      __builtin_amdgcn_fence(__ATOMIC_ACQUIRE, "wavefront");
      TLP(8);
    }
    const unsigned srow = (0xffffu - (key & 0xffffu)) & 2047u;
    u32x4 af[4];
#pragma unroll
    for (int st = 0; st < 4; ++st) af[st] = ld16_sc1(rH2, (srow * D + 128 * wid + 32 * st + 8 * g) * 2u);
    f32x4 acc = {0.f, 0.f, 0.f, 0.f};
#pragma unroll
    for (int st = 0; st < 4; ++st) acc = __builtin_amdgcn_mfma_f32_16x16x32_bf16(__builtin_bit_cast(bf16x8, af[st]), bfr[st], acc, 0, 0, 0);
    if (g < 2) {
#pragma unroll
      for (int r = 0; r < 4; ++r) red[(wid * 8 + 4 * g + r) * 16 + col] = acc[r];
    }
    bar_lds();
    TLP(9);
    if (wid == 0) {
      const int c = lane >> 3, f = lane & 7;
      const float gg = rbf(red[c * 16 + f] + red[(8 + c) * 16 + f] + red[(16 + c) * 16 + f] + red[(24 + c) * 16 + f]);
      const float uu = rbf(red[c * 16 + 8 + f] + red[(8 + c) * 16 + 8 + f] + red[(16 + c) * 16 + 8 + f] + red[(24 + c) * 16 + 8 + f]);
      const float a = rbf(gg / (1.f + __expf(-gg))) * uu;
      const float an = __int_as_float(__builtin_amdgcn_update_dpp(0, __float_as_int(a), 0xb1, 0xf, 0xf, false));
      unsigned w = cvtpk(a, an);
      if (w == 0xffffffffu) w = 0xffc0ffc0u;
      if (!(f & 1)) __hip_atomic_store((gu32*)(ws + 49152 + f0 * 16 + c * 16 + f * 2), w, RLX_AGENT);
    }
    TLP(10);
  } else {
    drain(); __builtin_amdgcn_s_barrier();
    if (tid == 0) __hip_atomic_store(rdy + (row >> 3), 1u, RLX_AGENT);
    {
      const int wv_ = __builtin_amdgcn_readfirstlane(wid);
#pragma unroll
      for (int i = 0; i < 7; ++i)
        __builtin_amdgcn_global_load_lds((const __attribute__((address_space(1))) void*)(wd + n0 + 8 * (lane & 1) + (size_t)(896 + (wv_ * 7 + i) * 32 + (lane >> 1)) * D),
                                         (__attribute__((address_space(3))) void*)(lds + (wv_ * 7 + i) * 1024), 16, 0, 0);
    }
    const u16 gbs = (u16)(__builtin_amdgcn_raw_buffer_load_b32(rM, (int)((2560 + n0 + (col & ~1)) * 2u), 0, 16) >> (16 * (col & 1)));
    drain();
    TLP(7);
    const unsigned trb = (unsigned)(size_t)lds + 256u * g + 32u * ((lane >> 2) & 3) + 8u * (lane & 3);
    bf16x8 bfr[14];
#pragma unroll
    for (int i = 0; i < 7; ++i) tr_frags2b(trb + KT_W + 7168u * wid + 1024u * i, trb + 7168u * wid + 1024u * i, bfr[i], bfr[7 + i]);
    unsigned key = 0;
_Pragma("unroll 1")
    for (unsigned sp = 0; sp < SPIN_CAP; ++sp) {
      key = __hip_atomic_load(sel + ((tid >> 4) & 7), RLX_AGENT);
      if (__all(key != 0u)) break;
    }
    __builtin_amdgcn_fence(__ATOMIC_ACQUIRE, "wavefront");
    TLP(8);
    const unsigned srow = (0xffffu - (key & 0xffffu)) & 2047u;
    const unsigned x1v = __builtin_amdgcn_raw_buffer_load_b32(rX1, (int)((srow * D + n0 + (col & ~1)) * 2u), 0, 16) >> (16 * (col & 1));
    const unsigned h2v = __builtin_amdgcn_raw_buffer_load_b32(rH2, (int)((srow * D + n0 + (col & ~1)) * 2u), 0, 16) >> (16 * (col & 1));
    const float sg = rbf(1.f / (1.f + __expf(-key2r(key))));
    u32x4 af[14];
_Pragma("unroll 1")
    for (unsigned sp = 0; sp < SPIN_CAP; ++sp) {
      unsigned mx = 0;
#pragma unroll
      for (int st = 0; st < 14; ++st) af[st] = ld16_sc1(rM, 49152u + ((7 * wid + (st < 7 ? st : 21 + st)) * 4 + g) * 128u + (col & 7) * 16u);
#pragma unroll
      for (int st = 0; st < 14; ++st) mx = umax(umax(mx, umax(af[st][0], af[st][1])), umax(af[st][2], af[st][3]));
      if (__all(mx != 0xffffffffu)) break;
      asm volatile("" ::: "memory");
    }
    TLP(11);
    f32x4 acc = {0.f, 0.f, 0.f, 0.f};
#pragma unroll
    for (int st = 0; st < 14; ++st) acc = __builtin_amdgcn_mfma_f32_16x16x32_bf16(__builtin_bit_cast(bf16x8, af[st]), bfr[st], acc, 0, 0, 0);
    if (g < 2) {
#pragma unroll
      for (int r = 0; r < 4; ++r) red[(wid * 8 + 4 * g + r) * 16 + col] = acc[r];
    }
    bar_lds();
    TLP(12);
    if (tid < 128) {
      const int c = tid >> 4;
      const float y = red[c * 16 + col] + red[(8 + c) * 16 + col] + red[(16 + c) * 16 + col] + red[(24 + c) * 16 + col];
      const float ys = rbf(rbf(y) * sg);
      const float ob = rbf(bf2f((u16)h2v) + ys);
      __builtin_amdgcn_raw_buffer_store_b16(f2bf(bf2f((u16)x1v) + rbf(bf2f(gbs) * ob)), rOut, (int)((srow * D + n0 + col) * 2u), 0, 16);
    }
  }
  if (b == 255 && tid == 0) __hip_atomic_store(fl, tag, RLX_AGENT);
  TL_END(4);
  asm volatile("L_CODE_END:" ::: "memory");
}

extern "C" void kernel_launch(void* const* d_in, const int* in_sizes, int n_in, void* d_out, int out_size, void* d_ws, size_t ws_size, hipStream_t stream) {
  const u16* x = (const u16*)d_in[0]; const u16* kc = (const u16*)d_in[1]; const u16* vc = (const u16*)d_in[2];
  const u16* wq = (const u16*)d_in[3]; const u16* wo = (const u16*)d_in[4];
  const u16* wr = (const u16*)d_in[6]; const u16* wg = (const u16*)d_in[7]; const u16* wu = (const u16*)d_in[8]; const u16* wd = (const u16*)d_in[9];
  const u16* c = (const u16*)d_in[10]; const u16* wmod = (const u16*)d_in[11];
  u16* out = (u16*)d_out;
  char* ws = (char*)d_ws;
  (void)hipFuncSetAttribute((const void*)k_main, hipFuncAttributeMaxDynamicSharedMemorySize, KT_LDS);
  k_main<<<256, 256, KT_LDS, stream>>>(ws, wq, kc, vc, wo, wg, wu, wd, x, wr, c, wmod, out TL_PASS);
}
```
